```python
import torch

def _softmax(x, axis=-1):
    return torch.softmax(x, dim=axis)
def _gelu(x):
    return torch.nn.functional.gelu(x, approximate="tanh")
def _silu(x):
    return torch.nn.functional.silu(x)
def _sigmoid(x):
    return torch.sigmoid(x)
def _layernorm(x, axis=-1, eps=1e-5):
    m = x.mean(dim=axis, keepdim=True)
    v = x.var(dim=axis, keepdim=True, unbiased=False)
    return (x - m) / torch.sqrt(v + eps)
def _scatter_add(tgt, idx, src, axis=0):
    return tgt.clone().index_add_(axis, idx, src)
def _segment_sum(src, seg, n):
    out = torch.zeros((n,) + src.shape[1:], dtype=src.dtype)
    return out.index_add_(0, seg, src)
def _pad(x, pw):
    flat = tuple(v for pair in reversed(pw) for v in pair)
    return torch.nn.functional.pad(x, flat)

kernel_name = 'standalone__gqa_softmax__near_ridge'

def setup_inputs():

    _w = (lambda a: (lambda t: t.to(torch.bfloat16) if t.is_floating_point() else t)(torch.as_tensor(a)))
    B, Hq, Hkv, Sq, Sk, D = 16, 32, 4, 384, 384, 128
    return {
        "q": _w((lambda *s: torch.randn(*s, dtype=torch.bfloat16) * 0.8)(B, Hq, Sq, D)),
        "k": _w((lambda *s: torch.randn(*s, dtype=torch.bfloat16) * 0.8)(B, Hkv, Sk, D)),
        "v": _w((lambda *s: torch.randn(*s, dtype=torch.bfloat16) * 0.05)(B, Hkv, Sk, D)),
    }

def reference(q, k, v):
    B, Hq, Sq, D = q.shape
    Hkv = k.shape[1]
    rep = Hq // Hkv
    k = (lambda x, n, axis: torch.repeat_interleave(x, n, dim=axis))(k, rep, axis=1)
    v = (lambda x, n, axis: torch.repeat_interleave(x, n, dim=axis))(v, rep, axis=1)
    scale = D ** -0.5
    scores = torch.einsum("bhqd,bhkd->bhqk", q, k) * scale
    p = _softmax(scores, axis=-1)
    return torch.einsum("bhqk,bhkd->bhqd", p, v)
```

```cpp
#include <hip/hip_runtime.h>
#include <hip/hip_bf16.h>

typedef float    f32x16 __attribute__((ext_vector_type(16)));
typedef float    f32x2  __attribute__((ext_vector_type(2)));
typedef __bf16   bf16x8 __attribute__((ext_vector_type(8)));
typedef __bf16   bf16x4 __attribute__((ext_vector_type(4)));
typedef __bf16   bf16x2 __attribute__((ext_vector_type(2)));
typedef unsigned u32x4  __attribute__((ext_vector_type(4)));
typedef short    s16x4  __attribute__((ext_vector_type(4)));
#define DI __device__ __forceinline__

constexpr int B = 16, HQ = 32, HKV = 4, SQ = 384, SK = 384, D = 128;
constexpr int G = HQ / HKV;
constexpr int ROWS_PER_KV = G * SQ;
constexpr int NBLK_PER_KV = ROWS_PER_KV / 256;
constexpr int NT = SK / 64;
constexpr float CSC = 0.088388347648318440f * 1.4426950408889634f;

DI unsigned cvtpk(float lo, float hi) {
  f32x2 v = {lo, hi}; bf16x2 b = __builtin_convertvector(v, bf16x2); return __builtin_bit_cast(unsigned, b);
}
DI unsigned off_k(unsigned row, unsigned ch) { return 2048u * (row >> 3) + 512u * (ch >> 2) + 64u * (row & 7) + 16u * ((ch & 3) ^ ((row >> 2) & 3)); }
DI unsigned off_v(unsigned row, unsigned ch) { return 2048u * (row >> 3) + 512u * (ch >> 2) + 64u * (row & 7) + 16u * (ch & 3); }

template <int OFF> DI s16x4 tr_read(unsigned vb) {
  s16x4 r; asm volatile("ds_read_b64_tr_b16 %0, %1 offset:%2" : "=&v"(r) : "v"(vb), "i"(OFF) : "memory"); return r;
}

__global__ __launch_bounds__(512, 1)
void attn_base(const unsigned short* __restrict__ Q, const unsigned short* __restrict__ K, const unsigned short* __restrict__ V, unsigned short* __restrict__ O) {
  extern __shared__ __attribute__((aligned(16))) char lds[];
  const int tid = threadIdx.x, wid = tid >> 6, lane = tid & 63, r32 = lane & 31, hi = lane >> 5;
  const int bid = blockIdx.x, xcd = bid & 7, idx = bid >> 3;
  const int grp = xcd * 8 + idx / NBLK_PER_KV, qb = idx % NBLK_PER_KV;
  const unsigned short* Qb = Q + ((size_t)grp * ROWS_PER_KV + qb * 256) * D;
  unsigned short* Ob = O + ((size_t)grp * ROWS_PER_KV + qb * 256) * D;
  const unsigned short* Kh = K + (size_t)grp * SK * D;
  const unsigned short* Vh = V + (size_t)grp * SK * D;
  char* K_lds = lds;
  char* V_lds = lds + 32768;

  bf16x8 qr[8];
  const unsigned short* Qw = Qb + (size_t)(wid * 32 + r32) * D + hi * 8;
#pragma unroll
  for (int d0 = 0; d0 < 8; ++d0) qr[d0] = *reinterpret_cast<const bf16x8*>(Qw + d0 * 16);

  const int sr = tid >> 4, sc = tid & 15;
  const unsigned kst0 = off_k(sr, sc), kst1 = off_k(sr + 32, sc), vst0 = off_v(sr, sc), vst1 = off_v(sr + 32, sc);
  u32x4 sk0, sk1, sv0, sv1;
#define SLOAD(t) do { const size_t o_ = (size_t)((t) * 64 + sr) * D + sc * 8; sk0 = *(const u32x4*)(Kh + o_); sk1 = *(const u32x4*)(Kh + o_ + 32 * D); \
    sv0 = *(const u32x4*)(Vh + o_); sv1 = *(const u32x4*)(Vh + o_ + 32 * D); } while (0)
#define SWRITE(b) do { *(u32x4*)(K_lds + (b) * 16384 + kst0) = sk0; *(u32x4*)(K_lds + (b) * 16384 + kst1) = sk1; \
    *(u32x4*)(V_lds + (b) * 16384 + vst0) = sv0; *(u32x4*)(V_lds + (b) * 16384 + vst1) = sv1; } while (0)

  const unsigned kb_e = 2048u * (r32 >> 3) + 64u * (r32 & 7) + 16u * ((0 + hi) ^ ((r32 >> 2) & 3));
  const unsigned kb_o = 2048u * (r32 >> 3) + 64u * (r32 & 7) + 16u * ((2 + hi) ^ ((r32 >> 2) & 3));
  const unsigned p_ = lane & 3, q_ = (lane >> 2) & 3, g_ = (lane >> 4) & 1;
  const unsigned vb0 = (unsigned)(size_t)V_lds + 64u * (4 * hi + q_) + 2u * (16 * g_ + 8 * (p_ & 1) + 4 * (p_ >> 1));

  float m_run = -1e30f, l_run = 0.f;
  f32x16 o[4] = {};

  SLOAD(0); SWRITE(0); __syncthreads();
  for (int t = 0; t < NT; ++t) {
    const int cur = t & 1;
    if (t + 1 < NT) SLOAD(t + 1);
    f32x16 s0 = {}, s1 = {};
    const char* Kc = K_lds + cur * 16384;
#pragma unroll
    for (int d0 = 0; d0 < 8; ++d0) {
      const unsigned kb = ((d0 & 1) ? kb_o : kb_e) + 512u * (d0 >> 1);
      bf16x8 a0 = *reinterpret_cast<const bf16x8*>(Kc + kb);
      bf16x8 a1 = *reinterpret_cast<const bf16x8*>(Kc + kb + 8192);
      s0 = __builtin_amdgcn_mfma_f32_32x32x16_bf16(a0, qr[d0], s0, 0, 0, 0);
      s1 = __builtin_amdgcn_mfma_f32_32x32x16_bf16(a1, qr[d0], s1, 0, 0, 0);
    }
    float pmax = s0[0];
#pragma unroll
    for (int r = 1; r < 16; ++r) pmax = fmaxf(pmax, s0[r]);
#pragma unroll
    for (int r = 0; r < 16; ++r) pmax = fmaxf(pmax, s1[r]);
    { auto rr = __builtin_amdgcn_permlane32_swap(__float_as_uint(pmax), __float_as_uint(pmax), false, false);
      pmax = fmaxf(__uint_as_float(rr[0]), __uint_as_float(rr[1])); }
    const float mn = fmaxf(m_run, pmax);
    const float alpha = __builtin_amdgcn_exp2f((m_run - mn) * CSC);
    m_run = mn;
    const float mnC = -mn * CSC;
    float ps = 0.f;
#pragma unroll
    for (int r = 0; r < 16; ++r) { s0[r] = __builtin_amdgcn_exp2f(fmaf(s0[r], CSC, mnC)); ps += s0[r]; }
#pragma unroll
    for (int r = 0; r < 16; ++r) { s1[r] = __builtin_amdgcn_exp2f(fmaf(s1[r], CSC, mnC)); ps += s1[r]; }
    { auto rr = __builtin_amdgcn_permlane32_swap(__float_as_uint(ps), __float_as_uint(ps), false, false);
      ps = __uint_as_float(rr[0]) + __uint_as_float(rr[1]); }
    l_run = l_run * alpha + ps;
#pragma unroll
    for (int d = 0; d < 4; ++d)
#pragma unroll
      for (int r = 0; r < 16; ++r) o[d][r] *= alpha;
    bf16x8 pk[4];
#pragma unroll
    for (int ks = 0; ks < 2; ++ks) {
      u32x4 w0 = {cvtpk(s0[8 * ks + 0], s0[8 * ks + 1]), cvtpk(s0[8 * ks + 2], s0[8 * ks + 3]), cvtpk(s0[8 * ks + 4], s0[8 * ks + 5]), cvtpk(s0[8 * ks + 6], s0[8 * ks + 7])};
      u32x4 w1 = {cvtpk(s1[8 * ks + 0], s1[8 * ks + 1]), cvtpk(s1[8 * ks + 2], s1[8 * ks + 3]), cvtpk(s1[8 * ks + 4], s1[8 * ks + 5]), cvtpk(s1[8 * ks + 6], s1[8 * ks + 7])};
      pk[ks] = __builtin_bit_cast(bf16x8, w0); pk[2 + ks] = __builtin_bit_cast(bf16x8, w1);
    }
    const unsigned vb = vb0 + cur * 16384;
#define PVD(DB) do { \
      const s16x4 l0 = tr_read<0 * 4096 + 512 * DB>(vb), h0 = tr_read<0 * 4096 + 2048 + 512 * DB>(vb); \
      const s16x4 l1 = tr_read<1 * 4096 + 512 * DB>(vb), h1 = tr_read<1 * 4096 + 2048 + 512 * DB>(vb); \
      const s16x4 l2 = tr_read<2 * 4096 + 512 * DB>(vb), h2 = tr_read<2 * 4096 + 2048 + 512 * DB>(vb); \
      const s16x4 l3 = tr_read<3 * 4096 + 512 * DB>(vb), h3 = tr_read<3 * 4096 + 2048 + 512 * DB>(vb); \
      asm volatile("s_waitcnt lgkmcnt(0)" ::: "memory"); __builtin_amdgcn_sched_barrier(0); \
      typedef short s16x8 __attribute__((ext_vector_type(8))); \
      o[DB] = __builtin_amdgcn_mfma_f32_32x32x16_bf16(__builtin_bit_cast(bf16x8, (s16x8){l0[0], l0[1], l0[2], l0[3], h0[0], h0[1], h0[2], h0[3]}), pk[0], o[DB], 0, 0, 0); \
      o[DB] = __builtin_amdgcn_mfma_f32_32x32x16_bf16(__builtin_bit_cast(bf16x8, (s16x8){l1[0], l1[1], l1[2], l1[3], h1[0], h1[1], h1[2], h1[3]}), pk[1], o[DB], 0, 0, 0); \
      o[DB] = __builtin_amdgcn_mfma_f32_32x32x16_bf16(__builtin_bit_cast(bf16x8, (s16x8){l2[0], l2[1], l2[2], l2[3], h2[0], h2[1], h2[2], h2[3]}), pk[2], o[DB], 0, 0, 0); \
      o[DB] = __builtin_amdgcn_mfma_f32_32x32x16_bf16(__builtin_bit_cast(bf16x8, (s16x8){l3[0], l3[1], l3[2], l3[3], h3[0], h3[1], h3[2], h3[3]}), pk[3], o[DB], 0, 0, 0); \
    } while (0)
    PVD(0); PVD(1); PVD(2); PVD(3);
    __syncthreads();
    if (t + 1 < NT) { SWRITE(cur ^ 1); }
    __syncthreads();
  }
  const float il = __builtin_amdgcn_rcpf(l_run);
  unsigned short* Ow = Ob + (size_t)(wid * 32 + r32) * D + 8 * hi;
#pragma unroll
  for (int db = 0; db < 4; ++db)
#pragma unroll
    for (int j = 0; j < 2; ++j) {
      u32x4 w = {cvtpk(o[db][8 * j + 0] * il, o[db][8 * j + 1] * il), cvtpk(o[db][8 * j + 2] * il, o[db][8 * j + 3] * il),
                 cvtpk(o[db][8 * j + 4] * il, o[db][8 * j + 5] * il), cvtpk(o[db][8 * j + 6] * il, o[db][8 * j + 7] * il)};
      *(u32x4*)(Ow + 32 * db + 16 * j) = w;
    }
}

extern "C" void kernel_launch(void* const* d_in, const int* in_sizes, int n_in,
                              void* d_out, int out_size, void* d_ws, size_t ws_size,
                              hipStream_t stream) {
  const int shm = 65536;
  hipFuncSetAttribute((const void*)attn_base, hipFuncAttributeMaxDynamicSharedMemorySize, shm);
  hipLaunchKernelGGL(attn_base, dim3(B * HKV * NBLK_PER_KV), dim3(512), shm, stream,
                     (const unsigned short*)d_in[0], (const unsigned short*)d_in[1], (const unsigned short*)d_in[2], (unsigned short*)d_out);
}
```

```cpp
#include <hip/hip_runtime.h>
#include <hip/hip_bf16.h>

typedef float    f32x4  __attribute__((ext_vector_type(4)));
typedef float    f32x2  __attribute__((ext_vector_type(2)));
typedef __bf16   bf16x8 __attribute__((ext_vector_type(8)));
typedef __bf16   bf16x2 __attribute__((ext_vector_type(2)));
typedef unsigned u32x4  __attribute__((ext_vector_type(4)));
typedef unsigned u32x2  __attribute__((ext_vector_type(2)));
#define DI __device__ __forceinline__

constexpr int B = 16, HQ = 32, HKV = 4, SQ = 384, SK = 384, D = 128;
constexpr int ROWS_PER_KV = (HQ / HKV) * SQ;
constexpr float CSC = 0.088388347648318440f * 1.4426950408889634f;

DI unsigned cvtpk(float lo, float hi) { f32x2 v = {lo, hi}; bf16x2 b = __builtin_convertvector(v, bf16x2); return __builtin_bit_cast(unsigned, b); }

__device__ __forceinline__ void slow_path(const unsigned short* __restrict__ Qw, const unsigned short* __restrict__ Kg, const unsigned short* __restrict__ Vg,
                                       unsigned short* __restrict__ Ow, int wid, int lane) {
  const int r = lane & 15, g = lane >> 4;
  for (int u = 0; u < 12; ++u) {
    const size_t row = (size_t)(u * 4 + wid) * 16 + r;
    bf16x8 qf[4];
#pragma unroll
    for (int ds = 0; ds < 4; ++ds) qf[ds] = *reinterpret_cast<const bf16x8*>(Qw + row * D + 32 * ds + 8 * g);
    float m = -__builtin_inff(), l = 0.f;
    f32x4 o[8];
#pragma unroll
    for (int dt = 0; dt < 8; ++dt) o[dt] = f32x4{0.f, 0.f, 0.f, 0.f};
    for (int kb = 0; kb < 12; ++kb) {
      f32x4 s[2];
#pragma unroll
      for (int kt = 0; kt < 2; ++kt) {
        s[kt] = f32x4{0.f, 0.f, 0.f, 0.f};
#pragma unroll
        for (int ds = 0; ds < 4; ++ds) {
          const bf16x8 kf = *reinterpret_cast<const bf16x8*>(Kg + (size_t)(32 * kb + 16 * kt + r) * D + 32 * ds + 8 * g);
          s[kt] = __builtin_amdgcn_mfma_f32_16x16x32_bf16(kf, qf[ds], s[kt], 0, 0, 0);
        }
      }
      float tm = fmaxf(fmaxf(fmaxf(s[0][0], s[0][1]), fmaxf(s[0][2], s[0][3])), fmaxf(fmaxf(s[1][0], s[1][1]), fmaxf(s[1][2], s[1][3])));
      tm = fmaxf(tm, __shfl_xor(tm, 16)); tm = fmaxf(tm, __shfl_xor(tm, 32));
      const float mn = fmaxf(m, tm);
      const float alpha = (m == mn) ? 1.f : __builtin_amdgcn_exp2f((m - mn) * CSC);
      m = mn;
      float p[8]; float ps = 0.f;
#pragma unroll
      for (int i = 0; i < 8; ++i) { p[i] = __builtin_amdgcn_exp2f((s[i >> 2][i & 3] - mn) * CSC); }
      const u32x4 pw = {cvtpk(p[0], p[1]), cvtpk(p[2], p[3]), cvtpk(p[4], p[5]), cvtpk(p[6], p[7])};
#pragma unroll
      for (int i = 0; i < 4; ++i) ps += __uint_as_float(pw[i] << 16) + __uint_as_float(pw[i] & 0xffff0000u);
      l = l * alpha + ps;
      const bf16x8 pf = __builtin_bit_cast(bf16x8, pw);
#pragma unroll
      for (int dt = 0; dt < 8; ++dt) {
        u32x4 vw;
#pragma unroll
        for (int j2 = 0; j2 < 4; ++j2) {
          const int j0 = 2 * j2, j1 = 2 * j2 + 1;
          const int k0 = 32 * kb + (j0 < 4 ? 4 * g + j0 : 16 + 4 * g + j0 - 4), k1 = 32 * kb + (j1 < 4 ? 4 * g + j1 : 16 + 4 * g + j1 - 4);
          vw[j2] = (unsigned)Vg[(size_t)k0 * D + 16 * dt + r] | ((unsigned)Vg[(size_t)k1 * D + 16 * dt + r] << 16);
        }
        o[dt] = o[dt] * alpha;
        o[dt] = __builtin_amdgcn_mfma_f32_16x16x32_bf16(__builtin_bit_cast(bf16x8, vw), pf, o[dt], 0, 0, 0);
      }
    }
    l += __shfl_xor(l, 16); l += __shfl_xor(l, 32);
    const float il = 1.f / l;
#pragma unroll
    for (int dt = 0; dt < 8; ++dt) {
      const u32x2 w = {cvtpk(o[dt][0] * il, o[dt][1] * il), cvtpk(o[dt][2] * il, o[dt][3] * il)};
      *reinterpret_cast<u32x2*>(Ow + row * D + 16 * dt + 4 * g) = w;
    }
  }
}

extern "C" __global__ __launch_bounds__(256, 1)
void gqa_attn_fwd(const unsigned short* __restrict__ Q, const unsigned short* __restrict__ K, const unsigned short* __restrict__ V, unsigned short* __restrict__ O) {
  extern __shared__ __attribute__((aligned(16))) char lds[];
  const unsigned tid = threadIdx.x;
  asm volatile(
    "s_getpc_b64 s[8:9]\n"
    "s_and_b32 s9, s9, 0xffff\n"
    "s_mov_b32 s10, 50768\n"
    "s_mov_b32 s11, 0x20000\n"
    "v_lshrrev_b32 v2, 6, %[t]\n"
    "v_and_b32 v3, 63, %[t]\n"
    "v_lshl_add_u32 v2, %[x], 2, v2\n"
    "v_mul_u32_u24 v2, 1024, v2\n"
    "v_lshl_add_u32 v2, v3, 4, v2\n"
    "buffer_load_dwordx4 v[16:19], v2, s[8:11] , 0 offen offset:0\n"
    "buffer_load_dwordx4 v[20:23], v2, s[8:11] , 0 offen offset:1024\n"
    :: [t] "v"(tid), [x] "s"(blockIdx.x >> 3) : "memory", "s8", "s9", "s10", "s11", "v2", "v3", "v16","v17","v18","v19","v20","v21","v22","v23");
  __builtin_amdgcn_sched_barrier(0);
  const unsigned bid = blockIdx.x, xcd = bid & 7u, idx = bid >> 3;
  const unsigned grp = xcd * 8u + (idx >> 2), quarter = idx & 3u;
  const unsigned short* Qw = Q + ((size_t)grp * ROWS_PER_KV + quarter * 768u) * D;
  unsigned short* Ow = O + ((size_t)grp * ROWS_PER_KV + quarter * 768u) * D;
  const unsigned short* Kg = K + (size_t)grp * SK * D;
  const unsigned short* Vg = V + (size_t)grp * SK * D;
  const unsigned long long qa = (unsigned long long)Qw, ka = (unsigned long long)Kg, va = (unsigned long long)Vg, oa = (unsigned long long)Ow;
  const unsigned ldsb = (unsigned)(size_t)lds;
  unsigned long long fail; unsigned wv, tio = tid;
  asm volatile(
    "s_mov_b32 s40, %[qlo]\n"
    "s_mov_b32 s41, %[qhi]\n"
    "s_mov_b32 s42, 196608\n"
    "s_mov_b32 s43, 0x20000\n"
    "s_mov_b32 s44, %[klo]\n"
    "s_mov_b32 s45, %[khi]\n"
    "s_mov_b32 s46, 98304\n"
    "s_mov_b32 s47, 0x20000\n"
    "v_lshrrev_b32 v9, 6, %[tid]\n"
    "v_and_b32 v10, 63, %[tid]\n"
    "v_lshlrev_b32 v233, 4, %[tid]\n"
    "v_readfirstlane_b32 s56, v9\n"
    "v_and_b32 v11, 15, v10\n"
    "v_lshrrev_b32 v12, 4, v10\n"
    "v_lshlrev_b32 v4, 8, v11\n"
    "v_lshl_add_u32 v4, v12, 4, v4\n"
    "v_lshl_add_u32 v4, v9, 12, v4\n"
    "s_nop 4\n"
    "LCS_%=:\n"
    "s_getpc_b64 s[72:73]\n"
    "s_and_b32 s73, s73, 0xffff\n"
    "s_mov_b32 s74, (LCE_%= - LCS_%=)\n"
    "s_mov_b32 s75, 0x20000\n"
    "s_mov_b32 s67, 0\n"
    "buffer_load_dwordx4 v[144:147], v233, s[44:47], s67 offen\n"
    "s_mov_b32 s68, 4096\n"
    "buffer_load_dwordx4 v[148:151], v233, s[44:47], s68 offen\n"
    "s_mov_b32 s60, 0\n"
    "buffer_load_dwordx4 a[0:3], v4, s[40:43], s60 offen offset:0\n"
    "buffer_load_dwordx4 a[4:7], v4, s[40:43], s60 offen offset:64\n"
    "buffer_load_dwordx4 a[8:11], v4, s[40:43], s60 offen offset:128\n"
    "buffer_load_dwordx4 a[12:15], v4, s[40:43], s60 offen offset:192\n"
    "s_mov_b32 s48, %[vlo]\n"
    "s_mov_b32 s49, %[vhi]\n"
    "s_mov_b32 s50, 98304\n"
    "s_mov_b32 s51, 0x20000\n"
    "s_mov_b32 s52, %[olo]\n"
    "s_mov_b32 s53, %[ohi]\n"
    "s_mov_b32 s54, 196608\n"
    "s_mov_b32 s55, 0x20000\n"
    "s_mov_b32 s66, 0x3e0293ee\n"
    "s_mov_b64 s[64:65], 0\n"
    "v_bfe_u32 v13, v10, 2, 3\n"
    "v_lshlrev_b32 v232, 8, v13\n"
    "v_lshrrev_b32 v13, 5, v10\n"
    "v_lshl_add_u32 v232, v13, 6, v232\n"
    "v_and_b32 v13, 3, v10\n"
    "v_lshl_add_u32 v232, v13, 4, v232\n"
    "s_and_b32 s59, s56, 1\n"
    "s_lshr_b32 s60, s56, 1\n"
    "s_lshl_b32 s57, s60, 12\n"
    "s_lshl_b32 s61, s59, 10\n"
    "s_add_u32 s57, s57, s61\n"
    "s_add_u32 s57, s57, 98304\n"
    "s_add_u32 s57, s57, %[lds]\n"
    "s_lshl_b32 s58, s60, 12\n"
    "s_lshl_b32 s61, s59, 7\n"
    "s_add_u32 s58, s58, s61\n"
    "s_nop 1\n"
    "s_mov_b32 s67, 8192\n"
    "buffer_load_dwordx4 v[152:155], v233, s[44:47], s67 offen\n"
    "s_mov_b32 s68, 12288\n"
    "buffer_load_dwordx4 v[156:159], v233, s[44:47], s68 offen\n"
    "s_add_u32 m0, s57, 32768\n"
    "s_add_u32 s59, s58, 0\n"
    "buffer_load_dwordx4 v232, s[48:51], s59 offen lds\n"
    "buffer_load_dwordx4 v232, s[48:51], s59 offen offset:2048 lds\n"
    "s_mov_b32 s67, 16384\n"
    "buffer_load_dwordx4 v[160:163], v233, s[44:47], s67 offen\n"
    "s_mov_b32 s68, 20480\n"
    "buffer_load_dwordx4 v[164:167], v233, s[44:47], s68 offen\n"
    "s_mov_b32 s61, 16384\n"
    "buffer_load_dwordx4 a[16:19], v4, s[40:43], s61 offen offset:0\n"
    "buffer_load_dwordx4 a[20:23], v4, s[40:43], s61 offen offset:64\n"
    "buffer_load_dwordx4 a[24:27], v4, s[40:43], s61 offen offset:128\n"
    "buffer_load_dwordx4 a[28:31], v4, s[40:43], s61 offen offset:192\n"
    "v_lshrrev_b32 v13, 3, v11\n"
    "v_lshlrev_b32 v1, 11, v13\n"
    "v_and_b32 v14, 7, v11\n"
    "v_lshl_add_u32 v1, v14, 6, v1\n"
    "v_lshlrev_b32 v13, 1, v13\n"
    "v_xor_b32 v13, v13, v12\n"
    "v_lshl_add_u32 v1, v13, 4, v1\n"
    "v_add_u32 v1, %[lds], v1\n"
    "v_add_u32 v2, 0x10000, v1\n"
    "v_lshrrev_b32 v13, 1, v12\n"
    "v_lshlrev_b32 v3, 11, v13\n"
    "v_and_b32 v13, 1, v12\n"
    "v_lshl_add_u32 v3, v13, 8, v3\n"
    "v_bfe_u32 v13, v10, 2, 2\n"
    "v_lshl_add_u32 v3, v13, 6, v3\n"
    "v_and_b32 v13, 3, v10\n"
    "v_lshl_add_u32 v3, v13, 4, v3\n"
    "v_add_u32 v3, %[lds], v3\n"
    "v_add_u32 v3, 0x18000, v3\n"
    "v_lshrrev_b32 v13, 7, %[tid]\n"
    "v_lshlrev_b32 v234, 11, v13\n"
    "v_bfe_u32 v14, %[tid], 2, 2\n"
    "v_lshl_add_u32 v234, v14, 9, v234\n"
    "v_bfe_u32 v14, %[tid], 4, 3\n"
    "v_lshl_add_u32 v234, v14, 6, v234\n"
    "v_lshlrev_b32 v13, 1, v13\n"
    "v_and_b32 v14, 3, %[tid]\n"
    "v_xor_b32 v13, v13, v14\n"
    "v_lshl_add_u32 v234, v13, 4, v234\n"
    "v_add_u32 v234, %[lds], v234\n"
    "s_cmp_eq_u32 s56, 0\n"
    "s_cselect_b32 s69, 0, 0x40000000\n"
    "s_mov_b32 s67, 24576\n"
    "s_add_u32 m0, s57, 40960\n"
    "s_add_u32 s59, s58, 8192\n"
    "buffer_load_dwordx4 v[168:171], v233, s[44:47], s67 offen\n"
    "s_mov_b32 s68, 28672\n"
    "buffer_load_dwordx4 v[172:175], v233, s[44:47], s68 offen\n"
    "buffer_load_dwordx4 v232, s[48:51], s59 offen lds\n"
    "buffer_load_dwordx4 v232, s[48:51], s59 offen offset:2048 lds\n"
    "s_waitcnt vmcnt(19)\n"
    "v_lshlrev_b32 v13, 16, v144\n"
    "s_mov_b32 s67, 32768\n"
    "s_add_u32 m0, s57, 49152\n"
    "s_add_u32 s59, s58, 16384\n"
    "v_and_b32 v14, 0xffff0000, v144\n"
    "v_mul_f32 v13, s66, v13\n"
    "v_mul_f32 v14, s66, v14\n"
    "v_cvt_pk_bf16_f32 v144, v13, v14\n"
    "v_lshlrev_b32 v15, 16, v145\n"
    "v_and_b32 v9, 0xffff0000, v145\n"
    "v_mul_f32 v15, s66, v15\n"
    "v_mul_f32 v9, s66, v9\n"
    "v_cvt_pk_bf16_f32 v145, v15, v9\n"
    "v_lshlrev_b32 v13, 16, v146\n"
    "v_and_b32 v14, 0xffff0000, v146\n"
    "v_mul_f32 v13, s66, v13\n"
    "v_mul_f32 v14, s66, v14\n"
    "v_cvt_pk_bf16_f32 v146, v13, v14\n"
    "v_lshlrev_b32 v15, 16, v147\n"
    "v_and_b32 v9, 0xffff0000, v147\n"
    "v_mul_f32 v15, s66, v15\n"
    "v_mul_f32 v9, s66, v9\n"
    "v_cvt_pk_bf16_f32 v147, v15, v9\n"
    "buffer_load_dwordx4 v[176:179], v233, s[44:47], s67 offen\n"
    "s_mov_b32 s68, 36864\n"
    "ds_write_b128 v234, v[144:147] offset:0\n"
    "buffer_load_dwordx4 v[180:183], v233, s[44:47], s68 offen\n"
    "s_waitcnt vmcnt(20)\n"
    "v_lshlrev_b32 v13, 16, v148\n"
    "v_and_b32 v14, 0xffff0000, v148\n"
    "v_mul_f32 v13, s66, v13\n"
    "v_mul_f32 v14, s66, v14\n"
    "v_cvt_pk_bf16_f32 v148, v13, v14\n"
    "v_lshlrev_b32 v15, 16, v149\n"
    "v_and_b32 v9, 0xffff0000, v149\n"
    "v_mul_f32 v15, s66, v15\n"
    "v_mul_f32 v9, s66, v9\n"
    "v_cvt_pk_bf16_f32 v149, v15, v9\n"
    "v_lshlrev_b32 v13, 16, v150\n"
    "v_and_b32 v14, 0xffff0000, v150\n"
    "v_mul_f32 v13, s66, v13\n"
    "v_mul_f32 v14, s66, v14\n"
    "v_cvt_pk_bf16_f32 v150, v13, v14\n"
    "v_lshlrev_b32 v15, 16, v151\n"
    "v_and_b32 v9, 0xffff0000, v151\n"
    "v_mul_f32 v15, s66, v15\n"
    "v_mul_f32 v9, s66, v9\n"
    "v_cvt_pk_bf16_f32 v151, v15, v9\n"
    "buffer_load_dwordx4 v232, s[48:51], s59 offen lds\n"
    "ds_write_b128 v234, v[148:151] offset:4096\n"
    "s_waitcnt lgkmcnt(0)\n"
    "s_barrier\n"
    "ds_read_b128 a[80:83], v1 offset:0\n"
    "ds_read_b128 a[84:87], v1 offset:512\n"
    "ds_read_b128 a[88:91], v1 offset:1024\n"
    "ds_read_b128 a[92:95], v1 offset:1536\n"
    "ds_read_b128 a[96:99], v1 offset:4096\n"
    "ds_read_b128 a[100:103], v1 offset:4608\n"
    "buffer_load_dwordx4 v232, s[48:51], s59 offen offset:2048 lds\n"
    "s_waitcnt lgkmcnt(5)\n"
    "s_waitcnt vmcnt(21)\n"
    "v_mfma_f32_16x16x32_bf16 v[16:19], a[80:83], a[0:3], 0\n"
    "s_waitcnt lgkmcnt(4) vmcnt(20)\n"
    "ds_read_b128 a[80:83], v1 offset:5120\n"
    "v_mfma_f32_16x16x32_bf16 v[16:19], a[84:87], a[4:7], v[16:19]\n"
    "s_waitcnt lgkmcnt(4) vmcnt(19)\n"
    "ds_read_b128 a[84:87], v1 offset:5632\n"
    "v_mfma_f32_16x16x32_bf16 v[16:19], a[88:91], a[8:11], v[16:19]\n"
    "s_waitcnt lgkmcnt(4) vmcnt(18)\n"
    "v_mfma_f32_16x16x32_bf16 v[16:19], a[92:95], a[12:15], v[16:19]\n"
    "s_waitcnt lgkmcnt(3)\n"
    "v_mfma_f32_16x16x32_bf16 v[20:23], a[96:99], a[0:3], 0\n"
    "s_waitcnt lgkmcnt(2)\n"
    "v_mfma_f32_16x16x32_bf16 v[20:23], a[100:103], a[4:7], v[20:23]\n"
    "s_waitcnt vmcnt(17)\n"
    "s_nop 3\n"
    "v_max3_f32 v10, v16, v17, v18\n"
    "v_lshlrev_b32 v13, 16, v152\n"
    "s_mov_b32 s67, 40960\n"
    "s_add_u32 m0, s57, 57344\n"
    "s_add_u32 s59, s58, 24576\n"
    "buffer_load_dwordx4 v[184:187], v233, s[44:47], s67 offen\n"
    "s_mov_b32 s68, 45056\n"
    "buffer_load_dwordx4 v[188:191], v233, s[44:47], s68 offen\n"
    "v_and_b32 v14, 0xffff0000, v152\n"
    "v_mul_f32 v13, s66, v13\n"
    "v_mul_f32 v14, s66, v14\n"
    "v_cvt_pk_bf16_f32 v152, v13, v14\n"
    "v_lshlrev_b32 v15, 16, v153\n"
    "v_and_b32 v9, 0xffff0000, v153\n"
    "v_mul_f32 v15, s66, v15\n"
    "v_mul_f32 v9, s66, v9\n"
    "v_cvt_pk_bf16_f32 v153, v15, v9\n"
    "v_lshlrev_b32 v13, 16, v154\n"
    "v_and_b32 v14, 0xffff0000, v154\n"
    "v_mul_f32 v13, s66, v13\n"
    "v_mul_f32 v14, s66, v14\n"
    "v_cvt_pk_bf16_f32 v154, v13, v14\n"
    "v_lshlrev_b32 v15, 16, v155\n"
    "v_and_b32 v9, 0xffff0000, v155\n"
    "v_mul_f32 v15, s66, v15\n"
    "v_mul_f32 v9, s66, v9\n"
    "v_cvt_pk_bf16_f32 v155, v15, v9\n"
    "buffer_load_dwordx4 v232, s[48:51], s59 offen lds\n"
    "ds_write_b128 v234, v[152:155] offset:8192\n"
    "s_waitcnt lgkmcnt(2)\n"
    "v_mfma_f32_16x16x32_bf16 v[20:23], a[80:83], a[8:11], v[20:23]\n"
    "s_waitcnt vmcnt(19) lgkmcnt(1)\n"
    "v_lshlrev_b32 v13, 16, v156\n"
    "v_and_b32 v14, 0xffff0000, v156\n"
    "v_mfma_f32_16x16x32_bf16 v[20:23], a[84:87], a[12:15], v[20:23]\n"
    "v_mul_f32 v13, s66, v13\n"
    "v_mul_f32 v14, s66, v14\n"
    "v_cvt_pk_bf16_f32 v156, v13, v14\n"
    "v_lshlrev_b32 v15, 16, v157\n"
    "v_and_b32 v9, 0xffff0000, v157\n"
    "v_mul_f32 v15, s66, v15\n"
    "v_mul_f32 v9, s66, v9\n"
    "v_cvt_pk_bf16_f32 v157, v15, v9\n"
    "v_lshlrev_b32 v13, 16, v158\n"
    "v_and_b32 v14, 0xffff0000, v158\n"
    "v_mul_f32 v13, s66, v13\n"
    "v_mul_f32 v14, s66, v14\n"
    "v_max3_f32 v11, v20, v21, v22\n"
    "v_max3_f32 v10, v10, v19, v23\n"
    "buffer_load_dwordx4 v232, s[48:51], s59 offen offset:2048 lds\n"
    "v_max_f32 v10, v10, v11\n"
    "v_mov_b32 v11, v10\n"
    "v_cvt_pk_bf16_f32 v158, v13, v14\n"
    "v_lshlrev_b32 v15, 16, v159\n"
    "v_permlane32_swap_b32 v10, v11\n"
    "v_and_b32 v9, 0xffff0000, v159\n"
    "v_max_f32 v10, v10, v11\n"
    "v_mov_b32 v11, v10\n"
    "v_mul_f32 v15, s66, v15\n"
    "v_mul_f32 v9, s66, v9\n"
    "v_permlane16_swap_b32 v10, v11\n"
    "v_cvt_pk_bf16_f32 v159, v15, v9\n"
    "v_min_f32 v240, -v10, -v11\n"
    "v_min_f32 v241, -v10, -v11\n"
    "v_min_f32 v242, -v10, -v11\n"
    "v_min_f32 v243, -v10, -v11\n"
    "v_add_f32 v16, v16, v240\n"
    "v_add_f32 v17, v17, v240\n"
    "v_add_f32 v18, v18, v240\n"
    "v_add_f32 v19, v19, v240\n"
    "v_add_f32 v20, v20, v240\n"
    "v_add_f32 v21, v21, v240\n"
    "v_add_f32 v22, v22, v240\n"
    "v_add_f32 v23, v23, v240\n"
    "ds_write_b128 v234, v[156:159] offset:12288\n"
    "s_waitcnt lgkmcnt(0)\n"
    "s_waitcnt vmcnt(18)\n"
    "s_barrier\n"
    "ds_read_b128 a[88:91], v1 offset:8192\n"
    "ds_read_b128 a[92:95], v1 offset:8704\n"
    "ds_read_b128 a[96:99], v1 offset:9216\n"
    "ds_read_b128 a[100:103], v1 offset:9728\n"
    "ds_read_b128 a[80:83], v1 offset:12288\n"
    "ds_read_b128 a[84:87], v1 offset:12800\n"
    "ds_read_b64_tr_b16 a[128:129], v3 offset:32768\n"
    "ds_read_b64_tr_b16 a[130:131], v3 offset:36864\n"
    "ds_read_b64_tr_b16 a[132:133], v3 offset:32776\n"
    "ds_read_b64_tr_b16 a[134:135], v3 offset:36872\n"
    "ds_read_b64_tr_b16 a[136:137], v3 offset:33280\n"
    "ds_read_b64_tr_b16 a[138:139], v3 offset:37376\n"
    "ds_read_b64_tr_b16 a[140:141], v3 offset:33288\n"
    "ds_read_b64_tr_b16 a[142:143], v3 offset:37384\n"
    "ds_read_b64_tr_b16 a[144:145], v3 offset:33792\n"
    "ds_read_b64_tr_b16 a[146:147], v3 offset:37888\n"
    "ds_read_b64_tr_b16 a[148:149], v3 offset:33800\n"
    "ds_read_b64_tr_b16 a[150:151], v3 offset:37896\n"
    "ds_read_b64_tr_b16 a[152:153], v3 offset:34304\n"
    "ds_read_b64_tr_b16 a[154:155], v3 offset:38400\n"
    "ds_read_b64_tr_b16 a[156:157], v3 offset:34312\n"
    "ds_read_b64_tr_b16 a[158:159], v3 offset:38408\n"
    "v_exp_f32 v16, v16\n"
    "v_exp_f32 v17, v17\n"
    "v_exp_f32 v18, v18\n"
    "v_add_f32 v5, v16, v17\n"
    "v_cvt_pk_bf16_f32 v16, v16, v17\n"
    "v_add_f32 v5, v5, v18\n"
    "v_exp_f32 v19, v19\n"
    "s_waitcnt vmcnt(17)\n"
    "v_lshlrev_b32 v13, 16, v160\n"
    "s_mov_b32 s67, 49152\n"
    "s_add_u32 m0, s57, 0\n"
    "s_add_u32 s59, s58, 32768\n"
    "buffer_load_dwordx4 v[192:195], v233, s[44:47], s67 offen\n"
    "s_mov_b32 s68, 53248\n"
    "buffer_load_dwordx4 v[196:199], v233, s[44:47], s68 offen\n"
    "v_add_f32 v5, v5, v19\n"
    "v_cvt_pk_bf16_f32 v17, v18, v19\n"
    "v_and_b32 v14, 0xffff0000, v160\n"
    "v_mul_f32 v13, s66, v13\n"
    "v_mul_f32 v14, s66, v14\n"
    "v_cvt_pk_bf16_f32 v160, v13, v14\n"
    "v_lshlrev_b32 v15, 16, v161\n"
    "v_and_b32 v9, 0xffff0000, v161\n"
    "v_mul_f32 v15, s66, v15\n"
    "v_mul_f32 v9, s66, v9\n"
    "v_cvt_pk_bf16_f32 v161, v15, v9\n"
    "v_lshlrev_b32 v13, 16, v162\n"
    "v_and_b32 v14, 0xffff0000, v162\n"
    "v_mul_f32 v13, s66, v13\n"
    "v_mul_f32 v14, s66, v14\n"
    "v_cvt_pk_bf16_f32 v162, v13, v14\n"
    "v_lshlrev_b32 v15, 16, v163\n"
    "v_and_b32 v9, 0xffff0000, v163\n"
    "s_waitcnt lgkmcnt(15)\n"
    "v_mfma_f32_16x16x32_bf16 v[48:51], a[88:91], a[0:3], v[240:243]\n"
    "buffer_load_dwordx4 v232, s[48:51], s59 offen lds\n"
    "ds_read_b128 a[88:91], v1 offset:13312\n"
    "v_mfma_f32_16x16x32_bf16 v[48:51], a[92:95], a[4:7], v[48:51]\n"
    "ds_read_b128 a[92:95], v1 offset:13824\n"
    "v_mul_f32 v15, s66, v15\n"
    "v_mfma_f32_16x16x32_bf16 v[48:51], a[96:99], a[8:11], v[48:51]\n"
    "v_mul_f32 v9, s66, v9\n"
    "v_cvt_pk_bf16_f32 v163, v15, v9\n"
    "v_mfma_f32_16x16x32_bf16 v[48:51], a[100:103], a[12:15], v[48:51]\n"
    "s_waitcnt vmcnt(19)\n"
    "ds_write_b128 v234, v[160:163] offset:16384\n"
    "v_lshlrev_b32 v13, 16, v164\n"
    "v_mfma_f32_16x16x32_bf16 v[52:55], a[80:83], a[0:3], v[240:243]\n"
    "v_and_b32 v14, 0xffff0000, v164\n"
    "v_mul_f32 v13, s66, v13\n"
    "v_mfma_f32_16x16x32_bf16 v[52:55], a[84:87], a[4:7], v[52:55]\n"
    "v_mul_f32 v14, s66, v14\n"
    "v_cvt_pk_bf16_f32 v164, v13, v14\n"
    "v_lshlrev_b32 v15, 16, v165\n"
    "buffer_load_dwordx4 v232, s[48:51], s59 offen offset:2048 lds\n"
    "v_and_b32 v9, 0xffff0000, v165\n"
    "v_mul_f32 v15, s66, v15\n"
    "v_mul_f32 v9, s66, v9\n"
    "v_cvt_pk_bf16_f32 v165, v15, v9\n"
    "v_lshlrev_b32 v13, 16, v166\n"
    "v_and_b32 v14, 0xffff0000, v166\n"
    "v_mul_f32 v13, s66, v13\n"
    "v_mul_f32 v14, s66, v14\n"
    "v_cvt_pk_bf16_f32 v166, v13, v14\n"
    "v_lshlrev_b32 v15, 16, v167\n"
    "v_and_b32 v9, 0xffff0000, v167\n"
    "v_mul_f32 v15, s66, v15\n"
    "v_mul_f32 v9, s66, v9\n"
    "v_cvt_pk_bf16_f32 v167, v15, v9\n"
    "v_exp_f32 v48, v48\n"
    "ds_write_b128 v234, v[164:167] offset:20480\n"
    "v_exp_f32 v49, v49\n"
    "v_exp_f32 v50, v50\n"
    "v_exp_f32 v51, v51\n"
    "s_waitcnt vmcnt(15)\n"
    "v_lshlrev_b32 v13, 16, v168\n"
    "s_mov_b32 s67, 57344\n"
    "s_add_u32 m0, s57, 8192\n"
    "s_add_u32 s59, s58, 40960\n"
    "buffer_load_dwordx4 v[200:203], v233, s[44:47], s67 offen\n"
    "s_mov_b32 s68, 61440\n"
    "v_and_b32 v14, 0xffff0000, v168\n"
    "v_mul_f32 v13, s66, v13\n"
    "s_waitcnt lgkmcnt(3)\n"
    "v_mfma_f32_16x16x32_bf16 v[52:55], a[88:91], a[8:11], v[52:55]\n"
    "s_waitcnt lgkmcnt(2)\n"
    "v_mul_f32 v14, s66, v14\n"
    "v_cvt_pk_bf16_f32 v168, v13, v14\n"
    "v_mfma_f32_16x16x32_bf16 v[52:55], a[92:95], a[12:15], v[52:55]\n"
    "v_lshlrev_b32 v15, 16, v169\n"
    "v_and_b32 v9, 0xffff0000, v169\n"
    "v_mul_f32 v15, s66, v15\n"
    "v_mul_f32 v9, s66, v9\n"
    "buffer_load_dwordx4 v[204:207], v233, s[44:47], s68 offen\n"
    "v_cvt_pk_bf16_f32 v169, v15, v9\n"
    "v_lshlrev_b32 v13, 16, v170\n"
    "v_and_b32 v14, 0xffff0000, v170\n"
    "v_mul_f32 v13, s66, v13\n"
    "v_mul_f32 v14, s66, v14\n"
    "v_cvt_pk_bf16_f32 v170, v13, v14\n"
    "v_lshlrev_b32 v15, 16, v171\n"
    "v_and_b32 v9, 0xffff0000, v171\n"
    "v_mul_f32 v15, s66, v15\n"
    "v_mul_f32 v9, s66, v9\n"
    "v_cvt_pk_bf16_f32 v171, v15, v9\n"
    "s_waitcnt vmcnt(16)\n"
    "v_lshlrev_b32 v13, 16, v172\n"
    "v_and_b32 v14, 0xffff0000, v172\n"
    "v_mul_f32 v13, s66, v13\n"
    "v_mul_f32 v14, s66, v14\n"
    "v_cvt_pk_bf16_f32 v172, v13, v14\n"
    "v_lshlrev_b32 v15, 16, v173\n"
    "v_and_b32 v9, 0xffff0000, v173\n"
    "v_mul_f32 v15, s66, v15\n"
    "v_mul_f32 v9, s66, v9\n"
    "v_cvt_pk_bf16_f32 v173, v15, v9\n"
    "buffer_load_dwordx4 v232, s[48:51], s59 offen lds\n"
    "ds_write_b128 v234, v[168:171] offset:24576\n"
    "v_lshlrev_b32 v13, 16, v174\n"
    "v_and_b32 v14, 0xffff0000, v174\n"
    "v_mul_f32 v13, s66, v13\n"
    "v_mul_f32 v14, s66, v14\n"
    "v_cvt_pk_bf16_f32 v174, v13, v14\n"
    "v_lshlrev_b32 v15, 16, v175\n"
    "v_and_b32 v9, 0xffff0000, v175\n"
    "v_mul_f32 v15, s66, v15\n"
    "v_mul_f32 v9, s66, v9\n"
    "v_cvt_pk_bf16_f32 v175, v15, v9\n"
    "v_exp_f32 v20, v20\n"
    "ds_write_b128 v234, v[172:175] offset:28672\n"
    "v_add_f32 v5, v5, v20\n"
    "v_exp_f32 v21, v21\n"
    "v_exp_f32 v22, v22\n"
    "buffer_load_dwordx4 v232, s[48:51], s59 offen offset:2048 lds\n"
    "v_add_f32 v5, v5, v21\n"
    "v_cvt_pk_bf16_f32 v18, v20, v21\n"
    "v_add_f32 v5, v5, v22\n"
    "v_exp_f32 v23, v23\n"
    "v_exp_f32 v52, v52\n"
    "v_add_f32 v5, v5, v23\n"
    "v_add_f32 v5, v5, v48\n"
    "v_add_f32 v5, v5, v49\n"
    "v_cvt_pk_bf16_f32 v48, v48, v49\n"
    "v_add_f32 v5, v5, v50\n"
    "v_add_f32 v5, v5, v51\n"
    "v_cvt_pk_bf16_f32 v49, v50, v51\n"
    "v_cvt_pk_bf16_f32 v19, v22, v23\n"
    "v_add_f32 v5, v5, v52\n"
    "s_nop 0\n"
    "v_mfma_f32_16x16x32_bf16 v[80:83], a[128:131], v[16:19], 0\n"
    "v_exp_f32 v53, v53\n"
    "v_mfma_f32_16x16x32_bf16 v[84:87], a[132:135], v[16:19], 0\n"
    "v_add_f32 v5, v5, v53\n"
    "v_cvt_pk_bf16_f32 v50, v52, v53\n"
    "v_mfma_f32_16x16x32_bf16 v[88:91], a[136:139], v[16:19], 0\n"
    "v_exp_f32 v54, v54\n"
    "v_mfma_f32_16x16x32_bf16 v[92:95], a[140:143], v[16:19], 0\n"
    "v_add_f32 v5, v5, v54\n"
    "v_mfma_f32_16x16x32_bf16 v[96:99], a[144:147], v[16:19], 0\n"
    "v_exp_f32 v55, v55\n"
    "v_mfma_f32_16x16x32_bf16 v[100:103], a[148:151], v[16:19], 0\n"
    "v_add_f32 v5, v5, v55\n"
    "v_cvt_pk_bf16_f32 v51, v54, v55\n"
    "v_mfma_f32_16x16x32_bf16 v[104:107], a[152:155], v[16:19], 0\n"
    "v_mfma_f32_16x16x32_bf16 v[108:111], a[156:159], v[16:19], 0\n"
    "s_waitcnt vmcnt(15)\n"
    "s_mov_b32 s60, 32768\n"
    "buffer_load_dwordx4 a[32:35], v4, s[40:43], s60 offen offset:0\n"
    "buffer_load_dwordx4 a[36:39], v4, s[40:43], s60 offen offset:64\n"
    "buffer_load_dwordx4 a[40:43], v4, s[40:43], s60 offen offset:128\n"
    "buffer_load_dwordx4 a[44:47], v4, s[40:43], s60 offen offset:192\n"
    "v_lshlrev_b32 v13, 16, v176\n"
    "s_mov_b32 s67, 65536\n"
    "s_add_u32 m0, s57, 16384\n"
    "s_add_u32 s59, s58, 49152\n"
    "v_and_b32 v14, 0xffff0000, v176\n"
    "v_mul_f32 v13, s66, v13\n"
    "v_mul_f32 v14, s66, v14\n"
    "v_cvt_pk_bf16_f32 v176, v13, v14\n"
    "v_lshlrev_b32 v15, 16, v177\n"
    "buffer_load_dwordx4 v[208:211], v233, s[44:47], s67 offen\n"
    "s_mov_b32 s68, 69632\n"
    "v_and_b32 v9, 0xffff0000, v177\n"
    "v_mul_f32 v15, s66, v15\n"
    "v_mul_f32 v9, s66, v9\n"
    "v_cvt_pk_bf16_f32 v177, v15, v9\n"
    "v_lshlrev_b32 v13, 16, v178\n"
    "v_and_b32 v14, 0xffff0000, v178\n"
    "v_mul_f32 v13, s66, v13\n"
    "v_mul_f32 v14, s66, v14\n"
    "v_cvt_pk_bf16_f32 v178, v13, v14\n"
    "v_lshlrev_b32 v15, 16, v179\n"
    "v_and_b32 v9, 0xffff0000, v179\n"
    "v_mul_f32 v15, s66, v15\n"
    "v_mul_f32 v9, s66, v9\n"
    "v_cvt_pk_bf16_f32 v179, v15, v9\n"
    "s_waitcnt vmcnt(19)\n"
    "v_lshlrev_b32 v13, 16, v180\n"
    "v_and_b32 v14, 0xffff0000, v180\n"
    "v_mul_f32 v13, s66, v13\n"
    "v_mul_f32 v14, s66, v14\n"
    "v_cvt_pk_bf16_f32 v180, v13, v14\n"
    "v_lshlrev_b32 v15, 16, v181\n"
    "buffer_load_dwordx4 v[212:215], v233, s[44:47], s68 offen\n"
    "ds_write_b128 v234, v[176:179] offset:32768\n"
    "v_and_b32 v9, 0xffff0000, v181\n"
    "v_mul_f32 v15, s66, v15\n"
    "v_mul_f32 v9, s66, v9\n"
    "v_cvt_pk_bf16_f32 v181, v15, v9\n"
    "v_lshlrev_b32 v13, 16, v182\n"
    "v_and_b32 v14, 0xffff0000, v182\n"
    "v_mul_f32 v13, s66, v13\n"
    "v_mul_f32 v14, s66, v14\n"
    "v_cvt_pk_bf16_f32 v182, v13, v14\n"
    "v_lshlrev_b32 v15, 16, v183\n"
    "v_and_b32 v9, 0xffff0000, v183\n"
    "s_waitcnt lgkmcnt(3)\n"
    "s_waitcnt lgkmcnt(1)\n"
    "s_waitcnt vmcnt(18)\n"
    "s_barrier\n"
    "v_mul_f32 v15, s66, v15\n"
    "v_mul_f32 v9, s66, v9\n"
    "v_cvt_pk_bf16_f32 v183, v15, v9\n"
    "ds_read_b128 a[96:99], v1 offset:16384\n"
    "ds_write_b128 v234, v[180:183] offset:36864\n"
    "ds_read_b128 a[100:103], v1 offset:16896\n"
    "buffer_load_dwordx4 v232, s[48:51], s59 offen lds\n"
    "ds_read_b128 a[80:83], v1 offset:17408\n"
    "ds_read_b128 a[84:87], v1 offset:17920\n"
    "ds_read_b128 a[88:91], v1 offset:20480\n"
    "ds_read_b128 a[92:95], v1 offset:20992\n"
    "ds_read_b64_tr_b16 a[160:161], v3 offset:40960\n"
    "ds_read_b64_tr_b16 a[162:163], v3 offset:45056\n"
    "ds_read_b64_tr_b16 a[164:165], v3 offset:40968\n"
    "ds_read_b64_tr_b16 a[166:167], v3 offset:45064\n"
    "ds_read_b64_tr_b16 a[168:169], v3 offset:41472\n"
    "ds_read_b64_tr_b16 a[170:171], v3 offset:45568\n"
    "ds_read_b64_tr_b16 a[172:173], v3 offset:41480\n"
    "ds_read_b64_tr_b16 a[174:175], v3 offset:45576\n"
    "ds_read_b64_tr_b16 a[176:177], v3 offset:41984\n"
    "ds_read_b64_tr_b16 a[178:179], v3 offset:46080\n"
    "ds_read_b64_tr_b16 a[180:181], v3 offset:41992\n"
    "ds_read_b64_tr_b16 a[182:183], v3 offset:46088\n"
    "ds_read_b64_tr_b16 a[184:185], v3 offset:42496\n"
    "ds_read_b64_tr_b16 a[186:187], v3 offset:46592\n"
    "ds_read_b64_tr_b16 a[188:189], v3 offset:42504\n"
    "ds_read_b64_tr_b16 a[190:191], v3 offset:46600\n"
    "ds_read_b64_tr_b16 a[192:193], v3 offset:49152\n"
    "ds_read_b64_tr_b16 a[194:195], v3 offset:53248\n"
    "ds_read_b64_tr_b16 a[196:197], v3 offset:49160\n"
    "buffer_load_dwordx4 v232, s[48:51], s59 offen offset:2048 lds\n"
    "ds_read_b64_tr_b16 a[198:199], v3 offset:53256\n"
    "ds_read_b64_tr_b16 a[200:201], v3 offset:49664\n"
    "ds_read_b64_tr_b16 a[202:203], v3 offset:53760\n"
    "ds_read_b64_tr_b16 a[204:205], v3 offset:49672\n"
    "ds_read_b64_tr_b16 a[206:207], v3 offset:53768\n"
    "ds_read_b64_tr_b16 a[208:209], v3 offset:50176\n"
    "ds_read_b64_tr_b16 a[210:211], v3 offset:54272\n"
    "ds_read_b64_tr_b16 a[212:213], v3 offset:50184\n"
    "ds_read_b64_tr_b16 a[214:215], v3 offset:54280\n"
    "ds_read_b64_tr_b16 a[216:217], v3 offset:50688\n"
    "ds_read_b64_tr_b16 a[218:219], v3 offset:54784\n"
    "ds_read_b64_tr_b16 a[220:221], v3 offset:50696\n"
    "ds_read_b64_tr_b16 a[222:223], v3 offset:54792\n"
    "s_waitcnt lgkmcnt(15)\n"
    "v_mfma_f32_16x16x32_bf16 v[16:19], a[96:99], a[0:3], v[240:243]\n"
    "ds_read_b128 a[96:99], v1 offset:21504\n"
    "v_mfma_f32_16x16x32_bf16 v[16:19], a[100:103], a[4:7], v[16:19]\n"
    "ds_read_b128 a[100:103], v1 offset:22016\n"
    "v_mfma_f32_16x16x32_bf16 v[16:19], a[80:83], a[8:11], v[16:19]\n"
    "ds_read_b128 a[80:83], v1 offset:24576\n"
    "v_mfma_f32_16x16x32_bf16 v[16:19], a[84:87], a[12:15], v[16:19]\n"
    "s_waitcnt vmcnt(19)\n"
    "ds_read_b128 a[84:87], v1 offset:25088\n"
    "v_lshlrev_b32 v13, 16, v184\n"
    "v_mfma_f32_16x16x32_bf16 v[20:23], a[88:91], a[0:3], v[240:243]\n"
    "s_mov_b32 s67, 73728\n"
    "s_add_u32 m0, s57, 24576\n"
    "s_add_u32 s59, s58, 57344\n"
    "v_mfma_f32_16x16x32_bf16 v[20:23], a[92:95], a[4:7], v[20:23]\n"
    "buffer_load_dwordx4 v[216:219], v233, s[44:47], s67 offen\n"
    "s_mov_b32 s68, 77824\n"
    "buffer_load_dwordx4 v[220:223], v233, s[44:47], s68 offen\n"
    "ds_read_b128 a[88:91], v1 offset:25600\n"
    "ds_read_b128 a[92:95], v1 offset:26112\n"
    "v_exp_f32 v16, v16\n"
    "v_exp_f32 v17, v17\n"
    "v_add_f32 v5, v5, v16\n"
    "v_add_f32 v5, v5, v17\n"
    "v_cvt_pk_bf16_f32 v16, v16, v17\n"
    "v_exp_f32 v18, v18\n"
    "v_exp_f32 v19, v19\n"
    "v_add_f32 v5, v5, v18\n"
    "v_add_f32 v5, v5, v19\n"
    "v_cvt_pk_bf16_f32 v17, v18, v19\n"
    "v_and_b32 v14, 0xffff0000, v184\n"
    "v_mul_f32 v13, s66, v13\n"
    "v_mul_f32 v14, s66, v14\n"
    "v_cvt_pk_bf16_f32 v184, v13, v14\n"
    "v_lshlrev_b32 v15, 16, v185\n"
    "buffer_load_dwordx4 v232, s[48:51], s59 offen lds\n"
    "v_and_b32 v9, 0xffff0000, v185\n"
    "v_mul_f32 v15, s66, v15\n"
    "v_mul_f32 v9, s66, v9\n"
    "v_cvt_pk_bf16_f32 v185, v15, v9\n"
    "v_lshlrev_b32 v13, 16, v186\n"
    "v_and_b32 v14, 0xffff0000, v186\n"
    "v_mul_f32 v13, s66, v13\n"
    "v_mul_f32 v14, s66, v14\n"
    "v_cvt_pk_bf16_f32 v186, v13, v14\n"
    "v_lshlrev_b32 v15, 16, v187\n"
    "v_and_b32 v9, 0xffff0000, v187\n"
    "v_mul_f32 v15, s66, v15\n"
    "v_mul_f32 v9, s66, v9\n"
    "s_waitcnt lgkmcnt(5)\n"
    "v_mfma_f32_16x16x32_bf16 v[20:23], a[96:99], a[8:11], v[20:23]\n"
    "s_waitcnt lgkmcnt(4)\n"
    "ds_read_b128 a[96:99], v1 offset:28672\n"
    "v_cvt_pk_bf16_f32 v187, v15, v9\n"
    "v_mfma_f32_16x16x32_bf16 v[20:23], a[100:103], a[12:15], v[20:23]\n"
    "ds_write_b128 v234, v[184:187] offset:40960\n"
    "ds_read_b128 a[100:103], v1 offset:29184\n"
    "v_mfma_f32_16x16x32_bf16 v[80:83], a[160:163], v[48:51], v[80:83]\n"
    "s_waitcnt vmcnt(21)\n"
    "buffer_load_dwordx4 v232, s[48:51], s59 offen offset:2048 lds\n"
    "v_lshlrev_b32 v13, 16, v188\n"
    "v_mfma_f32_16x16x32_bf16 v[84:87], a[164:167], v[48:51], v[84:87]\n"
    "v_and_b32 v14, 0xffff0000, v188\n"
    "v_mul_f32 v13, s66, v13\n"
    "v_mfma_f32_16x16x32_bf16 v[88:91], a[168:171], v[48:51], v[88:91]\n"
    "v_mul_f32 v14, s66, v14\n"
    "v_cvt_pk_bf16_f32 v188, v13, v14\n"
    "v_mfma_f32_16x16x32_bf16 v[92:95], a[172:175], v[48:51], v[92:95]\n"
    "v_lshlrev_b32 v15, 16, v189\n"
    "v_and_b32 v9, 0xffff0000, v189\n"
    "v_mfma_f32_16x16x32_bf16 v[96:99], a[176:179], v[48:51], v[96:99]\n"
    "v_mul_f32 v15, s66, v15\n"
    "v_mul_f32 v9, s66, v9\n"
    "v_mfma_f32_16x16x32_bf16 v[100:103], a[180:183], v[48:51], v[100:103]\n"
    "v_cvt_pk_bf16_f32 v189, v15, v9\n"
    "v_lshlrev_b32 v13, 16, v190\n"
    "v_mfma_f32_16x16x32_bf16 v[104:107], a[184:187], v[48:51], v[104:107]\n"
    "v_and_b32 v14, 0xffff0000, v190\n"
    "v_mul_f32 v13, s66, v13\n"
    "v_mfma_f32_16x16x32_bf16 v[108:111], a[188:191], v[48:51], v[108:111]\n"
    "s_waitcnt lgkmcnt(6)\n"
    "v_mul_f32 v14, s66, v14\n"
    "v_cvt_pk_bf16_f32 v190, v13, v14\n"
    "v_mfma_f32_16x16x32_bf16 v[48:51], a[80:83], a[0:3], v[240:243]\n"
    "v_lshlrev_b32 v15, 16, v191\n"
    "v_and_b32 v9, 0xffff0000, v191\n"
    "v_mfma_f32_16x16x32_bf16 v[56:59], a[80:83], a[16:19], 0\n"
    "s_waitcnt lgkmcnt(5)\n"
    "ds_read_b128 a[80:83], v1 offset:29696\n"
    "v_mul_f32 v15, s66, v15\n"
    "v_mfma_f32_16x16x32_bf16 v[48:51], a[84:87], a[4:7], v[48:51]\n"
    "v_mul_f32 v9, s66, v9\n"
    "v_cvt_pk_bf16_f32 v191, v15, v9\n"
    "v_mfma_f32_16x16x32_bf16 v[56:59], a[84:87], a[20:23], v[56:59]\n"
    "s_waitcnt lgkmcnt(5)\n"
    "ds_write_b128 v234, v[188:191] offset:45056\n"
    "ds_read_b128 a[84:87], v1 offset:30208\n"
    "v_mfma_f32_16x16x32_bf16 v[48:51], a[88:91], a[8:11], v[48:51]\n"
    "v_exp_f32 v20, v20\n"
    "v_mfma_f32_16x16x32_bf16 v[56:59], a[88:91], a[24:27], v[56:59]\n"
    "s_waitcnt lgkmcnt(6)\n"
    "v_add_f32 v5, v5, v20\n"
    "v_mfma_f32_16x16x32_bf16 v[48:51], a[92:95], a[12:15], v[48:51]\n"
    "v_exp_f32 v21, v21\n"
    "v_mfma_f32_16x16x32_bf16 v[56:59], a[92:95], a[28:31], v[56:59]\n"
    "s_waitcnt lgkmcnt(5)\n"
    "v_add_f32 v5, v5, v21\n"
    "v_cvt_pk_bf16_f32 v18, v20, v21\n"
    "v_mfma_f32_16x16x32_bf16 v[52:55], a[96:99], a[0:3], v[240:243]\n"
    "v_exp_f32 v22, v22\n"
    "v_mfma_f32_16x16x32_bf16 v[60:63], a[96:99], a[16:19], 0\n"
    "s_waitcnt lgkmcnt(3)\n"
    "v_exp_f32 v48, v48\n"
    "v_mfma_f32_16x16x32_bf16 v[52:55], a[100:103], a[4:7], v[52:55]\n"
    "v_max3_f32 v10, v56, v57, v58\n"
    "v_add_f32 v5, v5, v22\n"
    "v_mfma_f32_16x16x32_bf16 v[60:63], a[100:103], a[20:23], v[60:63]\n"
    "s_waitcnt vmcnt(19)\n"
    "v_exp_f32 v49, v49\n"
    "v_exp_f32 v50, v50\n"
    "v_exp_f32 v51, v51\n"
    "v_exp_f32 v23, v23\n"
    "v_lshlrev_b32 v13, 16, v192\n"
    "s_mov_b32 s67, 81920\n"
    "buffer_load_dwordx4 v[224:227], v233, s[44:47], s67 offen\n"
    "s_mov_b32 s68, 86016\n"
    "buffer_load_dwordx4 v[228:231], v233, s[44:47], s68 offen\n"
    "v_add_f32 v5, v5, v23\n"
    "v_add_f32 v5, v5, v48\n"
    "v_add_f32 v5, v5, v49\n"
    "v_cvt_pk_bf16_f32 v48, v48, v49\n"
    "v_add_f32 v5, v5, v50\n"
    "s_waitcnt lgkmcnt(2)\n"
    "v_mfma_f32_16x16x32_bf16 v[52:55], a[80:83], a[8:11], v[52:55]\n"
    "v_add_f32 v5, v5, v51\n"
    "v_cvt_pk_bf16_f32 v49, v50, v51\n"
    "v_mfma_f32_16x16x32_bf16 v[60:63], a[80:83], a[24:27], v[60:63]\n"
    "s_waitcnt lgkmcnt(0)\n"
    "v_cvt_pk_bf16_f32 v19, v22, v23\n"
    "v_and_b32 v14, 0xffff0000, v192\n"
    "v_mul_f32 v13, s66, v13\n"
    "v_mfma_f32_16x16x32_bf16 v[52:55], a[84:87], a[12:15], v[52:55]\n"
    "s_waitcnt vmcnt(18)\n"
    "s_barrier\n"
    "v_mul_f32 v14, s66, v14\n"
    "s_add_u32 m0, s57, 32768\n"
    "v_mfma_f32_16x16x32_bf16 v[60:63], a[84:87], a[28:31], v[60:63]\n"
    "s_add_u32 s59, s58, 65536\n"
    "ds_read_b128 a[88:91], v1 offset:32768\n"
    "buffer_load_dwordx4 v232, s[48:51], s59 offen lds\n"
    "v_mfma_f32_16x16x32_bf16 v[80:83], a[192:195], v[16:19], v[80:83]\n"
    "ds_read_b128 a[92:95], v1 offset:33280\n"
    "ds_read_b128 a[96:99], v1 offset:33792\n"
    "v_mfma_f32_16x16x32_bf16 v[84:87], a[196:199], v[16:19], v[84:87]\n"
    "ds_read_b128 a[100:103], v1 offset:34304\n"
    "ds_read_b128 a[80:83], v1 offset:36864\n"
    "v_mfma_f32_16x16x32_bf16 v[88:91], a[200:203], v[16:19], v[88:91]\n"
    "ds_read_b128 a[84:87], v1 offset:37376\n"
    "ds_read_b64_tr_b16 a[224:225], v3 offset:57344\n"
    "v_mfma_f32_16x16x32_bf16 v[92:95], a[204:207], v[16:19], v[92:95]\n"
    "ds_read_b64_tr_b16 a[226:227], v3 offset:61440\n"
    "ds_read_b64_tr_b16 a[228:229], v3 offset:57352\n"
    "v_mfma_f32_16x16x32_bf16 v[96:99], a[208:211], v[16:19], v[96:99]\n"
    "ds_read_b64_tr_b16 a[230:231], v3 offset:61448\n"
    "ds_read_b64_tr_b16 a[232:233], v3 offset:57856\n"
    "v_mfma_f32_16x16x32_bf16 v[100:103], a[212:215], v[16:19], v[100:103]\n"
    "buffer_load_dwordx4 v232, s[48:51], s59 offen offset:2048 lds\n"
    "ds_read_b64_tr_b16 a[234:235], v3 offset:61952\n"
    "v_mfma_f32_16x16x32_bf16 v[104:107], a[216:219], v[16:19], v[104:107]\n"
    "ds_read_b64_tr_b16 a[236:237], v3 offset:57864\n"
    "ds_read_b64_tr_b16 a[238:239], v3 offset:61960\n"
    "v_mfma_f32_16x16x32_bf16 v[108:111], a[220:223], v[16:19], v[108:111]\n"
    "ds_read_b64_tr_b16 a[240:241], v3 offset:58368\n"
    "ds_read_b64_tr_b16 a[242:243], v3 offset:62464\n"
    "ds_read_b64_tr_b16 a[244:245], v3 offset:58376\n"
    "ds_read_b64_tr_b16 a[246:247], v3 offset:62472\n"
    "ds_read_b64_tr_b16 a[248:249], v3 offset:58880\n"
    "ds_read_b64_tr_b16 a[250:251], v3 offset:62976\n"
    "ds_read_b64_tr_b16 a[252:253], v3 offset:58888\n"
    "ds_read_b64_tr_b16 a[254:255], v3 offset:62984\n"
    "ds_read_b64_tr_b16 a[104:105], v3 offset:0\n"
    "ds_read_b64_tr_b16 a[106:107], v3 offset:4096\n"
    "ds_read_b64_tr_b16 a[108:109], v3 offset:8\n"
    "ds_read_b64_tr_b16 a[110:111], v3 offset:4104\n"
    "ds_read_b64_tr_b16 a[112:113], v3 offset:512\n"
    "ds_read_b64_tr_b16 a[114:115], v3 offset:4608\n"
    "ds_read_b64_tr_b16 a[116:117], v3 offset:520\n"
    "ds_read_b64_tr_b16 a[118:119], v3 offset:4616\n"
    "ds_read_b64_tr_b16 a[120:121], v3 offset:1024\n"
    "ds_read_b64_tr_b16 a[122:123], v3 offset:5120\n"
    "ds_read_b64_tr_b16 a[124:125], v3 offset:1032\n"
    "ds_read_b64_tr_b16 a[126:127], v3 offset:5128\n"
    "v_max3_f32 v11, v60, v61, v62\n"
    "v_max3_f32 v10, v10, v59, v63\n"
    "v_max_f32 v10, v10, v11\n"
    "v_mov_b32 v11, v10\n"
    "v_cvt_pk_bf16_f32 v192, v13, v14\n"
    "v_lshlrev_b32 v15, 16, v193\n"
    "v_permlane32_swap_b32 v10, v11\n"
    "v_and_b32 v9, 0xffff0000, v193\n"
    "s_waitcnt lgkmcnt(15)\n"
    "v_mfma_f32_16x16x32_bf16 v[16:19], a[88:91], a[0:3], v[240:243]\n"
    "v_max_f32 v10, v10, v11\n"
    "v_mov_b32 v11, v10\n"
    "v_mul_f32 v15, s66, v15\n"
    "v_mul_f32 v9, s66, v9\n"
    "v_permlane16_swap_b32 v10, v11\n"
    "v_cvt_pk_bf16_f32 v193, v15, v9\n"
    "v_min_f32 v244, -v10, -v11\n"
    "v_min_f32 v245, -v10, -v11\n"
    "v_min_f32 v246, -v10, -v11\n"
    "v_min_f32 v247, -v10, -v11\n"
    "v_add_f32 v56, v56, v244\n"
    "v_add_f32 v57, v57, v244\n"
    "v_mfma_f32_16x16x32_bf16 v[24:27], a[88:91], a[16:19], v[244:247]\n"
    "ds_read_b128 a[88:91], v1 offset:37888\n"
    "v_add_f32 v58, v58, v244\n"
    "v_mfma_f32_16x16x32_bf16 v[16:19], a[92:95], a[4:7], v[16:19]\n"
    "v_add_f32 v59, v59, v244\n"
    "v_add_f32 v60, v60, v244\n"
    "v_mfma_f32_16x16x32_bf16 v[24:27], a[92:95], a[20:23], v[24:27]\n"
    "ds_read_b128 a[92:95], v1 offset:38400\n"
    "v_add_f32 v61, v61, v244\n"
    "v_mfma_f32_16x16x32_bf16 v[16:19], a[96:99], a[8:11], v[16:19]\n"
    "v_add_f32 v62, v62, v244\n"
    "v_add_f32 v63, v63, v244\n"
    "v_mfma_f32_16x16x32_bf16 v[24:27], a[96:99], a[24:27], v[24:27]\n"
    "v_exp_f32 v56, v56\n"
    "v_mfma_f32_16x16x32_bf16 v[16:19], a[100:103], a[12:15], v[16:19]\n"
    "v_exp_f32 v57, v57\n"
    "v_mfma_f32_16x16x32_bf16 v[24:27], a[100:103], a[28:31], v[24:27]\n"
    "ds_read_b128 a[96:99], v1 offset:40960\n"
    "ds_read_b128 a[100:103], v1 offset:41472\n"
    "v_mfma_f32_16x16x32_bf16 v[20:23], a[80:83], a[0:3], v[240:243]\n"
    "v_add_f32 v6, v56, v57\n"
    "v_cvt_pk_bf16_f32 v56, v56, v57\n"
    "v_mfma_f32_16x16x32_bf16 v[28:31], a[80:83], a[16:19], v[244:247]\n"
    "v_exp_f32 v58, v58\n"
    "v_mfma_f32_16x16x32_bf16 v[20:23], a[84:87], a[4:7], v[20:23]\n"
    "ds_read_b128 a[80:83], v1 offset:41984\n"
    "v_add_f32 v6, v6, v58\n"
    "v_mfma_f32_16x16x32_bf16 v[28:31], a[84:87], a[20:23], v[28:31]\n"
    "ds_read_b128 a[84:87], v1 offset:42496\n"
    "v_exp_f32 v59, v59\n"
    "v_lshlrev_b32 v13, 16, v194\n"
    "v_add_f32 v6, v6, v59\n"
    "v_cvt_pk_bf16_f32 v57, v58, v59\n"
    "v_and_b32 v14, 0xffff0000, v194\n"
    "v_mul_f32 v13, s66, v13\n"
    "v_mul_f32 v14, s66, v14\n"
    "v_cvt_pk_bf16_f32 v194, v13, v14\n"
    "v_lshlrev_b32 v15, 16, v195\n"
    "v_and_b32 v9, 0xffff0000, v195\n"
    "v_mul_f32 v15, s66, v15\n"
    "v_mul_f32 v9, s66, v9\n"
    "v_cvt_pk_bf16_f32 v195, v15, v9\n"
    "v_lshlrev_b32 v13, 16, v196\n"
    "v_and_b32 v14, 0xffff0000, v196\n"
    "v_mul_f32 v13, s66, v13\n"
    "v_mul_f32 v14, s66, v14\n"
    "v_cvt_pk_bf16_f32 v196, v13, v14\n"
    "s_waitcnt lgkmcnt(5)\n"
    "v_mfma_f32_16x16x32_bf16 v[20:23], a[88:91], a[8:11], v[20:23]\n"
    "ds_write_b128 v234, v[192:195] offset:49152\n"
    "v_lshlrev_b32 v15, 16, v197\n"
    "v_mfma_f32_16x16x32_bf16 v[28:31], a[88:91], a[24:27], v[28:31]\n"
    "s_waitcnt lgkmcnt(5)\n"
    "ds_read_b128 a[88:91], v1 offset:45056\n"
    "v_and_b32 v9, 0xffff0000, v197\n"
    "v_mfma_f32_16x16x32_bf16 v[20:23], a[92:95], a[12:15], v[20:23]\n"
    "v_mul_f32 v15, s66, v15\n"
    "v_mul_f32 v9, s66, v9\n"
    "v_mfma_f32_16x16x32_bf16 v[28:31], a[92:95], a[28:31], v[28:31]\n"
    "ds_read_b128 a[92:95], v1 offset:45568\n"
    "v_cvt_pk_bf16_f32 v197, v15, v9\n"
    "v_lshlrev_b32 v13, 16, v198\n"
    "v_and_b32 v14, 0xffff0000, v198\n"
    "v_mul_f32 v13, s66, v13\n"
    "v_mul_f32 v14, s66, v14\n"
    "v_cvt_pk_bf16_f32 v198, v13, v14\n"
    "v_lshlrev_b32 v15, 16, v199\n"
    "v_and_b32 v9, 0xffff0000, v199\n"
    "v_mul_f32 v15, s66, v15\n"
    "v_mul_f32 v9, s66, v9\n"
    "v_cvt_pk_bf16_f32 v199, v15, v9\n"
    "v_exp_f32 v52, v52\n"
    "ds_write_b128 v234, v[196:199] offset:53248\n"
    "v_add_f32 v5, v5, v52\n"
    "v_exp_f32 v53, v53\n"
    "v_exp_f32 v54, v54\n"
    "v_add_f32 v5, v5, v53\n"
    "v_cvt_pk_bf16_f32 v50, v52, v53\n"
    "v_add_f32 v5, v5, v54\n"
    "v_exp_f32 v55, v55\n"
    "v_exp_f32 v16, v16\n"
    "v_add_f32 v5, v5, v55\n"
    "v_cvt_pk_bf16_f32 v51, v54, v55\n"
    "v_add_f32 v5, v5, v16\n"
    "s_waitcnt vmcnt(19)\n"
    "v_lshlrev_b32 v13, 16, v200\n"
    "v_mfma_f32_16x16x32_bf16 v[80:83], a[224:227], v[48:51], v[80:83]\n"
    "s_mov_b32 s67, 90112\n"
    "s_add_u32 m0, s57, 40960\n"
    "s_add_u32 s59, s58, 73728\n"
    "buffer_load_dwordx4 v[176:179], v233, s[44:47], s67 offen\n"
    "s_mov_b32 s68, 94208\n"
    "buffer_load_dwordx4 v[180:183], v233, s[44:47], s68 offen\n"
    "v_exp_f32 v17, v17\n"
    "v_exp_f32 v18, v18\n"
    "v_add_f32 v5, v5, v17\n"
    "v_cvt_pk_bf16_f32 v16, v16, v17\n"
    "v_add_f32 v5, v5, v18\n"
    "v_exp_f32 v19, v19\n"
    "v_exp_f32 v60, v60\n"
    "v_add_f32 v5, v5, v19\n"
    "v_cvt_pk_bf16_f32 v17, v18, v19\n"
    "v_add_f32 v6, v6, v60\n"
    "v_exp_f32 v61, v61\n"
    "v_exp_f32 v62, v62\n"
    "v_add_f32 v6, v6, v61\n"
    "v_cvt_pk_bf16_f32 v58, v60, v61\n"
    "v_add_f32 v6, v6, v62\n"
    "v_exp_f32 v63, v63\n"
    "v_exp_f32 v24, v24\n"
    "v_add_f32 v6, v6, v63\n"
    "v_cvt_pk_bf16_f32 v59, v62, v63\n"
    "v_add_f32 v6, v6, v24\n"
    "v_and_b32 v14, 0xffff0000, v200\n"
    "v_mfma_f32_16x16x32_bf16 v[112:115], a[224:227], v[56:59], 0\n"
    "v_exp_f32 v25, v25\n"
    "v_mfma_f32_16x16x32_bf16 v[84:87], a[228:231], v[48:51], v[84:87]\n"
    "buffer_load_dwordx4 v232, s[48:51], s59 offen lds\n"
    "v_add_f32 v6, v6, v25\n"
    "v_mfma_f32_16x16x32_bf16 v[116:119], a[228:231], v[56:59], 0\n"
    "v_cvt_pk_bf16_f32 v24, v24, v25\n"
    "v_mul_f32 v13, s66, v13\n"
    "v_mfma_f32_16x16x32_bf16 v[88:91], a[232:235], v[48:51], v[88:91]\n"
    "v_exp_f32 v26, v26\n"
    "v_mfma_f32_16x16x32_bf16 v[120:123], a[232:235], v[56:59], 0\n"
    "buffer_load_dwordx4 v232, s[48:51], s59 offen offset:2048 lds\n"
    "v_add_f32 v6, v6, v26\n"
    "v_mfma_f32_16x16x32_bf16 v[92:95], a[236:239], v[48:51], v[92:95]\n"
    "v_exp_f32 v27, v27\n"
    "v_mfma_f32_16x16x32_bf16 v[124:127], a[236:239], v[56:59], 0\n"
    "v_add_f32 v6, v6, v27\n"
    "v_cvt_pk_bf16_f32 v25, v26, v27\n"
    "v_mfma_f32_16x16x32_bf16 v[96:99], a[240:243], v[48:51], v[96:99]\n"
    "v_mul_f32 v14, s66, v14\n"
    "v_cvt_pk_bf16_f32 v200, v13, v14\n"
    "v_mfma_f32_16x16x32_bf16 v[128:131], a[240:243], v[56:59], 0\n"
    "v_lshlrev_b32 v15, 16, v201\n"
    "v_and_b32 v9, 0xffff0000, v201\n"
    "v_mfma_f32_16x16x32_bf16 v[100:103], a[244:247], v[48:51], v[100:103]\n"
    "v_mul_f32 v15, s66, v15\n"
    "v_mul_f32 v9, s66, v9\n"
    "v_mfma_f32_16x16x32_bf16 v[132:135], a[244:247], v[56:59], 0\n"
    "v_cvt_pk_bf16_f32 v201, v15, v9\n"
    "v_lshlrev_b32 v13, 16, v202\n"
    "v_mfma_f32_16x16x32_bf16 v[104:107], a[248:251], v[48:51], v[104:107]\n"
    "v_and_b32 v14, 0xffff0000, v202\n"
    "v_mul_f32 v13, s66, v13\n"
    "v_mfma_f32_16x16x32_bf16 v[136:139], a[248:251], v[56:59], 0\n"
    "v_mul_f32 v14, s66, v14\n"
    "v_cvt_pk_bf16_f32 v202, v13, v14\n"
    "v_mfma_f32_16x16x32_bf16 v[108:111], a[252:255], v[48:51], v[108:111]\n"
    "v_lshlrev_b32 v15, 16, v203\n"
    "v_and_b32 v9, 0xffff0000, v203\n"
    "v_mfma_f32_16x16x32_bf16 v[140:143], a[252:255], v[56:59], 0\n"
    "s_waitcnt lgkmcnt(7)\n"
    "s_mov_b32 s61, 49152\n"
    "v_mul_f32 v15, s66, v15\n"
    "v_mul_f32 v9, s66, v9\n"
    "v_mfma_f32_16x16x32_bf16 v[48:51], a[96:99], a[0:3], v[240:243]\n"
    "buffer_load_dwordx4 a[48:51], v4, s[40:43], s61 offen offset:0\n"
    "v_cvt_pk_bf16_f32 v203, v15, v9\n"
    "v_mfma_f32_16x16x32_bf16 v[56:59], a[96:99], a[16:19], v[244:247]\n"
    "s_waitcnt lgkmcnt(6)\n"
    "ds_write_b128 v234, v[200:203] offset:57344\n"
    "ds_read_b128 a[96:99], v1 offset:46080\n"
    "v_mfma_f32_16x16x32_bf16 v[48:51], a[100:103], a[4:7], v[48:51]\n"
    "s_waitcnt vmcnt(23)\n"
    "buffer_load_dwordx4 a[52:55], v4, s[40:43], s61 offen offset:64\n"
    "v_lshlrev_b32 v13, 16, v204\n"
    "v_mfma_f32_16x16x32_bf16 v[56:59], a[100:103], a[20:23], v[56:59]\n"
    "s_waitcnt lgkmcnt(7)\n"
    "ds_read_b128 a[100:103], v1 offset:46592\n"
    "v_and_b32 v14, 0xffff0000, v204\n"
    "v_mfma_f32_16x16x32_bf16 v[48:51], a[80:83], a[8:11], v[48:51]\n"
    "v_mul_f32 v13, s66, v13\n"
    "v_mul_f32 v14, s66, v14\n"
    "v_mfma_f32_16x16x32_bf16 v[56:59], a[80:83], a[24:27], v[56:59]\n"
    "s_waitcnt lgkmcnt(7)\n"
    "v_cvt_pk_bf16_f32 v204, v13, v14\n"
    "v_lshlrev_b32 v15, 16, v205\n"
    "v_mfma_f32_16x16x32_bf16 v[48:51], a[84:87], a[12:15], v[48:51]\n"
    "v_and_b32 v9, 0xffff0000, v205\n"
    "v_mul_f32 v15, s66, v15\n"
    "v_mfma_f32_16x16x32_bf16 v[56:59], a[84:87], a[28:31], v[56:59]\n"
    "s_waitcnt lgkmcnt(5)\n"
    "buffer_load_dwordx4 a[56:59], v4, s[40:43], s61 offen offset:128\n"
    "v_mul_f32 v9, s66, v9\n"
    "v_mfma_f32_16x16x32_bf16 v[52:55], a[88:91], a[0:3], v[240:243]\n"
    "v_cvt_pk_bf16_f32 v205, v15, v9\n"
    "v_lshlrev_b32 v13, 16, v206\n"
    "v_mfma_f32_16x16x32_bf16 v[60:63], a[88:91], a[16:19], v[244:247]\n"
    "s_waitcnt lgkmcnt(4)\n"
    "v_and_b32 v14, 0xffff0000, v206\n"
    "v_mul_f32 v13, s66, v13\n"
    "v_mfma_f32_16x16x32_bf16 v[52:55], a[92:95], a[4:7], v[52:55]\n"
    "v_mul_f32 v14, s66, v14\n"
    "v_cvt_pk_bf16_f32 v206, v13, v14\n"
    "v_mfma_f32_16x16x32_bf16 v[60:63], a[92:95], a[20:23], v[60:63]\n"
    "v_lshlrev_b32 v15, 16, v207\n"
    "v_and_b32 v9, 0xffff0000, v207\n"
    "v_mul_f32 v15, s66, v15\n"
    "v_mul_f32 v9, s66, v9\n"
    "v_cvt_pk_bf16_f32 v207, v15, v9\n"
    "v_exp_f32 v20, v20\n"
    "ds_write_b128 v234, v[204:207] offset:61440\n"
    "s_waitcnt lgkmcnt(4)\n"
    "s_waitcnt vmcnt(23)\n"
    "s_waitcnt lgkmcnt(0)\n"
    "s_waitcnt vmcnt(15)\n"
    "s_barrier\n"
    "buffer_load_dwordx4 a[60:63], v4, s[40:43], s61 offen offset:192\n"
    "ds_read_b128 a[80:83], v1 offset:49152\n"
    "ds_read_b128 a[84:87], v1 offset:49664\n"
    "ds_read_b128 a[88:91], v1 offset:50176\n"
    "ds_read_b128 a[92:95], v1 offset:50688\n"
    "v_add_f32 v5, v5, v20\n"
    "v_exp_f32 v21, v21\n"
    "v_exp_f32 v22, v22\n"
    "v_mfma_f32_16x16x32_bf16 v[52:55], a[96:99], a[8:11], v[52:55]\n"
    "s_mov_b32 s61, 81920\n"
    "v_add_f32 v5, v5, v21\n"
    "v_cvt_pk_bf16_f32 v18, v20, v21\n"
    "v_mfma_f32_16x16x32_bf16 v[60:63], a[96:99], a[24:27], v[60:63]\n"
    "ds_read_b128 a[96:99], v1 offset:53248\n"
    "v_add_f32 v5, v5, v22\n"
    "v_mfma_f32_16x16x32_bf16 v[52:55], a[100:103], a[12:15], v[52:55]\n"
    "v_exp_f32 v23, v23\n"
    "v_mfma_f32_16x16x32_bf16 v[60:63], a[100:103], a[28:31], v[60:63]\n"
    "ds_read_b128 a[100:103], v1 offset:53760\n"
    "v_add_f32 v5, v5, v23\n"
    "v_cvt_pk_bf16_f32 v19, v22, v23\n"
    "v_exp_f32 v48, v48\n"
    "s_nop 0\n"
    "v_mfma_f32_16x16x32_bf16 v[80:83], a[104:107], v[16:19], v[80:83]\n"
    "v_add_f32 v5, v5, v48\n"
    "v_exp_f32 v49, v49\n"
    "v_exp_f32 v50, v50\n"
    "v_add_f32 v5, v5, v49\n"
    "v_cvt_pk_bf16_f32 v48, v48, v49\n"
    "v_add_f32 v5, v5, v50\n"
    "v_exp_f32 v51, v51\n"
    "v_exp_f32 v28, v28\n"
    "v_add_f32 v5, v5, v51\n"
    "v_cvt_pk_bf16_f32 v49, v50, v51\n"
    "v_add_f32 v6, v6, v28\n"
    "v_exp_f32 v29, v29\n"
    "v_exp_f32 v30, v30\n"
    "v_add_f32 v6, v6, v29\n"
    "v_cvt_pk_bf16_f32 v26, v28, v29\n"
    "v_add_f32 v6, v6, v30\n"
    "v_exp_f32 v31, v31\n"
    "v_exp_f32 v56, v56\n"
    "v_add_f32 v6, v6, v31\n"
    "v_cvt_pk_bf16_f32 v27, v30, v31\n"
    "v_add_f32 v6, v6, v56\n"
    "v_lshlrev_b32 v13, 16, v208\n"
    "v_mfma_f32_16x16x32_bf16 v[112:115], a[104:107], v[24:27], v[112:115]\n"
    "s_add_u32 m0, s57, 49152\n"
    "v_exp_f32 v57, v57\n"
    "v_mfma_f32_16x16x32_bf16 v[84:87], a[108:111], v[16:19], v[84:87]\n"
    "s_add_u32 s59, s58, 81920\n"
    "ds_read_b64_tr_b16 a[104:105], v3 offset:1536\n"
    "ds_read_b64_tr_b16 a[106:107], v3 offset:5632\n"
    "v_mfma_f32_16x16x32_bf16 v[116:119], a[108:111], v[24:27], v[116:119]\n"
    "ds_read_b64_tr_b16 a[108:109], v3 offset:1544\n"
    "ds_read_b64_tr_b16 a[110:111], v3 offset:5640\n"
    "v_mfma_f32_16x16x32_bf16 v[88:91], a[112:115], v[16:19], v[88:91]\n"
    "buffer_load_dwordx4 v232, s[48:51], s59 offen lds\n"
    "buffer_load_dwordx4 v232, s[48:51], s59 offen offset:2048 lds\n"
    "v_mfma_f32_16x16x32_bf16 v[120:123], a[112:115], v[24:27], v[120:123]\n"
    "ds_read_b64_tr_b16 a[112:113], v3 offset:8192\n"
    "ds_read_b64_tr_b16 a[114:115], v3 offset:12288\n"
    "v_mfma_f32_16x16x32_bf16 v[92:95], a[116:119], v[16:19], v[92:95]\n"
    "v_add_f32 v6, v6, v57\n"
    "v_cvt_pk_bf16_f32 v56, v56, v57\n"
    "v_mfma_f32_16x16x32_bf16 v[124:127], a[116:119], v[24:27], v[124:127]\n"
    "v_exp_f32 v58, v58\n"
    "v_mfma_f32_16x16x32_bf16 v[96:99], a[120:123], v[16:19], v[96:99]\n"
    "ds_read_b64_tr_b16 a[116:117], v3 offset:8200\n"
    "ds_read_b64_tr_b16 a[118:119], v3 offset:12296\n"
    "v_mfma_f32_16x16x32_bf16 v[128:131], a[120:123], v[24:27], v[128:131]\n"
    "ds_read_b64_tr_b16 a[120:121], v3 offset:8704\n"
    "ds_read_b64_tr_b16 a[122:123], v3 offset:12800\n"
    "v_mfma_f32_16x16x32_bf16 v[100:103], a[124:127], v[16:19], v[100:103]\n"
    "v_add_f32 v6, v6, v58\n"
    "v_and_b32 v14, 0xffff0000, v208\n"
    "v_mfma_f32_16x16x32_bf16 v[132:135], a[124:127], v[24:27], v[132:135]\n"
    "ds_read_b64_tr_b16 a[124:125], v3 offset:8712\n"
    "ds_read_b64_tr_b16 a[126:127], v3 offset:12808\n"
    "v_exp_f32 v59, v59\n"
    "v_add_u32 v234, 0x10000, v234\n"
    "v_add_f32 v6, v6, v59\n"
    "v_cvt_pk_bf16_f32 v57, v58, v59\n"
    "v_mul_f32 v13, s66, v13\n"
    "v_mul_f32 v14, s66, v14\n"
    "v_cvt_pk_bf16_f32 v208, v13, v14\n"
    "v_lshlrev_b32 v15, 16, v209\n"
    "v_and_b32 v9, 0xffff0000, v209\n"
    "v_mul_f32 v15, s66, v15\n"
    "v_mul_f32 v9, s66, v9\n"
    "v_cvt_pk_bf16_f32 v209, v15, v9\n"
    "v_lshlrev_b32 v13, 16, v210\n"
    "v_and_b32 v14, 0xffff0000, v210\n"
    "v_mul_f32 v13, s66, v13\n"
    "v_mul_f32 v14, s66, v14\n"
    "v_cvt_pk_bf16_f32 v210, v13, v14\n"
    "v_lshlrev_b32 v15, 16, v211\n"
    "v_and_b32 v9, 0xffff0000, v211\n"
    "v_mul_f32 v15, s66, v15\n"
    "v_mul_f32 v9, s66, v9\n"
    "v_cvt_pk_bf16_f32 v211, v15, v9\n"
    "s_waitcnt lgkmcnt(10)\n"
    "v_mfma_f32_16x16x32_bf16 v[104:107], a[104:107], v[16:19], v[104:107]\n"
    "ds_write_b128 v234, v[208:211] offset:0\n"
    "v_lshlrev_b32 v13, 16, v212\n"
    "v_mfma_f32_16x16x32_bf16 v[136:139], a[104:107], v[24:27], v[136:139]\n"
    "s_waitcnt lgkmcnt(9)\n"
    "ds_read_b64_tr_b16 a[104:105], v3 offset:9216\n"
    "ds_read_b64_tr_b16 a[106:107], v3 offset:13312\n"
    "v_mfma_f32_16x16x32_bf16 v[108:111], a[108:111], v[16:19], v[108:111]\n"
    "v_and_b32 v14, 0xffff0000, v212\n"
    "v_mul_f32 v13, s66, v13\n"
    "v_mfma_f32_16x16x32_bf16 v[140:143], a[108:111], v[24:27], v[140:143]\n"
    "ds_read_b64_tr_b16 a[108:109], v3 offset:9224\n"
    "ds_read_b64_tr_b16 a[110:111], v3 offset:13320\n"
    "v_mfma_f32_16x16x32_bf16 v[16:19], a[80:83], a[0:3], v[240:243]\n"
    "v_mul_f32 v14, s66, v14\n"
    "v_cvt_pk_bf16_f32 v212, v13, v14\n"
    "v_mfma_f32_16x16x32_bf16 v[24:27], a[80:83], a[16:19], v[244:247]\n"
    "v_lshlrev_b32 v15, 16, v213\n"
    "v_and_b32 v9, 0xffff0000, v213\n"
    "v_mfma_f32_16x16x32_bf16 v[32:35], a[80:83], a[32:35], 0\n"
    "ds_read_b128 a[80:83], v1 offset:54272\n"
    "v_mul_f32 v15, s66, v15\n"
    "v_mfma_f32_16x16x32_bf16 v[16:19], a[84:87], a[4:7], v[16:19]\n"
    "v_mul_f32 v9, s66, v9\n"
    "v_cvt_pk_bf16_f32 v213, v15, v9\n"
    "v_mfma_f32_16x16x32_bf16 v[24:27], a[84:87], a[20:23], v[24:27]\n"
    "v_lshlrev_b32 v13, 16, v214\n"
    "v_and_b32 v14, 0xffff0000, v214\n"
    "v_mfma_f32_16x16x32_bf16 v[32:35], a[84:87], a[36:39], v[32:35]\n"
    "ds_read_b128 a[84:87], v1 offset:54784\n"
    "v_mul_f32 v13, s66, v13\n"
    "v_mfma_f32_16x16x32_bf16 v[16:19], a[88:91], a[8:11], v[16:19]\n"
    "v_mul_f32 v14, s66, v14\n"
    "v_cvt_pk_bf16_f32 v214, v13, v14\n"
    "v_mfma_f32_16x16x32_bf16 v[24:27], a[88:91], a[24:27], v[24:27]\n"
    "v_lshlrev_b32 v15, 16, v215\n"
    "v_and_b32 v9, 0xffff0000, v215\n"
    "v_mfma_f32_16x16x32_bf16 v[32:35], a[88:91], a[40:43], v[32:35]\n"
    "ds_read_b128 a[88:91], v1 offset:57344\n"
    "v_mul_f32 v15, s66, v15\n"
    "v_mfma_f32_16x16x32_bf16 v[16:19], a[92:95], a[12:15], v[16:19]\n"
    "v_mul_f32 v9, s66, v9\n"
    "v_cvt_pk_bf16_f32 v215, v15, v9\n"
    "v_mfma_f32_16x16x32_bf16 v[24:27], a[92:95], a[28:31], v[24:27]\n"
    "s_waitcnt vmcnt(17)\n"
    "ds_write_b128 v234, v[212:215] offset:4096\n"
    "v_lshlrev_b32 v13, 16, v216\n"
    "v_mfma_f32_16x16x32_bf16 v[32:35], a[92:95], a[44:47], v[32:35]\n"
    "s_add_u32 m0, s57, 57344\n"
    "v_exp_f32 v52, v52\n"
    "v_mfma_f32_16x16x32_bf16 v[20:23], a[96:99], a[0:3], v[240:243]\n"
    "s_add_u32 s59, s58, 90112\n"
    "ds_read_b128 a[92:95], v1 offset:57856\n"
    "buffer_load_dwordx4 v232, s[48:51], s59 offen lds\n"
    "v_mfma_f32_16x16x32_bf16 v[28:31], a[96:99], a[16:19], v[244:247]\n"
    "buffer_load_dwordx4 v232, s[48:51], s59 offen offset:2048 lds\n"
    "v_add_f32 v5, v5, v52\n"
    "v_mfma_f32_16x16x32_bf16 v[36:39], a[96:99], a[32:35], 0\n"
    "ds_read_b128 a[96:99], v1 offset:58368\n"
    "v_max3_f32 v10, v32, v33, v34\n"
    "v_mfma_f32_16x16x32_bf16 v[20:23], a[100:103], a[4:7], v[20:23]\n"
    "v_exp_f32 v53, v53\n"
    "v_mfma_f32_16x16x32_bf16 v[28:31], a[100:103], a[20:23], v[28:31]\n"
    "v_add_f32 v5, v5, v53\n"
    "v_cvt_pk_bf16_f32 v50, v52, v53\n"
    "v_mfma_f32_16x16x32_bf16 v[36:39], a[100:103], a[36:39], v[36:39]\n"
    "s_waitcnt lgkmcnt(5)\n"
    "v_exp_f32 v54, v54\n"
    "v_mfma_f32_16x16x32_bf16 v[20:23], a[80:83], a[8:11], v[20:23]\n"
    "ds_read_b128 a[100:103], v1 offset:58880\n"
    "v_add_f32 v5, v5, v54\n"
    "v_mfma_f32_16x16x32_bf16 v[28:31], a[80:83], a[24:27], v[28:31]\n"
    "v_exp_f32 v55, v55\n"
    "v_mfma_f32_16x16x32_bf16 v[36:39], a[80:83], a[40:43], v[36:39]\n"
    "s_waitcnt lgkmcnt(5)\n"
    "ds_read_b128 a[80:83], v1 offset:61440\n"
    "v_add_f32 v5, v5, v55\n"
    "v_mfma_f32_16x16x32_bf16 v[20:23], a[84:87], a[12:15], v[20:23]\n"
    "v_cvt_pk_bf16_f32 v51, v54, v55\n"
    "v_and_b32 v14, 0xffff0000, v216\n"
    "v_mfma_f32_16x16x32_bf16 v[28:31], a[84:87], a[28:31], v[28:31]\n"
    "v_exp_f32 v16, v16\n"
    "v_mfma_f32_16x16x32_bf16 v[36:39], a[84:87], a[44:47], v[36:39]\n"
    "ds_read_b128 a[84:87], v1 offset:61952\n"
    "v_add_f32 v5, v5, v16\n"
    "v_mfma_f32_16x16x32_bf16 v[80:83], a[112:115], v[48:51], v[80:83]\n"
    "v_exp_f32 v17, v17\n"
    "v_exp_f32 v18, v18\n"
    "v_add_f32 v5, v5, v17\n"
    "v_cvt_pk_bf16_f32 v16, v16, v17\n"
    "v_add_f32 v5, v5, v18\n"
    "v_exp_f32 v19, v19\n"
    "v_max3_f32 v11, v36, v37, v38\n"
    "v_max3_f32 v10, v10, v35, v39\n"
    "v_max_f32 v10, v10, v11\n"
    "v_mov_b32 v11, v10\n"
    "v_add_f32 v5, v5, v19\n"
    "v_cvt_pk_bf16_f32 v17, v18, v19\n"
    "v_permlane32_swap_b32 v10, v11\n"
    "v_exp_f32 v60, v60\n"
    "v_max_f32 v10, v10, v11\n"
    "v_mov_b32 v11, v10\n"
    "v_add_f32 v6, v6, v60\n"
    "v_exp_f32 v61, v61\n"
    "v_permlane16_swap_b32 v10, v11\n"
    "v_add_f32 v6, v6, v61\n"
    "v_min_f32 v248, -v10, -v11\n"
    "v_min_f32 v249, -v10, -v11\n"
    "v_min_f32 v250, -v10, -v11\n"
    "v_min_f32 v251, -v10, -v11\n"
    "v_add_f32 v32, v32, v248\n"
    "v_add_f32 v33, v33, v248\n"
    "v_add_f32 v34, v34, v248\n"
    "v_add_f32 v35, v35, v248\n"
    "v_add_f32 v36, v36, v248\n"
    "v_add_f32 v37, v37, v248\n"
    "v_add_f32 v38, v38, v248\n"
    "v_add_f32 v39, v39, v248\n"
    "v_cvt_pk_bf16_f32 v58, v60, v61\n"
    "v_exp_f32 v62, v62\n"
    "v_exp_f32 v63, v63\n"
    "v_add_f32 v6, v6, v62\n"
    "v_add_f32 v6, v6, v63\n"
    "v_cvt_pk_bf16_f32 v59, v62, v63\n"
    "v_exp_f32 v24, v24\n"
    "s_nop 0\n"
    "v_mfma_f32_16x16x32_bf16 v[112:115], a[112:115], v[56:59], v[112:115]\n"
    "ds_read_b64_tr_b16 a[112:113], v3 offset:9728\n"
    "ds_read_b64_tr_b16 a[114:115], v3 offset:13824\n"
    "v_mfma_f32_16x16x32_bf16 v[84:87], a[116:119], v[48:51], v[84:87]\n"
    "v_add_f32 v6, v6, v24\n"
    "v_mul_f32 v13, s66, v13\n"
    "v_mfma_f32_16x16x32_bf16 v[116:119], a[116:119], v[56:59], v[116:119]\n"
    "v_exp_f32 v25, v25\n"
    "v_mfma_f32_16x16x32_bf16 v[88:91], a[120:123], v[48:51], v[88:91]\n"
    "ds_read_b64_tr_b16 a[116:117], v3 offset:9736\n"
    "ds_read_b64_tr_b16 a[118:119], v3 offset:13832\n"
    "v_mfma_f32_16x16x32_bf16 v[120:123], a[120:123], v[56:59], v[120:123]\n"
    "ds_read_b64_tr_b16 a[120:121], v3 offset:16384\n"
    "ds_read_b64_tr_b16 a[122:123], v3 offset:20480\n"
    "v_mfma_f32_16x16x32_bf16 v[92:95], a[124:127], v[48:51], v[92:95]\n"
    "v_add_f32 v6, v6, v25\n"
    "v_cvt_pk_bf16_f32 v24, v24, v25\n"
    "v_mfma_f32_16x16x32_bf16 v[124:127], a[124:127], v[56:59], v[124:127]\n"
    "v_exp_f32 v26, v26\n"
    "v_mfma_f32_16x16x32_bf16 v[96:99], a[104:107], v[48:51], v[96:99]\n"
    "ds_read_b64_tr_b16 a[124:125], v3 offset:16392\n"
    "ds_read_b64_tr_b16 a[126:127], v3 offset:20488\n"
    "v_mfma_f32_16x16x32_bf16 v[128:131], a[104:107], v[56:59], v[128:131]\n"
    "ds_read_b64_tr_b16 a[104:105], v3 offset:16896\n"
    "ds_read_b64_tr_b16 a[106:107], v3 offset:20992\n"
    "v_mfma_f32_16x16x32_bf16 v[100:103], a[108:111], v[48:51], v[100:103]\n"
    "v_add_f32 v6, v6, v26\n"
    "v_mul_f32 v14, s66, v14\n"
    "v_mfma_f32_16x16x32_bf16 v[132:135], a[108:111], v[56:59], v[132:135]\n"
    "ds_read_b64_tr_b16 a[108:109], v3 offset:16904\n"
    "ds_read_b64_tr_b16 a[110:111], v3 offset:21000\n"
    "v_exp_f32 v27, v27\n"
    "v_exp_f32 v32, v32\n"
    "v_add_f32 v6, v6, v27\n"
    "v_cvt_pk_bf16_f32 v25, v26, v27\n"
    "v_exp_f32 v33, v33\n"
    "v_exp_f32 v34, v34\n"
    "v_add_f32 v7, v32, v33\n"
    "v_cvt_pk_bf16_f32 v32, v32, v33\n"
    "v_add_f32 v7, v7, v34\n"
    "v_exp_f32 v35, v35\n"
    "v_exp_f32 v20, v20\n"
    "v_add_f32 v7, v7, v35\n"
    "v_cvt_pk_bf16_f32 v33, v34, v35\n"
    "s_waitcnt lgkmcnt(10)\n"
    "v_mfma_f32_16x16x32_bf16 v[104:107], a[112:115], v[48:51], v[104:107]\n"
    "v_add_f32 v5, v5, v20\n"
    "v_cvt_pk_bf16_f32 v216, v13, v14\n"
    "v_mfma_f32_16x16x32_bf16 v[136:139], a[112:115], v[56:59], v[136:139]\n"
    "s_waitcnt lgkmcnt(8)\n"
    "ds_read_b64_tr_b16 a[112:113], v3 offset:17408\n"
    "ds_read_b64_tr_b16 a[114:115], v3 offset:21504\n"
    "v_exp_f32 v21, v21\n"
    "v_exp_f32 v22, v22\n"
    "v_mfma_f32_16x16x32_bf16 v[108:111], a[116:119], v[48:51], v[108:111]\n"
    "v_add_f32 v5, v5, v21\n"
    "v_cvt_pk_bf16_f32 v18, v20, v21\n"
    "v_mfma_f32_16x16x32_bf16 v[140:143], a[116:119], v[56:59], v[140:143]\n"
    "ds_read_b64_tr_b16 a[116:117], v3 offset:17416\n"
    "ds_read_b64_tr_b16 a[118:119], v3 offset:21512\n"
    "v_mfma_f32_16x16x32_bf16 v[48:51], a[88:91], a[0:3], v[240:243]\n"
    "v_add_f32 v5, v5, v22\n"
    "v_lshlrev_b32 v15, 16, v217\n"
    "v_mfma_f32_16x16x32_bf16 v[56:59], a[88:91], a[16:19], v[244:247]\n"
    "v_exp_f32 v23, v23\n"
    "v_mfma_f32_16x16x32_bf16 v[64:67], a[88:91], a[32:35], v[248:251]\n"
    "ds_read_b128 a[88:91], v1 offset:62464\n"
    "v_add_f32 v5, v5, v23\n"
    "v_mfma_f32_16x16x32_bf16 v[48:51], a[92:95], a[4:7], v[48:51]\n"
    "v_cvt_pk_bf16_f32 v19, v22, v23\n"
    "v_and_b32 v9, 0xffff0000, v217\n"
    "v_mfma_f32_16x16x32_bf16 v[56:59], a[92:95], a[20:23], v[56:59]\n"
    "v_mul_f32 v15, s66, v15\n"
    "v_mul_f32 v9, s66, v9\n"
    "v_mfma_f32_16x16x32_bf16 v[64:67], a[92:95], a[36:39], v[64:67]\n"
    "ds_read_b128 a[92:95], v1 offset:62976\n"
    "v_cvt_pk_bf16_f32 v217, v15, v9\n"
    "v_mfma_f32_16x16x32_bf16 v[48:51], a[96:99], a[8:11], v[48:51]\n"
    "v_lshlrev_b32 v13, 16, v218\n"
    "v_and_b32 v14, 0xffff0000, v218\n"
    "v_mfma_f32_16x16x32_bf16 v[56:59], a[96:99], a[24:27], v[56:59]\n"
    "v_mul_f32 v13, s66, v13\n"
    "v_mul_f32 v14, s66, v14\n"
    "v_mfma_f32_16x16x32_bf16 v[64:67], a[96:99], a[40:43], v[64:67]\n"
    "v_cvt_pk_bf16_f32 v218, v13, v14\n"
    "v_lshlrev_b32 v15, 16, v219\n"
    "v_mfma_f32_16x16x32_bf16 v[48:51], a[100:103], a[12:15], v[48:51]\n"
    "v_and_b32 v9, 0xffff0000, v219\n"
    "v_mul_f32 v15, s66, v15\n"
    "v_mfma_f32_16x16x32_bf16 v[56:59], a[100:103], a[28:31], v[56:59]\n"
    "v_mul_f32 v9, s66, v9\n"
    "v_cvt_pk_bf16_f32 v219, v15, v9\n"
    "v_mfma_f32_16x16x32_bf16 v[64:67], a[100:103], a[44:47], v[64:67]\n"
    "s_waitcnt vmcnt(18)\n"
    "ds_write_b128 v234, v[216:219] offset:8192\n"
    "v_lshlrev_b32 v13, 16, v220\n"
    "v_mfma_f32_16x16x32_bf16 v[52:55], a[80:83], a[0:3], v[240:243]\n"
    "v_and_b32 v14, 0xffff0000, v220\n"
    "v_mul_f32 v13, s66, v13\n"
    "v_mfma_f32_16x16x32_bf16 v[60:63], a[80:83], a[16:19], v[244:247]\n"
    "v_mul_f32 v14, s66, v14\n"
    "v_cvt_pk_bf16_f32 v220, v13, v14\n"
    "v_mfma_f32_16x16x32_bf16 v[68:71], a[80:83], a[32:35], v[248:251]\n"
    "v_lshlrev_b32 v15, 16, v221\n"
    "v_and_b32 v9, 0xffff0000, v221\n"
    "v_mfma_f32_16x16x32_bf16 v[52:55], a[84:87], a[4:7], v[52:55]\n"
    "v_mul_f32 v15, s66, v15\n"
    "v_mul_f32 v9, s66, v9\n"
    "v_mfma_f32_16x16x32_bf16 v[60:63], a[84:87], a[20:23], v[60:63]\n"
    "v_cvt_pk_bf16_f32 v221, v15, v9\n"
    "v_lshlrev_b32 v13, 16, v222\n"
    "v_mfma_f32_16x16x32_bf16 v[68:71], a[84:87], a[36:39], v[68:71]\n"
    "s_waitcnt lgkmcnt(2)\n"
    "v_and_b32 v14, 0xffff0000, v222\n"
    "v_mul_f32 v13, s66, v13\n"
    "v_mfma_f32_16x16x32_bf16 v[52:55], a[88:91], a[8:11], v[52:55]\n"
    "v_mul_f32 v14, s66, v14\n"
    "v_cvt_pk_bf16_f32 v222, v13, v14\n"
    "v_mfma_f32_16x16x32_bf16 v[60:63], a[88:91], a[24:27], v[60:63]\n"
    "v_lshlrev_b32 v15, 16, v223\n"
    "v_and_b32 v9, 0xffff0000, v223\n"
    "v_mfma_f32_16x16x32_bf16 v[68:71], a[88:91], a[40:43], v[68:71]\n"
    "s_waitcnt lgkmcnt(1)\n"
    "v_mul_f32 v15, s66, v15\n"
    "v_mul_f32 v9, s66, v9\n"
    "v_mfma_f32_16x16x32_bf16 v[52:55], a[92:95], a[12:15], v[52:55]\n"
    "s_waitcnt vmcnt(15)\n"
    "v_cvt_pk_bf16_f32 v223, v15, v9\n"
    "v_lshlrev_b32 v13, 16, v224\n"
    "v_mfma_f32_16x16x32_bf16 v[60:63], a[92:95], a[28:31], v[60:63]\n"
    "s_waitcnt vmcnt(12)\n"
    "ds_write_b128 v234, v[220:223] offset:12288\n"
    "s_waitcnt lgkmcnt(0)\n"
    "s_barrier\n"
    "v_mfma_f32_16x16x32_bf16 v[68:71], a[92:95], a[44:47], v[68:71]\n"
    "v_exp_f32 v28, v28\n"
    "v_mfma_f32_16x16x32_bf16 v[80:83], a[120:123], v[16:19], v[80:83]\n"
    "ds_read_b128 a[96:99], v2 offset:0\n"
    "ds_read_b128 a[100:103], v2 offset:512\n"
    "ds_read_b128 a[80:83], v2 offset:1024\n"
    "ds_read_b128 a[84:87], v2 offset:1536\n"
    "ds_read_b128 a[88:91], v2 offset:4096\n"
    "ds_read_b128 a[92:95], v2 offset:4608\n"
    "v_add_f32 v6, v6, v28\n"
    "v_exp_f32 v29, v29\n"
    "v_exp_f32 v30, v30\n"
    "v_add_f32 v6, v6, v29\n"
    "v_cvt_pk_bf16_f32 v26, v28, v29\n"
    "v_add_f32 v6, v6, v30\n"
    "v_exp_f32 v31, v31\n"
    "v_exp_f32 v36, v36\n"
    "v_add_f32 v6, v6, v31\n"
    "v_cvt_pk_bf16_f32 v27, v30, v31\n"
    "v_add_f32 v7, v7, v36\n"
    "v_and_b32 v14, 0xffff0000, v224\n"
    "v_mfma_f32_16x16x32_bf16 v[112:115], a[120:123], v[24:27], v[112:115]\n"
    "v_exp_f32 v37, v37\n"
    "v_exp_f32 v38, v38\n"
    "v_add_f32 v7, v7, v37\n"
    "v_cvt_pk_bf16_f32 v34, v36, v37\n"
    "v_add_f32 v7, v7, v38\n"
    "v_exp_f32 v39, v39\n"
    "v_exp_f32 v48, v48\n"
    "v_add_f32 v7, v7, v39\n"
    "v_cvt_pk_bf16_f32 v35, v38, v39\n"
    "v_add_f32 v5, v5, v48\n"
    "v_mul_f32 v13, s66, v13\n"
    "v_mfma_f32_16x16x32_bf16 v[144:147], a[120:123], v[32:35], 0\n"
    "v_exp_f32 v49, v49\n"
    "v_mfma_f32_16x16x32_bf16 v[84:87], a[124:127], v[16:19], v[84:87]\n"
    "ds_read_b64_tr_b16 a[120:121], v3 offset:17920\n"
    "ds_read_b64_tr_b16 a[122:123], v3 offset:22016\n"
    "v_mfma_f32_16x16x32_bf16 v[116:119], a[124:127], v[24:27], v[116:119]\n"
    "v_add_f32 v5, v5, v49\n"
    "v_cvt_pk_bf16_f32 v48, v48, v49\n"
    "v_mfma_f32_16x16x32_bf16 v[148:151], a[124:127], v[32:35], 0\n"
    "v_exp_f32 v50, v50\n"
    "v_mfma_f32_16x16x32_bf16 v[88:91], a[104:107], v[16:19], v[88:91]\n"
    "ds_read_b64_tr_b16 a[124:125], v3 offset:17928\n"
    "ds_read_b64_tr_b16 a[126:127], v3 offset:22024\n"
    "v_mfma_f32_16x16x32_bf16 v[120:123], a[104:107], v[24:27], v[120:123]\n"
    "v_add_f32 v5, v5, v50\n"
    "v_mul_f32 v14, s66, v14\n"
    "v_mfma_f32_16x16x32_bf16 v[152:155], a[104:107], v[32:35], 0\n"
    "v_exp_f32 v51, v51\n"
    "v_mfma_f32_16x16x32_bf16 v[92:95], a[108:111], v[16:19], v[92:95]\n"
    "ds_read_b64_tr_b16 a[104:105], v3 offset:24576\n"
    "ds_read_b64_tr_b16 a[106:107], v3 offset:28672\n"
    "v_mfma_f32_16x16x32_bf16 v[124:127], a[108:111], v[24:27], v[124:127]\n"
    "v_add_f32 v5, v5, v51\n"
    "v_cvt_pk_bf16_f32 v49, v50, v51\n"
    "v_mfma_f32_16x16x32_bf16 v[156:159], a[108:111], v[32:35], 0\n"
    "v_exp_f32 v56, v56\n"
    "v_mfma_f32_16x16x32_bf16 v[96:99], a[112:115], v[16:19], v[96:99]\n"
    "ds_read_b64_tr_b16 a[108:109], v3 offset:24584\n"
    "ds_read_b64_tr_b16 a[110:111], v3 offset:28680\n"
    "v_mfma_f32_16x16x32_bf16 v[128:131], a[112:115], v[24:27], v[128:131]\n"
    "v_add_f32 v6, v6, v56\n"
    "v_cvt_pk_bf16_f32 v224, v13, v14\n"
    "v_mfma_f32_16x16x32_bf16 v[160:163], a[112:115], v[32:35], 0\n"
    "v_exp_f32 v57, v57\n"
    "v_mfma_f32_16x16x32_bf16 v[100:103], a[116:119], v[16:19], v[100:103]\n"
    "ds_read_b64_tr_b16 a[112:113], v3 offset:25088\n"
    "ds_read_b64_tr_b16 a[114:115], v3 offset:29184\n"
    "v_mfma_f32_16x16x32_bf16 v[132:135], a[116:119], v[24:27], v[132:135]\n"
    "v_add_f32 v6, v6, v57\n"
    "v_cvt_pk_bf16_f32 v56, v56, v57\n"
    "v_mfma_f32_16x16x32_bf16 v[164:167], a[116:119], v[32:35], 0\n"
    "s_waitcnt lgkmcnt(8)\n"
    "ds_read_b64_tr_b16 a[116:117], v3 offset:25096\n"
    "ds_read_b64_tr_b16 a[118:119], v3 offset:29192\n"
    "v_exp_f32 v58, v58\n"
    "v_lshlrev_b32 v15, 16, v225\n"
    "v_mfma_f32_16x16x32_bf16 v[104:107], a[120:123], v[16:19], v[104:107]\n"
    "v_add_f32 v6, v6, v58\n"
    "v_and_b32 v9, 0xffff0000, v225\n"
    "v_mfma_f32_16x16x32_bf16 v[136:139], a[120:123], v[24:27], v[136:139]\n"
    "v_exp_f32 v59, v59\n"
    "v_mfma_f32_16x16x32_bf16 v[168:171], a[120:123], v[32:35], 0\n"
    "s_waitcnt lgkmcnt(8)\n"
    "ds_read_b64_tr_b16 a[120:121], v3 offset:25600\n"
    "ds_read_b64_tr_b16 a[122:123], v3 offset:29696\n"
    "v_mfma_f32_16x16x32_bf16 v[108:111], a[124:127], v[16:19], v[108:111]\n"
    "v_add_f32 v6, v6, v59\n"
    "v_cvt_pk_bf16_f32 v57, v58, v59\n"
    "v_mfma_f32_16x16x32_bf16 v[140:143], a[124:127], v[24:27], v[140:143]\n"
    "v_exp_f32 v64, v64\n"
    "v_mfma_f32_16x16x32_bf16 v[172:175], a[124:127], v[32:35], 0\n"
    "s_mov_b32 s60, 65536\n"
    "ds_read_b64_tr_b16 a[124:125], v3 offset:25608\n"
    "ds_read_b64_tr_b16 a[126:127], v3 offset:29704\n"
    "v_mfma_f32_16x16x32_bf16 v[16:19], a[96:99], a[0:3], v[240:243]\n"
    "buffer_load_dwordx4 a[64:67], v4, s[40:43], s60 offen offset:0\n"
    "buffer_load_dwordx4 a[68:71], v4, s[40:43], s60 offen offset:64\n"
    "v_mfma_f32_16x16x32_bf16 v[24:27], a[96:99], a[16:19], v[244:247]\n"
    "v_add_f32 v7, v7, v64\n"
    "v_mul_f32 v15, s66, v15\n"
    "v_mfma_f32_16x16x32_bf16 v[32:35], a[96:99], a[32:35], v[248:251]\n"
    "v_exp_f32 v65, v65\n"
    "v_mfma_f32_16x16x32_bf16 v[16:19], a[100:103], a[4:7], v[16:19]\n"
    "ds_read_b128 a[96:99], v2 offset:5120\n"
    "v_add_f32 v7, v7, v65\n"
    "v_mfma_f32_16x16x32_bf16 v[24:27], a[100:103], a[20:23], v[24:27]\n"
    "v_cvt_pk_bf16_f32 v64, v64, v65\n"
    "v_mul_f32 v9, s66, v9\n"
    "v_mfma_f32_16x16x32_bf16 v[32:35], a[100:103], a[36:39], v[32:35]\n"
    "v_exp_f32 v66, v66\n"
    "v_mfma_f32_16x16x32_bf16 v[16:19], a[80:83], a[8:11], v[16:19]\n"
    "ds_read_b128 a[100:103], v2 offset:5632\n"
    "buffer_load_dwordx4 a[72:75], v4, s[40:43], s60 offen offset:128\n"
    "v_mfma_f32_16x16x32_bf16 v[24:27], a[80:83], a[24:27], v[24:27]\n"
    "v_add_f32 v7, v7, v66\n"
    "v_cvt_pk_bf16_f32 v225, v15, v9\n"
    "v_mfma_f32_16x16x32_bf16 v[32:35], a[80:83], a[40:43], v[32:35]\n"
    "v_exp_f32 v67, v67\n"
    "v_mfma_f32_16x16x32_bf16 v[16:19], a[84:87], a[12:15], v[16:19]\n"
    "ds_read_b128 a[80:83], v2 offset:8192\n"
    "v_add_f32 v7, v7, v67\n"
    "v_mfma_f32_16x16x32_bf16 v[24:27], a[84:87], a[28:31], v[24:27]\n"
    "v_cvt_pk_bf16_f32 v65, v66, v67\n"
    "v_lshlrev_b32 v13, 16, v226\n"
    "v_mfma_f32_16x16x32_bf16 v[32:35], a[84:87], a[44:47], v[32:35]\n"
    "v_exp_f32 v52, v52\n"
    "v_mfma_f32_16x16x32_bf16 v[20:23], a[88:91], a[0:3], v[240:243]\n"
    "buffer_load_dwordx4 a[76:79], v4, s[40:43], s60 offen offset:192\n"
    "ds_read_b128 a[84:87], v2 offset:8704\n"
    "v_mfma_f32_16x16x32_bf16 v[28:31], a[88:91], a[16:19], v[244:247]\n"
    "s_mov_b32 s60, 98304\n"
    "v_add_f32 v5, v5, v52\n"
    "v_and_b32 v14, 0xffff0000, v226\n"
    "v_mfma_f32_16x16x32_bf16 v[36:39], a[88:91], a[32:35], v[248:251]\n"
    "v_exp_f32 v53, v53\n"
    "v_mfma_f32_16x16x32_bf16 v[20:23], a[92:95], a[4:7], v[20:23]\n"
    "ds_read_b128 a[88:91], v2 offset:9216\n"
    "v_add_f32 v5, v5, v53\n"
    "v_mfma_f32_16x16x32_bf16 v[28:31], a[92:95], a[20:23], v[28:31]\n"
    "v_cvt_pk_bf16_f32 v50, v52, v53\n"
    "v_mul_f32 v13, s66, v13\n"
    "v_mfma_f32_16x16x32_bf16 v[36:39], a[92:95], a[36:39], v[36:39]\n"
    "s_waitcnt lgkmcnt(4)\n"
    "ds_read_b128 a[92:95], v2 offset:9728\n"
    "v_exp_f32 v54, v54\n"
    "v_mul_f32 v14, s66, v14\n"
    "v_mfma_f32_16x16x32_bf16 v[20:23], a[96:99], a[8:11], v[20:23]\n"
    "v_add_f32 v5, v5, v54\n"
    "v_cvt_pk_bf16_f32 v226, v13, v14\n"
    "v_mfma_f32_16x16x32_bf16 v[28:31], a[96:99], a[24:27], v[28:31]\n"
    "v_exp_f32 v55, v55\n"
    "v_mfma_f32_16x16x32_bf16 v[36:39], a[96:99], a[40:43], v[36:39]\n"
    "s_waitcnt lgkmcnt(4)\n"
    "ds_read_b128 a[96:99], v2 offset:12288\n"
    "v_add_f32 v5, v5, v55\n"
    "v_mfma_f32_16x16x32_bf16 v[20:23], a[100:103], a[12:15], v[20:23]\n"
    "v_cvt_pk_bf16_f32 v51, v54, v55\n"
    "v_lshlrev_b32 v15, 16, v227\n"
    "v_mfma_f32_16x16x32_bf16 v[28:31], a[100:103], a[28:31], v[28:31]\n"
    "v_and_b32 v9, 0xffff0000, v227\n"
    "v_mul_f32 v15, s66, v15\n"
    "v_mfma_f32_16x16x32_bf16 v[36:39], a[100:103], a[44:47], v[36:39]\n"
    "ds_read_b128 a[100:103], v2 offset:12800\n"
    "v_mul_f32 v9, s66, v9\n"
    "v_mfma_f32_16x16x32_bf16 v[80:83], a[104:107], v[48:51], v[80:83]\n"
    "v_cvt_pk_bf16_f32 v227, v15, v9\n"
    "v_lshlrev_b32 v13, 16, v228\n"
    "v_and_b32 v14, 0xffff0000, v228\n"
    "v_mul_f32 v13, s66, v13\n"
    "v_mul_f32 v14, s66, v14\n"
    "v_cvt_pk_bf16_f32 v228, v13, v14\n"
    "v_lshlrev_b32 v15, 16, v229\n"
    "v_and_b32 v9, 0xffff0000, v229\n"
    "v_mul_f32 v15, s66, v15\n"
    "v_mul_f32 v9, s66, v9\n"
    "v_cvt_pk_bf16_f32 v229, v15, v9\n"
    "v_lshlrev_b32 v13, 16, v230\n"
    "v_and_b32 v14, 0xffff0000, v230\n"
    "v_mul_f32 v13, s66, v13\n"
    "v_mul_f32 v14, s66, v14\n"
    "v_cvt_pk_bf16_f32 v230, v13, v14\n"
    "v_lshlrev_b32 v15, 16, v231\n"
    "v_and_b32 v9, 0xffff0000, v231\n"
    "v_mul_f32 v15, s66, v15\n"
    "v_mul_f32 v9, s66, v9\n"
    "v_cvt_pk_bf16_f32 v231, v15, v9\n"
    "ds_write_b128 v234, v[224:227] offset:16384\n"
    "ds_write_b128 v234, v[228:231] offset:20480\n"
    "v_exp_f32 v60, v60\n"
    "v_exp_f32 v61, v61\n"
    "v_add_f32 v6, v6, v60\n"
    "v_add_f32 v6, v6, v61\n"
    "v_cvt_pk_bf16_f32 v58, v60, v61\n"
    "v_exp_f32 v62, v62\n"
    "v_exp_f32 v63, v63\n"
    "v_add_f32 v6, v6, v62\n"
    "v_add_f32 v6, v6, v63\n"
    "v_cvt_pk_bf16_f32 v59, v62, v63\n"
    "v_exp_f32 v68, v68\n"
    "s_nop 0\n"
    "v_mfma_f32_16x16x32_bf16 v[112:115], a[104:107], v[56:59], v[112:115]\n"
    "v_add_f32 v7, v7, v68\n"
    "v_exp_f32 v69, v69\n"
    "v_exp_f32 v70, v70\n"
    "v_add_f32 v7, v7, v69\n"
    "v_cvt_pk_bf16_f32 v66, v68, v69\n"
    "v_add_f32 v7, v7, v70\n"
    "v_exp_f32 v71, v71\n"
    "v_exp_f32 v16, v16\n"
    "v_add_f32 v7, v7, v71\n"
    "v_cvt_pk_bf16_f32 v67, v70, v71\n"
    "v_add_f32 v5, v5, v16\n"
    "s_waitcnt vmcnt(15)\n"
    "v_lshlrev_b32 v13, 16, v176\n"
    "v_mfma_f32_16x16x32_bf16 v[144:147], a[104:107], v[64:67], v[144:147]\n"
    "v_exp_f32 v17, v17\n"
    "v_mfma_f32_16x16x32_bf16 v[84:87], a[108:111], v[48:51], v[84:87]\n"
    "ds_read_b64_tr_b16 a[104:105], v3 offset:26112\n"
    "ds_read_b64_tr_b16 a[106:107], v3 offset:30208\n"
    "v_mfma_f32_16x16x32_bf16 v[116:119], a[108:111], v[56:59], v[116:119]\n"
    "v_add_f32 v5, v5, v17\n"
    "v_cvt_pk_bf16_f32 v16, v16, v17\n"
    "v_mfma_f32_16x16x32_bf16 v[148:151], a[108:111], v[64:67], v[148:151]\n"
    "v_exp_f32 v18, v18\n"
    "v_mfma_f32_16x16x32_bf16 v[88:91], a[112:115], v[48:51], v[88:91]\n"
    "ds_read_b64_tr_b16 a[108:109], v3 offset:26120\n"
    "ds_read_b64_tr_b16 a[110:111], v3 offset:30216\n"
    "v_mfma_f32_16x16x32_bf16 v[120:123], a[112:115], v[56:59], v[120:123]\n"
    "v_add_f32 v5, v5, v18\n"
    "v_and_b32 v14, 0xffff0000, v176\n"
    "v_mfma_f32_16x16x32_bf16 v[152:155], a[112:115], v[64:67], v[152:155]\n"
    "v_exp_f32 v19, v19\n"
    "v_mfma_f32_16x16x32_bf16 v[92:95], a[116:119], v[48:51], v[92:95]\n"
    "ds_read_b64_tr_b16 a[112:113], v3 offset:32768\n"
    "ds_read_b64_tr_b16 a[114:115], v3 offset:36864\n"
    "v_mfma_f32_16x16x32_bf16 v[124:127], a[116:119], v[56:59], v[124:127]\n"
    "v_add_f32 v5, v5, v19\n"
    "v_cvt_pk_bf16_f32 v17, v18, v19\n"
    "v_mfma_f32_16x16x32_bf16 v[156:159], a[116:119], v[64:67], v[156:159]\n"
    "v_exp_f32 v24, v24\n"
    "v_mfma_f32_16x16x32_bf16 v[96:99], a[120:123], v[48:51], v[96:99]\n"
    "ds_read_b64_tr_b16 a[116:117], v3 offset:32776\n"
    "ds_read_b64_tr_b16 a[118:119], v3 offset:36872\n"
    "v_mfma_f32_16x16x32_bf16 v[128:131], a[120:123], v[56:59], v[128:131]\n"
    "v_add_f32 v6, v6, v24\n"
    "v_mul_f32 v13, s66, v13\n"
    "v_mfma_f32_16x16x32_bf16 v[160:163], a[120:123], v[64:67], v[160:163]\n"
    "v_exp_f32 v25, v25\n"
    "v_mfma_f32_16x16x32_bf16 v[100:103], a[124:127], v[48:51], v[100:103]\n"
    "ds_read_b64_tr_b16 a[120:121], v3 offset:33280\n"
    "ds_read_b64_tr_b16 a[122:123], v3 offset:37376\n"
    "v_mfma_f32_16x16x32_bf16 v[132:135], a[124:127], v[56:59], v[132:135]\n"
    "v_add_f32 v6, v6, v25\n"
    "v_cvt_pk_bf16_f32 v24, v24, v25\n"
    "v_mfma_f32_16x16x32_bf16 v[164:167], a[124:127], v[64:67], v[164:167]\n"
    "s_waitcnt lgkmcnt(8)\n"
    "ds_read_b64_tr_b16 a[124:125], v3 offset:33288\n"
    "ds_read_b64_tr_b16 a[126:127], v3 offset:37384\n"
    "v_exp_f32 v26, v26\n"
    "v_mul_f32 v14, s66, v14\n"
    "v_mfma_f32_16x16x32_bf16 v[104:107], a[104:107], v[48:51], v[104:107]\n"
    "v_add_f32 v6, v6, v26\n"
    "v_cvt_pk_bf16_f32 v176, v13, v14\n"
    "v_mfma_f32_16x16x32_bf16 v[136:139], a[104:107], v[56:59], v[136:139]\n"
    "v_exp_f32 v27, v27\n"
    "v_mfma_f32_16x16x32_bf16 v[168:171], a[104:107], v[64:67], v[168:171]\n"
    "s_waitcnt lgkmcnt(8)\n"
    "ds_read_b64_tr_b16 a[104:105], v3 offset:33792\n"
    "ds_read_b64_tr_b16 a[106:107], v3 offset:37888\n"
    "v_mfma_f32_16x16x32_bf16 v[108:111], a[108:111], v[48:51], v[108:111]\n"
    "v_add_f32 v6, v6, v27\n"
    "v_cvt_pk_bf16_f32 v25, v26, v27\n"
    "v_mfma_f32_16x16x32_bf16 v[140:143], a[108:111], v[56:59], v[140:143]\n"
    "v_exp_f32 v32, v32\n"
    "v_mfma_f32_16x16x32_bf16 v[172:175], a[108:111], v[64:67], v[172:175]\n"
    "ds_read_b64_tr_b16 a[108:109], v3 offset:33800\n"
    "ds_read_b64_tr_b16 a[110:111], v3 offset:37896\n"
    "v_mfma_f32_16x16x32_bf16 v[48:51], a[80:83], a[0:3], v[240:243]\n"
    "v_add_f32 v7, v7, v32\n"
    "v_lshlrev_b32 v15, 16, v177\n"
    "v_mfma_f32_16x16x32_bf16 v[56:59], a[80:83], a[16:19], v[244:247]\n"
    "v_exp_f32 v33, v33\n"
    "v_mfma_f32_16x16x32_bf16 v[64:67], a[80:83], a[32:35], v[248:251]\n"
    "s_waitcnt vmcnt(11)\n"
    "v_add_f32 v7, v7, v33\n"
    "v_cvt_pk_bf16_f32 v32, v32, v33\n"
    "v_mfma_f32_16x16x32_bf16 v[72:75], a[80:83], a[48:51], 0\n"
    "v_exp_f32 v34, v34\n"
    "v_mfma_f32_16x16x32_bf16 v[48:51], a[84:87], a[4:7], v[48:51]\n"
    "ds_read_b128 a[80:83], v2 offset:13312\n"
    "v_add_f32 v7, v7, v34\n"
    "v_mfma_f32_16x16x32_bf16 v[56:59], a[84:87], a[20:23], v[56:59]\n"
    "v_exp_f32 v35, v35\n"
    "v_mfma_f32_16x16x32_bf16 v[64:67], a[84:87], a[36:39], v[64:67]\n"
    "s_waitcnt vmcnt(10)\n"
    "v_add_f32 v7, v7, v35\n"
    "v_cvt_pk_bf16_f32 v33, v34, v35\n"
    "v_mfma_f32_16x16x32_bf16 v[72:75], a[84:87], a[52:55], v[72:75]\n"
    "v_exp_f32 v20, v20\n"
    "v_mfma_f32_16x16x32_bf16 v[48:51], a[88:91], a[8:11], v[48:51]\n"
    "ds_read_b128 a[84:87], v2 offset:13824\n"
    "v_add_f32 v5, v5, v20\n"
    "v_mfma_f32_16x16x32_bf16 v[56:59], a[88:91], a[24:27], v[56:59]\n"
    "v_exp_f32 v21, v21\n"
    "v_mfma_f32_16x16x32_bf16 v[64:67], a[88:91], a[40:43], v[64:67]\n"
    "s_waitcnt vmcnt(9)\n"
    "v_add_f32 v5, v5, v21\n"
    "v_cvt_pk_bf16_f32 v18, v20, v21\n"
    "v_mfma_f32_16x16x32_bf16 v[72:75], a[88:91], a[56:59], v[72:75]\n"
    "v_exp_f32 v22, v22\n"
    "v_mfma_f32_16x16x32_bf16 v[48:51], a[92:95], a[12:15], v[48:51]\n"
    "v_add_f32 v5, v5, v22\n"
    "v_and_b32 v9, 0xffff0000, v177\n"
    "v_mfma_f32_16x16x32_bf16 v[56:59], a[92:95], a[28:31], v[56:59]\n"
    "v_exp_f32 v23, v23\n"
    "v_mfma_f32_16x16x32_bf16 v[64:67], a[92:95], a[44:47], v[64:67]\n"
    "s_waitcnt vmcnt(8)\n"
    "v_add_f32 v5, v5, v23\n"
    "v_cvt_pk_bf16_f32 v19, v22, v23\n"
    "v_mfma_f32_16x16x32_bf16 v[72:75], a[92:95], a[60:63], v[72:75]\n"
    "v_mul_f32 v15, s66, v15\n"
    "v_mul_f32 v9, s66, v9\n"
    "v_mfma_f32_16x16x32_bf16 v[52:55], a[96:99], a[0:3], v[240:243]\n"
    "v_cvt_pk_bf16_f32 v177, v15, v9\n"
    "v_lshlrev_b32 v13, 16, v178\n"
    "v_mfma_f32_16x16x32_bf16 v[60:63], a[96:99], a[16:19], v[244:247]\n"
    "v_and_b32 v14, 0xffff0000, v178\n"
    "v_mul_f32 v13, s66, v13\n"
    "v_mfma_f32_16x16x32_bf16 v[68:71], a[96:99], a[32:35], v[248:251]\n"
    "v_mul_f32 v14, s66, v14\n"
    "v_cvt_pk_bf16_f32 v178, v13, v14\n"
    "v_mfma_f32_16x16x32_bf16 v[76:79], a[96:99], a[48:51], 0\n"
    "v_lshlrev_b32 v15, 16, v179\n"
    "v_and_b32 v9, 0xffff0000, v179\n"
    "v_mfma_f32_16x16x32_bf16 v[52:55], a[100:103], a[4:7], v[52:55]\n"
    "v_mul_f32 v15, s66, v15\n"
    "v_mul_f32 v9, s66, v9\n"
    "v_mfma_f32_16x16x32_bf16 v[60:63], a[100:103], a[20:23], v[60:63]\n"
    "v_cvt_pk_bf16_f32 v179, v15, v9\n"
    "v_lshlrev_b32 v13, 16, v180\n"
    "v_mfma_f32_16x16x32_bf16 v[68:71], a[100:103], a[36:39], v[68:71]\n"
    "ds_write_b128 v234, v[176:179] offset:24576\n"
    "v_and_b32 v14, 0xffff0000, v180\n"
    "v_mfma_f32_16x16x32_bf16 v[76:79], a[100:103], a[52:55], v[76:79]\n"
    "s_waitcnt lgkmcnt(2)\n"
    "v_mul_f32 v13, s66, v13\n"
    "v_mul_f32 v14, s66, v14\n"
    "v_mfma_f32_16x16x32_bf16 v[52:55], a[80:83], a[8:11], v[52:55]\n"
    "v_cvt_pk_bf16_f32 v180, v13, v14\n"
    "v_lshlrev_b32 v15, 16, v181\n"
    "v_mfma_f32_16x16x32_bf16 v[60:63], a[80:83], a[24:27], v[60:63]\n"
    "v_and_b32 v9, 0xffff0000, v181\n"
    "v_mul_f32 v15, s66, v15\n"
    "v_mfma_f32_16x16x32_bf16 v[68:71], a[80:83], a[40:43], v[68:71]\n"
    "v_mul_f32 v9, s66, v9\n"
    "v_cvt_pk_bf16_f32 v181, v15, v9\n"
    "v_mfma_f32_16x16x32_bf16 v[76:79], a[80:83], a[56:59], v[76:79]\n"
    "s_waitcnt lgkmcnt(1)\n"
    "v_lshlrev_b32 v13, 16, v182\n"
    "v_and_b32 v14, 0xffff0000, v182\n"
    "v_mfma_f32_16x16x32_bf16 v[52:55], a[84:87], a[12:15], v[52:55]\n"
    "v_mul_f32 v13, s66, v13\n"
    "v_mul_f32 v14, s66, v14\n"
    "v_mfma_f32_16x16x32_bf16 v[60:63], a[84:87], a[28:31], v[60:63]\n"
    "v_cvt_pk_bf16_f32 v182, v13, v14\n"
    "v_lshlrev_b32 v15, 16, v183\n"
    "v_mfma_f32_16x16x32_bf16 v[68:71], a[84:87], a[44:47], v[68:71]\n"
    "v_and_b32 v9, 0xffff0000, v183\n"
    "v_mul_f32 v15, s66, v15\n"
    "v_mfma_f32_16x16x32_bf16 v[76:79], a[84:87], a[60:63], v[76:79]\n"
    "v_mul_f32 v9, s66, v9\n"
    "v_cvt_pk_bf16_f32 v183, v15, v9\n"
    "v_mfma_f32_16x16x32_bf16 v[80:83], a[112:115], v[16:19], v[80:83]\n"
    "s_waitcnt vmcnt(6)\n"
    "ds_write_b128 v234, v[180:183] offset:28672\n"
    "s_waitcnt lgkmcnt(0)\n"
    "s_barrier\n"
    "ds_read_b128 a[88:91], v2 offset:16384\n"
    "ds_read_b128 a[92:95], v2 offset:16896\n"
    "ds_read_b128 a[96:99], v2 offset:17408\n"
    "ds_read_b128 a[100:103], v2 offset:17920\n"
    "ds_read_b128 a[80:83], v2 offset:20480\n"
    "ds_read_b128 a[84:87], v2 offset:20992\n"
    "v_max3_f32 v10, v72, v73, v74\n"
    "v_max3_f32 v11, v76, v77, v78\n"
    "v_max3_f32 v10, v10, v75, v79\n"
    "v_max_f32 v10, v10, v11\n"
    "v_mov_b32 v11, v10\n"
    "v_exp_f32 v28, v28\n"
    "s_nop 0\n"
    "v_permlane32_swap_b32 v10, v11\n"
    "v_add_f32 v6, v6, v28\n"
    "v_max_f32 v10, v10, v11\n"
    "v_mov_b32 v11, v10\n"
    "v_exp_f32 v29, v29\n"
    "s_nop 0\n"
    "v_permlane16_swap_b32 v10, v11\n"
    "v_add_f32 v6, v6, v29\n"
    "v_min_f32 v252, -v10, -v11\n"
    "v_min_f32 v253, -v10, -v11\n"
    "v_min_f32 v254, -v10, -v11\n"
    "v_min_f32 v255, -v10, -v11\n"
    "v_add_f32 v72, v72, v252\n"
    "v_add_f32 v73, v73, v252\n"
    "v_add_f32 v74, v74, v252\n"
    "v_add_f32 v75, v75, v252\n"
    "v_add_f32 v76, v76, v252\n"
    "v_add_f32 v77, v77, v252\n"
    "v_add_f32 v78, v78, v252\n"
    "v_add_f32 v79, v79, v252\n"
    "v_cvt_pk_bf16_f32 v26, v28, v29\n"
    "v_exp_f32 v30, v30\n"
    "v_exp_f32 v31, v31\n"
    "v_add_f32 v6, v6, v30\n"
    "v_add_f32 v6, v6, v31\n"
    "v_cvt_pk_bf16_f32 v27, v30, v31\n"
    "v_exp_f32 v36, v36\n"
    "s_nop 0\n"
    "v_mfma_f32_16x16x32_bf16 v[112:115], a[112:115], v[24:27], v[112:115]\n"
    "v_add_f32 v7, v7, v36\n"
    "v_exp_f32 v37, v37\n"
    "v_exp_f32 v38, v38\n"
    "v_add_f32 v7, v7, v37\n"
    "v_cvt_pk_bf16_f32 v34, v36, v37\n"
    "v_add_f32 v7, v7, v38\n"
    "v_exp_f32 v39, v39\n"
    "v_exp_f32 v48, v48\n"
    "v_add_f32 v7, v7, v39\n"
    "v_cvt_pk_bf16_f32 v35, v38, v39\n"
    "v_add_f32 v5, v5, v48\n"
    "v_mbcnt_lo_u32_b32 v14, -1, 0\n"
    "v_mfma_f32_16x16x32_bf16 v[144:147], a[112:115], v[32:35], v[144:147]\n"
    "v_exp_f32 v49, v49\n"
    "v_mfma_f32_16x16x32_bf16 v[84:87], a[116:119], v[16:19], v[84:87]\n"
    "ds_read_b64_tr_b16 a[112:113], v3 offset:34304\n"
    "ds_read_b64_tr_b16 a[114:115], v3 offset:38400\n"
    "v_mfma_f32_16x16x32_bf16 v[116:119], a[116:119], v[24:27], v[116:119]\n"
    "v_add_f32 v5, v5, v49\n"
    "v_cvt_pk_bf16_f32 v48, v48, v49\n"
    "v_mfma_f32_16x16x32_bf16 v[148:151], a[116:119], v[32:35], v[148:151]\n"
    "v_exp_f32 v50, v50\n"
    "v_mfma_f32_16x16x32_bf16 v[88:91], a[120:123], v[16:19], v[88:91]\n"
    "ds_read_b64_tr_b16 a[116:117], v3 offset:34312\n"
    "ds_read_b64_tr_b16 a[118:119], v3 offset:38408\n"
    "v_mfma_f32_16x16x32_bf16 v[120:123], a[120:123], v[24:27], v[120:123]\n"
    "v_add_f32 v5, v5, v50\n"
    "v_mbcnt_hi_u32_b32 v14, -1, v14\n"
    "v_mfma_f32_16x16x32_bf16 v[152:155], a[120:123], v[32:35], v[152:155]\n"
    "v_exp_f32 v51, v51\n"
    "v_mfma_f32_16x16x32_bf16 v[92:95], a[124:127], v[16:19], v[92:95]\n"
    "ds_read_b64_tr_b16 a[120:121], v3 offset:40960\n"
    "ds_read_b64_tr_b16 a[122:123], v3 offset:45056\n"
    "v_mfma_f32_16x16x32_bf16 v[124:127], a[124:127], v[24:27], v[124:127]\n"
    "v_add_f32 v5, v5, v51\n"
    "v_cvt_pk_bf16_f32 v49, v50, v51\n"
    "v_mfma_f32_16x16x32_bf16 v[156:159], a[124:127], v[32:35], v[156:159]\n"
    "v_exp_f32 v56, v56\n"
    "v_mfma_f32_16x16x32_bf16 v[96:99], a[104:107], v[16:19], v[96:99]\n"
    "ds_read_b64_tr_b16 a[124:125], v3 offset:40968\n"
    "ds_read_b64_tr_b16 a[126:127], v3 offset:45064\n"
    "v_mfma_f32_16x16x32_bf16 v[128:131], a[104:107], v[24:27], v[128:131]\n"
    "v_add_f32 v6, v6, v56\n"
    "v_lshlrev_b32 v14, 6, v14\n"
    "v_mfma_f32_16x16x32_bf16 v[160:163], a[104:107], v[32:35], v[160:163]\n"
    "v_exp_f32 v57, v57\n"
    "v_mfma_f32_16x16x32_bf16 v[100:103], a[108:111], v[16:19], v[100:103]\n"
    "ds_read_b64_tr_b16 a[104:105], v3 offset:41472\n"
    "ds_read_b64_tr_b16 a[106:107], v3 offset:45568\n"
    "v_mfma_f32_16x16x32_bf16 v[132:135], a[108:111], v[24:27], v[132:135]\n"
    "v_add_f32 v6, v6, v57\n"
    "v_cvt_pk_bf16_f32 v56, v56, v57\n"
    "v_mfma_f32_16x16x32_bf16 v[164:167], a[108:111], v[32:35], v[164:167]\n"
    "s_waitcnt lgkmcnt(8)\n"
    "ds_read_b64_tr_b16 a[108:109], v3 offset:41480\n"
    "ds_read_b64_tr_b16 a[110:111], v3 offset:45576\n"
    "v_exp_f32 v58, v58\n"
    "v_add_u32 v14, s69, v14\n"
    "v_mfma_f32_16x16x32_bf16 v[104:107], a[112:115], v[16:19], v[104:107]\n"
    "v_add_f32 v6, v6, v58\n"
    "v_mfma_f32_16x16x32_bf16 v[136:139], a[112:115], v[24:27], v[136:139]\n"
    "v_exp_f32 v59, v59\n"
    "v_mfma_f32_16x16x32_bf16 v[168:171], a[112:115], v[32:35], v[168:171]\n"
    "s_waitcnt lgkmcnt(8)\n"
    "ds_read_b64_tr_b16 a[112:113], v3 offset:41984\n"
    "ds_read_b64_tr_b16 a[114:115], v3 offset:46080\n"
    "v_mfma_f32_16x16x32_bf16 v[108:111], a[116:119], v[16:19], v[108:111]\n"
    "v_add_f32 v6, v6, v59\n"
    "v_cvt_pk_bf16_f32 v57, v58, v59\n"
    "v_mfma_f32_16x16x32_bf16 v[140:143], a[116:119], v[24:27], v[140:143]\n"
    "v_exp_f32 v64, v64\n"
    "v_mfma_f32_16x16x32_bf16 v[172:175], a[116:119], v[32:35], v[172:175]\n"
    "ds_read_b64_tr_b16 a[116:117], v3 offset:41992\n"
    "ds_read_b64_tr_b16 a[118:119], v3 offset:46088\n"
    "v_mfma_f32_16x16x32_bf16 v[16:19], a[88:91], a[0:3], v[240:243]\n"
    "v_add_f32 v7, v7, v64\n"
    "v_mfma_f32_16x16x32_bf16 v[24:27], a[88:91], a[16:19], v[244:247]\n"
    "v_exp_f32 v65, v65\n"
    "v_mfma_f32_16x16x32_bf16 v[32:35], a[88:91], a[32:35], v[248:251]\n"
    "v_add_f32 v7, v7, v65\n"
    "v_cvt_pk_bf16_f32 v64, v64, v65\n"
    "v_mfma_f32_16x16x32_bf16 v[40:43], a[88:91], a[48:51], v[252:255]\n"
    "v_exp_f32 v66, v66\n"
    "v_mfma_f32_16x16x32_bf16 v[16:19], a[92:95], a[4:7], v[16:19]\n"
    "ds_read_b128 a[88:91], v2 offset:21504\n"
    "v_add_f32 v7, v7, v66\n"
    "v_mfma_f32_16x16x32_bf16 v[24:27], a[92:95], a[20:23], v[24:27]\n"
    "v_exp_f32 v67, v67\n"
    "v_mfma_f32_16x16x32_bf16 v[32:35], a[92:95], a[36:39], v[32:35]\n"
    "v_add_f32 v7, v7, v67\n"
    "v_cvt_pk_bf16_f32 v65, v66, v67\n"
    "v_mfma_f32_16x16x32_bf16 v[40:43], a[92:95], a[52:55], v[40:43]\n"
    "v_exp_f32 v72, v72\n"
    "v_mfma_f32_16x16x32_bf16 v[16:19], a[96:99], a[8:11], v[16:19]\n"
    "v_exp_f32 v73, v73\n"
    "v_mfma_f32_16x16x32_bf16 v[24:27], a[96:99], a[24:27], v[24:27]\n"
    "ds_read_b128 a[92:95], v2 offset:22016\n"
    "v_add_f32 v8, v72, v73\n"
    "v_mfma_f32_16x16x32_bf16 v[32:35], a[96:99], a[40:43], v[32:35]\n"
    "v_cvt_pk_bf16_f32 v72, v72, v73\n"
    "v_mfma_f32_16x16x32_bf16 v[40:43], a[96:99], a[56:59], v[40:43]\n"
    "v_exp_f32 v74, v74\n"
    "v_mfma_f32_16x16x32_bf16 v[16:19], a[100:103], a[12:15], v[16:19]\n"
    "ds_read_b128 a[96:99], v2 offset:24576\n"
    "v_add_f32 v8, v8, v74\n"
    "v_mfma_f32_16x16x32_bf16 v[24:27], a[100:103], a[28:31], v[24:27]\n"
    "v_exp_f32 v75, v75\n"
    "v_mfma_f32_16x16x32_bf16 v[32:35], a[100:103], a[44:47], v[32:35]\n"
    "v_add_f32 v8, v8, v75\n"
    "v_cvt_pk_bf16_f32 v73, v74, v75\n"
    "v_mfma_f32_16x16x32_bf16 v[40:43], a[100:103], a[60:63], v[40:43]\n"
    "v_exp_f32 v52, v52\n"
    "v_mfma_f32_16x16x32_bf16 v[20:23], a[80:83], a[0:3], v[240:243]\n"
    "ds_read_b128 a[100:103], v2 offset:25088\n"
    "v_add_f32 v5, v5, v52\n"
    "v_mfma_f32_16x16x32_bf16 v[28:31], a[80:83], a[16:19], v[244:247]\n"
    "v_exp_f32 v53, v53\n"
    "v_mfma_f32_16x16x32_bf16 v[36:39], a[80:83], a[32:35], v[248:251]\n"
    "v_add_f32 v5, v5, v53\n"
    "v_cvt_pk_bf16_f32 v50, v52, v53\n"
    "v_mfma_f32_16x16x32_bf16 v[44:47], a[80:83], a[48:51], v[252:255]\n"
    "v_exp_f32 v54, v54\n"
    "v_mfma_f32_16x16x32_bf16 v[20:23], a[84:87], a[4:7], v[20:23]\n"
    "ds_read_b128 a[80:83], v2 offset:25600\n"
    "v_add_f32 v5, v5, v54\n"
    "v_mfma_f32_16x16x32_bf16 v[28:31], a[84:87], a[20:23], v[28:31]\n"
    "v_exp_f32 v55, v55\n"
    "v_mfma_f32_16x16x32_bf16 v[36:39], a[84:87], a[36:39], v[36:39]\n"
    "v_add_f32 v5, v5, v55\n"
    "v_cvt_pk_bf16_f32 v51, v54, v55\n"
    "v_mfma_f32_16x16x32_bf16 v[44:47], a[84:87], a[52:55], v[44:47]\n"
    "s_waitcnt lgkmcnt(4)\n"
    "v_exp_f32 v60, v60\n"
    "v_mfma_f32_16x16x32_bf16 v[20:23], a[88:91], a[8:11], v[20:23]\n"
    "ds_read_b128 a[84:87], v2 offset:26112\n"
    "v_add_f32 v6, v6, v60\n"
    "v_mfma_f32_16x16x32_bf16 v[28:31], a[88:91], a[24:27], v[28:31]\n"
    "v_exp_f32 v61, v61\n"
    "v_mfma_f32_16x16x32_bf16 v[36:39], a[88:91], a[40:43], v[36:39]\n"
    "v_add_f32 v6, v6, v61\n"
    "v_cvt_pk_bf16_f32 v58, v60, v61\n"
    "v_mfma_f32_16x16x32_bf16 v[44:47], a[88:91], a[56:59], v[44:47]\n"
    "s_waitcnt lgkmcnt(4)\n"
    "v_exp_f32 v62, v62\n"
    "v_mfma_f32_16x16x32_bf16 v[20:23], a[92:95], a[12:15], v[20:23]\n"
    "ds_read_b128 a[88:91], v2 offset:28672\n"
    "v_add_f32 v6, v6, v62\n"
    "v_mfma_f32_16x16x32_bf16 v[28:31], a[92:95], a[28:31], v[28:31]\n"
    "v_exp_f32 v63, v63\n"
    "v_mfma_f32_16x16x32_bf16 v[36:39], a[92:95], a[44:47], v[36:39]\n"
    "v_add_f32 v6, v6, v63\n"
    "v_cvt_pk_bf16_f32 v59, v62, v63\n"
    "v_mfma_f32_16x16x32_bf16 v[44:47], a[92:95], a[60:63], v[44:47]\n"
    "v_exp_f32 v68, v68\n"
    "v_mfma_f32_16x16x32_bf16 v[80:83], a[120:123], v[48:51], v[80:83]\n"
    "ds_read_b128 a[92:95], v2 offset:29184\n"
    "v_add_f32 v7, v7, v68\n"
    "v_mfma_f32_16x16x32_bf16 v[112:115], a[120:123], v[56:59], v[112:115]\n"
    "v_exp_f32 v69, v69\n"
    "v_exp_f32 v70, v70\n"
    "v_add_f32 v7, v7, v69\n"
    "v_cvt_pk_bf16_f32 v66, v68, v69\n"
    "v_add_f32 v7, v7, v70\n"
    "v_exp_f32 v71, v71\n"
    "v_exp_f32 v76, v76\n"
    "v_add_f32 v7, v7, v71\n"
    "v_cvt_pk_bf16_f32 v67, v70, v71\n"
    "v_add_f32 v8, v8, v76\n"
    "s_nop 0\n"
    "v_mfma_f32_16x16x32_bf16 v[144:147], a[120:123], v[64:67], v[144:147]\n"
    "v_exp_f32 v77, v77\n"
    "v_exp_f32 v78, v78\n"
    "v_add_f32 v8, v8, v77\n"
    "v_cvt_pk_bf16_f32 v74, v76, v77\n"
    "v_add_f32 v8, v8, v78\n"
    "v_exp_f32 v79, v79\n"
    "v_exp_f32 v16, v16\n"
    "v_add_f32 v8, v8, v79\n"
    "v_cvt_pk_bf16_f32 v75, v78, v79\n"
    "v_add_f32 v5, v5, v16\n"
    "s_nop 0\n"
    "v_mfma_f32_16x16x32_bf16 v[176:179], a[120:123], v[72:75], 0\n"
    "v_exp_f32 v17, v17\n"
    "v_mfma_f32_16x16x32_bf16 v[84:87], a[124:127], v[48:51], v[84:87]\n"
    "ds_read_b64_tr_b16 a[120:121], v3 offset:42496\n"
    "ds_read_b64_tr_b16 a[122:123], v3 offset:46592\n"
    "v_mfma_f32_16x16x32_bf16 v[116:119], a[124:127], v[56:59], v[116:119]\n"
    "v_add_f32 v5, v5, v17\n"
    "v_cvt_pk_bf16_f32 v16, v16, v17\n"
    "v_mfma_f32_16x16x32_bf16 v[148:151], a[124:127], v[64:67], v[148:151]\n"
    "v_exp_f32 v18, v18\n"
    "v_mfma_f32_16x16x32_bf16 v[180:183], a[124:127], v[72:75], 0\n"
    "ds_read_b64_tr_b16 a[124:125], v3 offset:42504\n"
    "ds_read_b64_tr_b16 a[126:127], v3 offset:46600\n"
    "v_mfma_f32_16x16x32_bf16 v[88:91], a[104:107], v[48:51], v[88:91]\n"
    "v_add_f32 v5, v5, v18\n"
    "v_mfma_f32_16x16x32_bf16 v[120:123], a[104:107], v[56:59], v[120:123]\n"
    "v_exp_f32 v19, v19\n"
    "v_mfma_f32_16x16x32_bf16 v[152:155], a[104:107], v[64:67], v[152:155]\n"
    "v_add_f32 v5, v5, v19\n"
    "v_cvt_pk_bf16_f32 v17, v18, v19\n"
    "v_mfma_f32_16x16x32_bf16 v[184:187], a[104:107], v[72:75], 0\n"
    "v_exp_f32 v24, v24\n"
    "v_mfma_f32_16x16x32_bf16 v[92:95], a[108:111], v[48:51], v[92:95]\n"
    "ds_read_b64_tr_b16 a[104:105], v3 offset:49152\n"
    "ds_read_b64_tr_b16 a[106:107], v3 offset:53248\n"
    "v_mfma_f32_16x16x32_bf16 v[124:127], a[108:111], v[56:59], v[124:127]\n"
    "v_add_f32 v6, v6, v24\n"
    "v_mfma_f32_16x16x32_bf16 v[156:159], a[108:111], v[64:67], v[156:159]\n"
    "v_exp_f32 v25, v25\n"
    "v_mfma_f32_16x16x32_bf16 v[188:191], a[108:111], v[72:75], 0\n"
    "ds_read_b64_tr_b16 a[108:109], v3 offset:49160\n"
    "ds_read_b64_tr_b16 a[110:111], v3 offset:53256\n"
    "v_mfma_f32_16x16x32_bf16 v[96:99], a[112:115], v[48:51], v[96:99]\n"
    "v_add_f32 v6, v6, v25\n"
    "v_cvt_pk_bf16_f32 v24, v24, v25\n"
    "v_mfma_f32_16x16x32_bf16 v[128:131], a[112:115], v[56:59], v[128:131]\n"
    "v_exp_f32 v26, v26\n"
    "v_mfma_f32_16x16x32_bf16 v[160:163], a[112:115], v[64:67], v[160:163]\n"
    "v_add_f32 v6, v6, v26\n"
    "v_mfma_f32_16x16x32_bf16 v[192:195], a[112:115], v[72:75], 0\n"
    "v_exp_f32 v27, v27\n"
    "v_mfma_f32_16x16x32_bf16 v[100:103], a[116:119], v[48:51], v[100:103]\n"
    "ds_read_b64_tr_b16 a[112:113], v3 offset:49664\n"
    "ds_read_b64_tr_b16 a[114:115], v3 offset:53760\n"
    "v_mfma_f32_16x16x32_bf16 v[132:135], a[116:119], v[56:59], v[132:135]\n"
    "v_add_f32 v6, v6, v27\n"
    "v_cvt_pk_bf16_f32 v25, v26, v27\n"
    "v_mfma_f32_16x16x32_bf16 v[164:167], a[116:119], v[64:67], v[164:167]\n"
    "v_exp_f32 v32, v32\n"
    "v_mfma_f32_16x16x32_bf16 v[196:199], a[116:119], v[72:75], 0\n"
    "s_waitcnt lgkmcnt(8)\n"
    "ds_read_b64_tr_b16 a[116:117], v3 offset:49672\n"
    "ds_read_b64_tr_b16 a[118:119], v3 offset:53768\n"
    "v_mfma_f32_16x16x32_bf16 v[104:107], a[120:123], v[48:51], v[104:107]\n"
    "v_add_f32 v7, v7, v32\n"
    "v_mfma_f32_16x16x32_bf16 v[136:139], a[120:123], v[56:59], v[136:139]\n"
    "v_exp_f32 v33, v33\n"
    "v_mfma_f32_16x16x32_bf16 v[168:171], a[120:123], v[64:67], v[168:171]\n"
    "v_add_f32 v7, v7, v33\n"
    "v_cvt_pk_bf16_f32 v32, v32, v33\n"
    "v_mfma_f32_16x16x32_bf16 v[200:203], a[120:123], v[72:75], 0\n"
    "s_waitcnt lgkmcnt(8)\n"
    "v_exp_f32 v34, v34\n"
    "v_mfma_f32_16x16x32_bf16 v[108:111], a[124:127], v[48:51], v[108:111]\n"
    "ds_read_b64_tr_b16 a[120:121], v3 offset:50176\n"
    "ds_read_b64_tr_b16 a[122:123], v3 offset:54272\n"
    "v_mfma_f32_16x16x32_bf16 v[140:143], a[124:127], v[56:59], v[140:143]\n"
    "v_add_f32 v7, v7, v34\n"
    "v_mfma_f32_16x16x32_bf16 v[172:175], a[124:127], v[64:67], v[172:175]\n"
    "v_exp_f32 v35, v35\n"
    "v_mfma_f32_16x16x32_bf16 v[204:207], a[124:127], v[72:75], 0\n"
    "ds_read_b64_tr_b16 a[124:125], v3 offset:50184\n"
    "ds_read_b64_tr_b16 a[126:127], v3 offset:54280\n"
    "v_mfma_f32_16x16x32_bf16 v[48:51], a[96:99], a[0:3], v[240:243]\n"
    "v_add_f32 v7, v7, v35\n"
    "v_cvt_pk_bf16_f32 v33, v34, v35\n"
    "v_mfma_f32_16x16x32_bf16 v[56:59], a[96:99], a[16:19], v[244:247]\n"
    "v_exp_f32 v40, v40\n"
    "v_mfma_f32_16x16x32_bf16 v[64:67], a[96:99], a[32:35], v[248:251]\n"
    "v_add_f32 v8, v8, v40\n"
    "v_mfma_f32_16x16x32_bf16 v[72:75], a[96:99], a[48:51], v[252:255]\n"
    "v_exp_f32 v41, v41\n"
    "v_mfma_f32_16x16x32_bf16 v[48:51], a[100:103], a[4:7], v[48:51]\n"
    "ds_read_b128 a[96:99], v2 offset:29696\n"
    "v_add_f32 v8, v8, v41\n"
    "v_mfma_f32_16x16x32_bf16 v[56:59], a[100:103], a[20:23], v[56:59]\n"
    "v_cvt_pk_bf16_f32 v40, v40, v41\n"
    "v_mfma_f32_16x16x32_bf16 v[64:67], a[100:103], a[36:39], v[64:67]\n"
    "v_exp_f32 v42, v42\n"
    "v_mfma_f32_16x16x32_bf16 v[72:75], a[100:103], a[52:55], v[72:75]\n"
    "ds_read_b128 a[100:103], v2 offset:30208\n"
    "v_add_f32 v8, v8, v42\n"
    "v_mfma_f32_16x16x32_bf16 v[48:51], a[80:83], a[8:11], v[48:51]\n"
    "v_exp_f32 v43, v43\n"
    "v_mfma_f32_16x16x32_bf16 v[56:59], a[80:83], a[24:27], v[56:59]\n"
    "v_add_f32 v8, v8, v43\n"
    "v_cvt_pk_bf16_f32 v41, v42, v43\n"
    "v_mfma_f32_16x16x32_bf16 v[64:67], a[80:83], a[40:43], v[64:67]\n"
    "v_exp_f32 v20, v20\n"
    "v_mfma_f32_16x16x32_bf16 v[72:75], a[80:83], a[56:59], v[72:75]\n"
    "ds_read_b128 a[80:83], v1 offset:0\n"
    "v_add_f32 v5, v5, v20\n"
    "v_mfma_f32_16x16x32_bf16 v[48:51], a[84:87], a[12:15], v[48:51]\n"
    "v_exp_f32 v21, v21\n"
    "v_mfma_f32_16x16x32_bf16 v[56:59], a[84:87], a[28:31], v[56:59]\n"
    "v_add_f32 v5, v5, v21\n"
    "v_cvt_pk_bf16_f32 v18, v20, v21\n"
    "v_mfma_f32_16x16x32_bf16 v[64:67], a[84:87], a[44:47], v[64:67]\n"
    "v_exp_f32 v22, v22\n"
    "v_mfma_f32_16x16x32_bf16 v[72:75], a[84:87], a[60:63], v[72:75]\n"
    "ds_read_b128 a[84:87], v1 offset:512\n"
    "v_add_f32 v5, v5, v22\n"
    "v_mfma_f32_16x16x32_bf16 v[52:55], a[88:91], a[0:3], v[240:243]\n"
    "v_exp_f32 v23, v23\n"
    "v_mfma_f32_16x16x32_bf16 v[60:63], a[88:91], a[16:19], v[244:247]\n"
    "v_add_f32 v5, v5, v23\n"
    "v_cvt_pk_bf16_f32 v19, v22, v23\n"
    "v_mfma_f32_16x16x32_bf16 v[68:71], a[88:91], a[32:35], v[248:251]\n"
    "v_exp_f32 v28, v28\n"
    "v_mfma_f32_16x16x32_bf16 v[76:79], a[88:91], a[48:51], v[252:255]\n"
    "ds_read_b128 a[88:91], v1 offset:1024\n"
    "v_add_f32 v6, v6, v28\n"
    "v_mfma_f32_16x16x32_bf16 v[52:55], a[92:95], a[4:7], v[52:55]\n"
    "v_exp_f32 v29, v29\n"
    "v_mfma_f32_16x16x32_bf16 v[60:63], a[92:95], a[20:23], v[60:63]\n"
    "v_add_f32 v6, v6, v29\n"
    "v_cvt_pk_bf16_f32 v26, v28, v29\n"
    "v_mfma_f32_16x16x32_bf16 v[68:71], a[92:95], a[36:39], v[68:71]\n"
    "v_exp_f32 v30, v30\n"
    "v_mfma_f32_16x16x32_bf16 v[76:79], a[92:95], a[52:55], v[76:79]\n"
    "s_waitcnt lgkmcnt(4)\n"
    "ds_read_b128 a[92:95], v1 offset:1536\n"
    "v_add_f32 v6, v6, v30\n"
    "v_mfma_f32_16x16x32_bf16 v[52:55], a[96:99], a[8:11], v[52:55]\n"
    "v_exp_f32 v31, v31\n"
    "v_mfma_f32_16x16x32_bf16 v[60:63], a[96:99], a[24:27], v[60:63]\n"
    "v_add_f32 v6, v6, v31\n"
    "v_cvt_pk_bf16_f32 v27, v30, v31\n"
    "v_mfma_f32_16x16x32_bf16 v[68:71], a[96:99], a[40:43], v[68:71]\n"
    "v_exp_f32 v36, v36\n"
    "v_mfma_f32_16x16x32_bf16 v[76:79], a[96:99], a[56:59], v[76:79]\n"
    "s_waitcnt lgkmcnt(4)\n"
    "ds_read_b128 a[96:99], v1 offset:4096\n"
    "v_add_f32 v7, v7, v36\n"
    "v_mfma_f32_16x16x32_bf16 v[52:55], a[100:103], a[12:15], v[52:55]\n"
    "v_exp_f32 v37, v37\n"
    "v_mfma_f32_16x16x32_bf16 v[60:63], a[100:103], a[28:31], v[60:63]\n"
    "buffer_load_dwordx4 a[0:3], v4, s[40:43], s61 offen offset:0\n"
    "buffer_load_dwordx4 a[4:7], v4, s[40:43], s61 offen offset:64\n"
    "v_mfma_f32_16x16x32_bf16 v[68:71], a[100:103], a[44:47], v[68:71]\n"
    "s_waitcnt vmcnt(6)\n"
    "s_barrier\n"
    "v_add_f32 v7, v7, v37\n"
    "v_cvt_pk_bf16_f32 v34, v36, v37\n"
    "v_mfma_f32_16x16x32_bf16 v[76:79], a[100:103], a[60:63], v[76:79]\n"
    "v_exp_f32 v38, v38\n"
    "v_mfma_f32_16x16x32_bf16 v[80:83], a[104:107], v[16:19], v[80:83]\n"
    "ds_read_b128 a[100:103], v1 offset:4608\n"
    "v_add_f32 v7, v7, v38\n"
    "v_mfma_f32_16x16x32_bf16 v[112:115], a[104:107], v[24:27], v[112:115]\n"
    "v_exp_f32 v39, v39\n"
    "v_exp_f32 v44, v44\n"
    "v_add_f32 v7, v7, v39\n"
    "v_cvt_pk_bf16_f32 v35, v38, v39\n"
    "v_add_f32 v8, v8, v44\n"
    "s_nop 0\n"
    "v_mfma_f32_16x16x32_bf16 v[144:147], a[104:107], v[32:35], v[144:147]\n"
    "buffer_load_dwordx4 a[8:11], v4, s[40:43], s61 offen offset:128\n"
    "v_exp_f32 v45, v45\n"
    "v_exp_f32 v46, v46\n"
    "v_add_f32 v8, v8, v45\n"
    "v_cvt_pk_bf16_f32 v42, v44, v45\n"
    "v_add_f32 v8, v8, v46\n"
    "v_exp_f32 v47, v47\n"
    "v_exp_f32 v48, v48\n"
    "v_add_f32 v8, v8, v47\n"
    "v_cvt_pk_bf16_f32 v43, v46, v47\n"
    "v_add_f32 v5, v5, v48\n"
    "s_nop 0\n"
    "v_mfma_f32_16x16x32_bf16 v[176:179], a[104:107], v[40:43], v[176:179]\n"
    "v_exp_f32 v49, v49\n"
    "v_mfma_f32_16x16x32_bf16 v[84:87], a[108:111], v[16:19], v[84:87]\n"
    "ds_read_b64_tr_b16 a[104:105], v3 offset:50688\n"
    "ds_read_b64_tr_b16 a[106:107], v3 offset:54784\n"
    "v_mfma_f32_16x16x32_bf16 v[116:119], a[108:111], v[24:27], v[116:119]\n"
    "buffer_load_dwordx4 a[12:15], v4, s[40:43], s61 offen offset:192\n"
    "v_add_f32 v5, v5, v49\n"
    "v_mfma_f32_16x16x32_bf16 v[148:151], a[108:111], v[32:35], v[148:151]\n"
    "s_mov_b32 s61, 114688\n"
    "v_cvt_pk_bf16_f32 v48, v48, v49\n"
    "v_mfma_f32_16x16x32_bf16 v[180:183], a[108:111], v[40:43], v[180:183]\n"
    "v_exp_f32 v50, v50\n"
    "v_mfma_f32_16x16x32_bf16 v[88:91], a[112:115], v[16:19], v[88:91]\n"
    "ds_read_b64_tr_b16 a[108:109], v3 offset:50696\n"
    "ds_read_b64_tr_b16 a[110:111], v3 offset:54792\n"
    "v_mfma_f32_16x16x32_bf16 v[120:123], a[112:115], v[24:27], v[120:123]\n"
    "v_add_f32 v5, v5, v50\n"
    "v_mfma_f32_16x16x32_bf16 v[152:155], a[112:115], v[32:35], v[152:155]\n"
    "v_exp_f32 v51, v51\n"
    "v_mfma_f32_16x16x32_bf16 v[184:187], a[112:115], v[40:43], v[184:187]\n"
    "ds_read_b64_tr_b16 a[112:113], v3 offset:57344\n"
    "ds_read_b64_tr_b16 a[114:115], v3 offset:61440\n"
    "v_mfma_f32_16x16x32_bf16 v[92:95], a[116:119], v[16:19], v[92:95]\n"
    "v_add_f32 v5, v5, v51\n"
    "v_cvt_pk_bf16_f32 v49, v50, v51\n"
    "v_mfma_f32_16x16x32_bf16 v[124:127], a[116:119], v[24:27], v[124:127]\n"
    "v_exp_f32 v56, v56\n"
    "v_mfma_f32_16x16x32_bf16 v[156:159], a[116:119], v[32:35], v[156:159]\n"
    "v_add_f32 v6, v6, v56\n"
    "v_mfma_f32_16x16x32_bf16 v[188:191], a[116:119], v[40:43], v[188:191]\n"
    "v_exp_f32 v57, v57\n"
    "v_mfma_f32_16x16x32_bf16 v[96:99], a[120:123], v[16:19], v[96:99]\n"
    "ds_read_b64_tr_b16 a[116:117], v3 offset:57352\n"
    "ds_read_b64_tr_b16 a[118:119], v3 offset:61448\n"
    "v_mfma_f32_16x16x32_bf16 v[128:131], a[120:123], v[24:27], v[128:131]\n"
    "v_add_f32 v6, v6, v57\n"
    "v_cvt_pk_bf16_f32 v56, v56, v57\n"
    "v_mfma_f32_16x16x32_bf16 v[160:163], a[120:123], v[32:35], v[160:163]\n"
    "v_exp_f32 v58, v58\n"
    "v_mfma_f32_16x16x32_bf16 v[192:195], a[120:123], v[40:43], v[192:195]\n"
    "ds_read_b64_tr_b16 a[120:121], v3 offset:57856\n"
    "ds_read_b64_tr_b16 a[122:123], v3 offset:61952\n"
    "v_mfma_f32_16x16x32_bf16 v[100:103], a[124:127], v[16:19], v[100:103]\n"
    "v_add_f32 v6, v6, v58\n"
    "v_mfma_f32_16x16x32_bf16 v[132:135], a[124:127], v[24:27], v[132:135]\n"
    "v_exp_f32 v59, v59\n"
    "v_mfma_f32_16x16x32_bf16 v[164:167], a[124:127], v[32:35], v[164:167]\n"
    "v_add_f32 v6, v6, v59\n"
    "v_cvt_pk_bf16_f32 v57, v58, v59\n"
    "v_mfma_f32_16x16x32_bf16 v[196:199], a[124:127], v[40:43], v[196:199]\n"
    "s_waitcnt lgkmcnt(8)\n"
    "v_exp_f32 v64, v64\n"
    "v_mfma_f32_16x16x32_bf16 v[104:107], a[104:107], v[16:19], v[104:107]\n"
    "ds_read_b64_tr_b16 a[124:125], v3 offset:57864\n"
    "ds_read_b64_tr_b16 a[126:127], v3 offset:61960\n"
    "v_mfma_f32_16x16x32_bf16 v[136:139], a[104:107], v[24:27], v[136:139]\n"
    "v_add_f32 v7, v7, v64\n"
    "v_mfma_f32_16x16x32_bf16 v[168:171], a[104:107], v[32:35], v[168:171]\n"
    "v_exp_f32 v65, v65\n"
    "v_mfma_f32_16x16x32_bf16 v[200:203], a[104:107], v[40:43], v[200:203]\n"
    "s_waitcnt lgkmcnt(8)\n"
    "ds_read_b64_tr_b16 a[104:105], v3 offset:58368\n"
    "ds_read_b64_tr_b16 a[106:107], v3 offset:62464\n"
    "v_mfma_f32_16x16x32_bf16 v[108:111], a[108:111], v[16:19], v[108:111]\n"
    "v_add_f32 v7, v7, v65\n"
    "v_cvt_pk_bf16_f32 v64, v64, v65\n"
    "v_mfma_f32_16x16x32_bf16 v[140:143], a[108:111], v[24:27], v[140:143]\n"
    "v_exp_f32 v66, v66\n"
    "v_mfma_f32_16x16x32_bf16 v[172:175], a[108:111], v[32:35], v[172:175]\n"
    "v_add_f32 v7, v7, v66\n"
    "v_mfma_f32_16x16x32_bf16 v[204:207], a[108:111], v[40:43], v[204:207]\n"
    "v_exp_f32 v67, v67\n"
    "v_mfma_f32_16x16x32_bf16 v[24:27], a[80:83], a[16:19], v[244:247]\n"
    "ds_read_b64_tr_b16 a[108:109], v3 offset:58376\n"
    "ds_read_b64_tr_b16 a[110:111], v3 offset:62472\n"
    "v_mfma_f32_16x16x32_bf16 v[32:35], a[80:83], a[32:35], v[248:251]\n"
    "v_add_f32 v7, v7, v67\n"
    "v_cvt_pk_bf16_f32 v65, v66, v67\n"
    "v_mfma_f32_16x16x32_bf16 v[40:43], a[80:83], a[48:51], v[252:255]\n"
    "s_waitcnt vmcnt(7)\n"
    "v_exp_f32 v72, v72\n"
    "v_mfma_f32_16x16x32_bf16 v[16:19], a[80:83], a[64:67], 0\n"
    "ds_read_b128 a[80:83], v1 offset:5120\n"
    "v_add_f32 v8, v8, v72\n"
    "v_mfma_f32_16x16x32_bf16 v[24:27], a[84:87], a[20:23], v[24:27]\n"
    "v_exp_f32 v73, v73\n"
    "v_mfma_f32_16x16x32_bf16 v[32:35], a[84:87], a[36:39], v[32:35]\n"
    "v_add_f32 v8, v8, v73\n"
    "v_cvt_pk_bf16_f32 v72, v72, v73\n"
    "v_mfma_f32_16x16x32_bf16 v[40:43], a[84:87], a[52:55], v[40:43]\n"
    "s_waitcnt vmcnt(6)\n"
    "v_exp_f32 v74, v74\n"
    "v_mfma_f32_16x16x32_bf16 v[16:19], a[84:87], a[68:71], v[16:19]\n"
    "ds_read_b128 a[84:87], v1 offset:5632\n"
    "v_add_f32 v8, v8, v74\n"
    "v_mfma_f32_16x16x32_bf16 v[24:27], a[88:91], a[24:27], v[24:27]\n"
    "v_exp_f32 v75, v75\n"
    "v_mfma_f32_16x16x32_bf16 v[32:35], a[88:91], a[40:43], v[32:35]\n"
    "v_add_f32 v8, v8, v75\n"
    "v_cvt_pk_bf16_f32 v73, v74, v75\n"
    "v_mfma_f32_16x16x32_bf16 v[40:43], a[88:91], a[56:59], v[40:43]\n"
    "s_waitcnt vmcnt(5)\n"
    "v_exp_f32 v52, v52\n"
    "v_mfma_f32_16x16x32_bf16 v[16:19], a[88:91], a[72:75], v[16:19]\n"
    "ds_read_b128 a[88:91], v1 offset:8192\n"
    "v_add_f32 v5, v5, v52\n"
    "v_mfma_f32_16x16x32_bf16 v[24:27], a[92:95], a[28:31], v[24:27]\n"
    "v_exp_f32 v53, v53\n"
    "v_mfma_f32_16x16x32_bf16 v[32:35], a[92:95], a[44:47], v[32:35]\n"
    "v_add_f32 v5, v5, v53\n"
    "v_cvt_pk_bf16_f32 v50, v52, v53\n"
    "v_mfma_f32_16x16x32_bf16 v[40:43], a[92:95], a[60:63], v[40:43]\n"
    "s_waitcnt vmcnt(4)\n"
    "v_exp_f32 v54, v54\n"
    "v_mfma_f32_16x16x32_bf16 v[16:19], a[92:95], a[76:79], v[16:19]\n"
    "ds_read_b128 a[92:95], v1 offset:8704\n"
    "v_add_f32 v5, v5, v54\n"
    "v_mfma_f32_16x16x32_bf16 v[28:31], a[96:99], a[16:19], v[244:247]\n"
    "v_exp_f32 v55, v55\n"
    "v_mfma_f32_16x16x32_bf16 v[36:39], a[96:99], a[32:35], v[248:251]\n"
    "v_add_f32 v5, v5, v55\n"
    "v_cvt_pk_bf16_f32 v51, v54, v55\n"
    "v_mfma_f32_16x16x32_bf16 v[44:47], a[96:99], a[48:51], v[252:255]\n"
    "v_exp_f32 v60, v60\n"
    "v_mfma_f32_16x16x32_bf16 v[20:23], a[96:99], a[64:67], 0\n"
    "ds_read_b128 a[96:99], v1 offset:9216\n"
    "v_add_f32 v6, v6, v60\n"
    "v_mfma_f32_16x16x32_bf16 v[28:31], a[100:103], a[20:23], v[28:31]\n"
    "v_exp_f32 v61, v61\n"
    "v_mfma_f32_16x16x32_bf16 v[36:39], a[100:103], a[36:39], v[36:39]\n"
    "v_add_f32 v6, v6, v61\n"
    "v_cvt_pk_bf16_f32 v58, v60, v61\n"
    "v_mfma_f32_16x16x32_bf16 v[44:47], a[100:103], a[52:55], v[44:47]\n"
    "v_exp_f32 v62, v62\n"
    "v_mfma_f32_16x16x32_bf16 v[20:23], a[100:103], a[68:71], v[20:23]\n"
    "s_waitcnt lgkmcnt(4)\n"
    "ds_read_b128 a[100:103], v1 offset:9728\n"
    "v_add_f32 v6, v6, v62\n"
    "v_mfma_f32_16x16x32_bf16 v[28:31], a[80:83], a[24:27], v[28:31]\n"
    "v_exp_f32 v63, v63\n"
    "v_mfma_f32_16x16x32_bf16 v[36:39], a[80:83], a[40:43], v[36:39]\n"
    "v_add_f32 v6, v6, v63\n"
    "v_cvt_pk_bf16_f32 v59, v62, v63\n"
    "v_mfma_f32_16x16x32_bf16 v[44:47], a[80:83], a[56:59], v[44:47]\n"
    "v_exp_f32 v68, v68\n"
    "v_mfma_f32_16x16x32_bf16 v[20:23], a[80:83], a[72:75], v[20:23]\n"
    "s_waitcnt lgkmcnt(4)\n"
    "ds_read_b128 a[80:83], v1 offset:12288\n"
    "v_add_f32 v7, v7, v68\n"
    "v_mfma_f32_16x16x32_bf16 v[28:31], a[84:87], a[28:31], v[28:31]\n"
    "v_exp_f32 v69, v69\n"
    "v_mfma_f32_16x16x32_bf16 v[36:39], a[84:87], a[44:47], v[36:39]\n"
    "v_add_f32 v7, v7, v69\n"
    "v_cvt_pk_bf16_f32 v66, v68, v69\n"
    "v_mfma_f32_16x16x32_bf16 v[44:47], a[84:87], a[60:63], v[44:47]\n"
    "v_exp_f32 v70, v70\n"
    "v_mfma_f32_16x16x32_bf16 v[20:23], a[84:87], a[76:79], v[20:23]\n"
    "ds_read_b128 a[84:87], v1 offset:12800\n"
    "v_add_f32 v7, v7, v70\n"
    "v_mfma_f32_16x16x32_bf16 v[80:83], a[112:115], v[48:51], v[80:83]\n"
    "v_exp_f32 v71, v71\n"
    "v_mfma_f32_16x16x32_bf16 v[112:115], a[112:115], v[56:59], v[112:115]\n"
    "v_add_f32 v7, v7, v71\n"
    "v_cvt_pk_bf16_f32 v67, v70, v71\n"
    "v_max3_f32 v10, v16, v17, v18\n"
    "v_mov_b32 v12, v5\n"
    "v_mfma_f32_16x16x32_bf16 v[144:147], a[112:115], v[64:67], v[144:147]\n"
    "s_mov_b32 s62, 0\n"
    "v_max3_f32 v11, v20, v21, v22\n"
    "v_max3_f32 v10, v10, v19, v23\n"
    "v_max_f32 v10, v10, v11\n"
    "v_mov_b32 v11, v10\n"
    "v_exp_f32 v76, v76\n"
    "s_nop 0\n"
    "v_permlane32_swap_b32 v10, v11\n"
    "v_add_f32 v8, v8, v76\n"
    "v_max_f32 v10, v10, v11\n"
    "v_mov_b32 v11, v10\n"
    "v_exp_f32 v77, v77\n"
    "s_nop 0\n"
    "v_permlane16_swap_b32 v10, v11\n"
    "v_add_f32 v8, v8, v77\n"
    "v_min_f32 v240, -v10, -v11\n"
    "v_min_f32 v241, -v10, -v11\n"
    "v_min_f32 v242, -v10, -v11\n"
    "v_min_f32 v243, -v10, -v11\n"
    "v_add_f32 v16, v16, v240\n"
    "v_add_f32 v17, v17, v240\n"
    "v_add_f32 v18, v18, v240\n"
    "v_add_f32 v19, v19, v240\n"
    "v_add_f32 v20, v20, v240\n"
    "v_add_f32 v21, v21, v240\n"
    "v_add_f32 v22, v22, v240\n"
    "v_add_f32 v23, v23, v240\n"
    "v_cvt_pk_bf16_f32 v74, v76, v77\n"
    "v_exp_f32 v78, v78\n"
    "v_exp_f32 v79, v79\n"
    "v_add_f32 v8, v8, v78\n"
    "v_add_f32 v8, v8, v79\n"
    "v_cvt_pk_bf16_f32 v75, v78, v79\n"
    "v_exp_f32 v24, v24\n"
    "s_nop 0\n"
    "v_mfma_f32_16x16x32_bf16 v[176:179], a[112:115], v[72:75], v[176:179]\n"
    "ds_read_b64_tr_b16 a[112:113], v3 offset:58880\n"
    "ds_read_b64_tr_b16 a[114:115], v3 offset:62976\n"
    "v_mfma_f32_16x16x32_bf16 v[84:87], a[116:119], v[48:51], v[84:87]\n"
    "v_add_f32 v6, v6, v24\n"
    "v_permlane32_swap_b32 v5, v12\n"
    "v_mfma_f32_16x16x32_bf16 v[116:119], a[116:119], v[56:59], v[116:119]\n"
    "v_exp_f32 v25, v25\n"
    "v_mfma_f32_16x16x32_bf16 v[148:151], a[116:119], v[64:67], v[148:151]\n"
    "v_add_f32 v6, v6, v25\n"
    "v_cvt_pk_bf16_f32 v24, v24, v25\n"
    "v_mfma_f32_16x16x32_bf16 v[180:183], a[116:119], v[72:75], v[180:183]\n"
    "v_exp_f32 v26, v26\n"
    "v_mfma_f32_16x16x32_bf16 v[88:91], a[120:123], v[48:51], v[88:91]\n"
    "ds_read_b64_tr_b16 a[116:117], v3 offset:58888\n"
    "ds_read_b64_tr_b16 a[118:119], v3 offset:62984\n"
    "v_mfma_f32_16x16x32_bf16 v[120:123], a[120:123], v[56:59], v[120:123]\n"
    "v_add_f32 v6, v6, v26\n"
    "v_add_f32 v5, v5, v12\n"
    "v_mfma_f32_16x16x32_bf16 v[152:155], a[120:123], v[64:67], v[152:155]\n"
    "v_exp_f32 v27, v27\n"
    "v_mfma_f32_16x16x32_bf16 v[184:187], a[120:123], v[72:75], v[184:187]\n"
    "ds_read_b64_tr_b16 a[120:121], v3 offset:0\n"
    "ds_read_b64_tr_b16 a[122:123], v3 offset:4096\n"
    "v_mfma_f32_16x16x32_bf16 v[92:95], a[124:127], v[48:51], v[92:95]\n"
    "v_add_f32 v6, v6, v27\n"
    "v_cvt_pk_bf16_f32 v25, v26, v27\n"
    "v_mfma_f32_16x16x32_bf16 v[124:127], a[124:127], v[56:59], v[124:127]\n"
    "v_exp_f32 v32, v32\n"
    "v_mfma_f32_16x16x32_bf16 v[156:159], a[124:127], v[64:67], v[156:159]\n"
    "v_add_f32 v7, v7, v32\n"
    "v_mov_b32 v12, v5\n"
    "v_mfma_f32_16x16x32_bf16 v[188:191], a[124:127], v[72:75], v[188:191]\n"
    "v_exp_f32 v33, v33\n"
    "v_mfma_f32_16x16x32_bf16 v[96:99], a[104:107], v[48:51], v[96:99]\n"
    "ds_read_b64_tr_b16 a[124:125], v3 offset:8\n"
    "ds_read_b64_tr_b16 a[126:127], v3 offset:4104\n"
    "v_mfma_f32_16x16x32_bf16 v[128:131], a[104:107], v[56:59], v[128:131]\n"
    "v_add_f32 v7, v7, v33\n"
    "v_cvt_pk_bf16_f32 v32, v32, v33\n"
    "v_mfma_f32_16x16x32_bf16 v[160:163], a[104:107], v[64:67], v[160:163]\n"
    "v_exp_f32 v34, v34\n"
    "v_mfma_f32_16x16x32_bf16 v[192:195], a[104:107], v[72:75], v[192:195]\n"
    "ds_read_b64_tr_b16 a[104:105], v3 offset:512\n"
    "ds_read_b64_tr_b16 a[106:107], v3 offset:4608\n"
    "v_mfma_f32_16x16x32_bf16 v[100:103], a[108:111], v[48:51], v[100:103]\n"
    "v_add_f32 v7, v7, v34\n"
    "v_permlane16_swap_b32 v5, v12\n"
    "v_mfma_f32_16x16x32_bf16 v[132:135], a[108:111], v[56:59], v[132:135]\n"
    "v_exp_f32 v35, v35\n"
    "v_mfma_f32_16x16x32_bf16 v[164:167], a[108:111], v[64:67], v[164:167]\n"
    "v_add_f32 v7, v7, v35\n"
    "v_cvt_pk_bf16_f32 v33, v34, v35\n"
    "v_mfma_f32_16x16x32_bf16 v[196:199], a[108:111], v[72:75], v[196:199]\n"
    "s_waitcnt lgkmcnt(8)\n"
    "v_exp_f32 v40, v40\n"
    "v_mfma_f32_16x16x32_bf16 v[104:107], a[112:115], v[48:51], v[104:107]\n"
    "ds_read_b64_tr_b16 a[108:109], v3 offset:520\n"
    "ds_read_b64_tr_b16 a[110:111], v3 offset:4616\n"
    "v_mfma_f32_16x16x32_bf16 v[136:139], a[112:115], v[56:59], v[136:139]\n"
    "v_add_f32 v8, v8, v40\n"
    "v_add_f32 v12, v5, v12\n"
    "v_mfma_f32_16x16x32_bf16 v[168:171], a[112:115], v[64:67], v[168:171]\n"
    "v_exp_f32 v41, v41\n"
    "v_mfma_f32_16x16x32_bf16 v[200:203], a[112:115], v[72:75], v[200:203]\n"
    "s_waitcnt lgkmcnt(8)\n"
    "ds_read_b64_tr_b16 a[112:113], v3 offset:1024\n"
    "ds_read_b64_tr_b16 a[114:115], v3 offset:5120\n"
    "v_mfma_f32_16x16x32_bf16 v[108:111], a[116:119], v[48:51], v[108:111]\n"
    "v_add_f32 v8, v8, v41\n"
    "v_cvt_pk_bf16_f32 v40, v40, v41\n"
    "v_mfma_f32_16x16x32_bf16 v[140:143], a[116:119], v[56:59], v[140:143]\n"
    "v_exp_f32 v42, v42\n"
    "v_mfma_f32_16x16x32_bf16 v[172:175], a[116:119], v[64:67], v[172:175]\n"
    "v_add_f32 v8, v8, v42\n"
    "v_cmp_ngt_f32 vcc, 0x5d800000, v12\n"
    "v_mfma_f32_16x16x32_bf16 v[204:207], a[116:119], v[72:75], v[204:207]\n"
    "s_mov_b32 s77, 20480\n"
    "v_exp_f32 v43, v43\n"
    "v_mfma_f32_16x16x32_bf16 v[56:59], a[88:91], a[16:19], v[244:247]\n"
    "s_or_b64 s[64:65], s[64:65], vcc\n"
    "buffer_load_dword v15, v14, s[72:75], s77 offen\n"
    "ds_read_b64_tr_b16 a[116:117], v3 offset:1032\n"
    "v_mfma_f32_16x16x32_bf16 v[64:67], a[88:91], a[32:35], v[248:251]\n"
    "ds_read_b64_tr_b16 a[118:119], v3 offset:5128\n"
    "v_add_f32 v8, v8, v43\n"
    "v_mfma_f32_16x16x32_bf16 v[72:75], a[88:91], a[48:51], v[252:255]\n"
    "v_cvt_pk_bf16_f32 v41, v42, v43\n"
    "v_mfma_f32_16x16x32_bf16 v[48:51], a[88:91], a[64:67], v[240:243]\n"
    "v_exp_f32 v16, v16\n"
    "v_mfma_f32_16x16x32_bf16 v[56:59], a[92:95], a[20:23], v[56:59]\n"
    "v_exp_f32 v17, v17\n"
    "v_mfma_f32_16x16x32_bf16 v[64:67], a[92:95], a[36:39], v[64:67]\n"
    "ds_read_b128 a[88:91], v1 offset:13312\n"
    "v_add_f32 v9, v16, v17\n"
    "v_mfma_f32_16x16x32_bf16 v[72:75], a[92:95], a[52:55], v[72:75]\n"
    "v_cvt_pk_bf16_f32 v16, v16, v17\n"
    "v_mfma_f32_16x16x32_bf16 v[48:51], a[92:95], a[68:71], v[48:51]\n"
    "v_exp_f32 v18, v18\n"
    "v_mfma_f32_16x16x32_bf16 v[56:59], a[96:99], a[24:27], v[56:59]\n"
    "ds_read_b128 a[92:95], v1 offset:13824\n"
    "v_add_f32 v9, v9, v18\n"
    "v_mfma_f32_16x16x32_bf16 v[64:67], a[96:99], a[40:43], v[64:67]\n"
    "v_exp_f32 v19, v19\n"
    "v_mfma_f32_16x16x32_bf16 v[72:75], a[96:99], a[56:59], v[72:75]\n"
    "v_add_f32 v9, v9, v19\n"
    "v_cvt_pk_bf16_f32 v17, v18, v19\n"
    "v_mfma_f32_16x16x32_bf16 v[48:51], a[96:99], a[72:75], v[48:51]\n"
    "v_exp_f32 v28, v28\n"
    "v_mfma_f32_16x16x32_bf16 v[56:59], a[100:103], a[28:31], v[56:59]\n"
    "ds_read_b128 a[96:99], v1 offset:16384\n"
    "v_add_f32 v6, v6, v28\n"
    "v_mfma_f32_16x16x32_bf16 v[64:67], a[100:103], a[44:47], v[64:67]\n"
    "v_exp_f32 v29, v29\n"
    "v_mfma_f32_16x16x32_bf16 v[72:75], a[100:103], a[60:63], v[72:75]\n"
    "v_add_f32 v6, v6, v29\n"
    "v_cvt_pk_bf16_f32 v26, v28, v29\n"
    "v_mfma_f32_16x16x32_bf16 v[48:51], a[100:103], a[76:79], v[48:51]\n"
    "v_exp_f32 v30, v30\n"
    "v_mfma_f32_16x16x32_bf16 v[60:63], a[80:83], a[16:19], v[244:247]\n"
    "ds_read_b128 a[100:103], v1 offset:16896\n"
    "v_add_f32 v6, v6, v30\n"
    "v_mfma_f32_16x16x32_bf16 v[68:71], a[80:83], a[32:35], v[248:251]\n"
    "v_exp_f32 v31, v31\n"
    "v_mfma_f32_16x16x32_bf16 v[76:79], a[80:83], a[48:51], v[252:255]\n"
    "v_add_f32 v6, v6, v31\n"
    "v_cvt_pk_bf16_f32 v27, v30, v31\n"
    "v_mfma_f32_16x16x32_bf16 v[52:55], a[80:83], a[64:67], v[240:243]\n"
    "v_exp_f32 v36, v36\n"
    "v_mfma_f32_16x16x32_bf16 v[60:63], a[84:87], a[20:23], v[60:63]\n"
    "ds_read_b128 a[80:83], v1 offset:17408\n"
    "v_add_f32 v7, v7, v36\n"
    "v_mfma_f32_16x16x32_bf16 v[68:71], a[84:87], a[36:39], v[68:71]\n"
    "v_exp_f32 v37, v37\n"
    "v_mfma_f32_16x16x32_bf16 v[76:79], a[84:87], a[52:55], v[76:79]\n"
    "v_add_f32 v7, v7, v37\n"
    "v_cvt_pk_bf16_f32 v34, v36, v37\n"
    "v_mfma_f32_16x16x32_bf16 v[52:55], a[84:87], a[68:71], v[52:55]\n"
    "s_waitcnt lgkmcnt(4)\n"
    "v_exp_f32 v38, v38\n"
    "v_mfma_f32_16x16x32_bf16 v[60:63], a[88:91], a[24:27], v[60:63]\n"
    "ds_read_b128 a[84:87], v1 offset:17920\n"
    "v_add_f32 v7, v7, v38\n"
    "v_mfma_f32_16x16x32_bf16 v[68:71], a[88:91], a[40:43], v[68:71]\n"
    "v_exp_f32 v39, v39\n"
    "v_mfma_f32_16x16x32_bf16 v[76:79], a[88:91], a[56:59], v[76:79]\n"
    "v_add_f32 v7, v7, v39\n"
    "v_cvt_pk_bf16_f32 v35, v38, v39\n"
    "v_mfma_f32_16x16x32_bf16 v[52:55], a[88:91], a[72:75], v[52:55]\n"
    "s_waitcnt lgkmcnt(4)\n"
    "v_exp_f32 v44, v44\n"
    "v_mfma_f32_16x16x32_bf16 v[60:63], a[92:95], a[28:31], v[60:63]\n"
    "ds_read_b128 a[88:91], v1 offset:20480\n"
    "v_add_f32 v8, v8, v44\n"
    "v_mfma_f32_16x16x32_bf16 v[68:71], a[92:95], a[44:47], v[68:71]\n"
    "v_exp_f32 v45, v45\n"
    "v_mfma_f32_16x16x32_bf16 v[76:79], a[92:95], a[60:63], v[76:79]\n"
    "v_add_f32 v8, v8, v45\n"
    "v_cvt_pk_bf16_f32 v42, v44, v45\n"
    "v_mfma_f32_16x16x32_bf16 v[52:55], a[92:95], a[76:79], v[52:55]\n"
    "v_exp_f32 v46, v46\n"
    "v_mfma_f32_16x16x32_bf16 v[112:115], a[128:131], v[24:27], v[112:115]\n"
    "ds_read_b128 a[92:95], v1 offset:20992\n"
    "v_add_f32 v8, v8, v46\n"
    "v_mfma_f32_16x16x32_bf16 v[144:147], a[128:131], v[32:35], v[144:147]\n"
    "v_exp_f32 v47, v47\n"
    "v_exp_f32 v20, v20\n"
    "v_add_f32 v8, v8, v47\n"
    "v_cvt_pk_bf16_f32 v43, v46, v47\n"
    "v_add_f32 v9, v9, v20\n"
    "s_nop 0\n"
    "v_mfma_f32_16x16x32_bf16 v[176:179], a[128:131], v[40:43], v[176:179]\n"
    "v_exp_f32 v21, v21\n"
    "v_exp_f32 v22, v22\n"
    "v_add_f32 v9, v9, v21\n"
    "v_cvt_pk_bf16_f32 v18, v20, v21\n"
    "v_add_f32 v9, v9, v22\n"
    "v_exp_f32 v23, v23\n"
    "v_rcp_f32 v12, v12\n"
    "v_add_f32 v9, v9, v23\n"
    "v_cvt_pk_bf16_f32 v19, v22, v23\n"
    "v_mul_f32 v80, v80, v12\n"
    "v_mul_f32 v81, v81, v12\n"
    "v_mfma_f32_16x16x32_bf16 v[208:211], a[128:131], v[16:19], 0\n"
    "v_mul_f32 v82, v82, v12\n"
    "v_mul_f32 v83, v83, v12\n"
    "v_mfma_f32_16x16x32_bf16 v[116:119], a[132:135], v[24:27], v[116:119]\n"
    "v_mul_f32 v84, v84, v12\n"
    "v_mul_f32 v85, v85, v12\n"
    "v_mfma_f32_16x16x32_bf16 v[148:151], a[132:135], v[32:35], v[148:151]\n"
    "v_mul_f32 v86, v86, v12\n"
    "v_mul_f32 v87, v87, v12\n"
    "v_mfma_f32_16x16x32_bf16 v[180:183], a[132:135], v[40:43], v[180:183]\n"
    "v_cvt_pk_bf16_f32 v80, v80, v81\n"
    "v_cvt_pk_bf16_f32 v81, v82, v83\n"
    "v_mfma_f32_16x16x32_bf16 v[212:215], a[132:135], v[16:19], 0\n"
    "v_cvt_pk_bf16_f32 v82, v84, v85\n"
    "v_cvt_pk_bf16_f32 v83, v86, v87\n"
    "v_mfma_f32_16x16x32_bf16 v[120:123], a[136:139], v[24:27], v[120:123]\n"
    "v_mul_f32 v88, v88, v12\n"
    "v_mul_f32 v89, v89, v12\n"
    "v_mfma_f32_16x16x32_bf16 v[152:155], a[136:139], v[32:35], v[152:155]\n"
    "v_mul_f32 v90, v90, v12\n"
    "v_mul_f32 v91, v91, v12\n"
    "v_mfma_f32_16x16x32_bf16 v[184:187], a[136:139], v[40:43], v[184:187]\n"
    "v_mul_f32 v92, v92, v12\n"
    "v_mul_f32 v93, v93, v12\n"
    "v_mfma_f32_16x16x32_bf16 v[216:219], a[136:139], v[16:19], 0\n"
    "v_mul_f32 v94, v94, v12\n"
    "v_mul_f32 v95, v95, v12\n"
    "v_mfma_f32_16x16x32_bf16 v[124:127], a[140:143], v[24:27], v[124:127]\n"
    "v_cvt_pk_bf16_f32 v88, v88, v89\n"
    "v_cvt_pk_bf16_f32 v89, v90, v91\n"
    "v_mfma_f32_16x16x32_bf16 v[156:159], a[140:143], v[32:35], v[156:159]\n"
    "v_cvt_pk_bf16_f32 v90, v92, v93\n"
    "v_cvt_pk_bf16_f32 v91, v94, v95\n"
    "v_mfma_f32_16x16x32_bf16 v[188:191], a[140:143], v[40:43], v[188:191]\n"
    "v_exp_f32 v56, v56\n"
    "v_mfma_f32_16x16x32_bf16 v[220:223], a[140:143], v[16:19], 0\n"
    "v_add_f32 v6, v6, v56\n"
    "v_mul_f32 v96, v96, v12\n"
    "v_mfma_f32_16x16x32_bf16 v[128:131], a[144:147], v[24:27], v[128:131]\n"
    "v_exp_f32 v57, v57\n"
    "v_mfma_f32_16x16x32_bf16 v[160:163], a[144:147], v[32:35], v[160:163]\n"
    "v_add_f32 v6, v6, v57\n"
    "v_cvt_pk_bf16_f32 v56, v56, v57\n"
    "v_mfma_f32_16x16x32_bf16 v[192:195], a[144:147], v[40:43], v[192:195]\n"
    "v_exp_f32 v58, v58\n"
    "v_mfma_f32_16x16x32_bf16 v[224:227], a[144:147], v[16:19], 0\n"
    "v_add_f32 v6, v6, v58\n"
    "v_mul_f32 v97, v97, v12\n"
    "v_mfma_f32_16x16x32_bf16 v[132:135], a[148:151], v[24:27], v[132:135]\n"
    "v_exp_f32 v59, v59\n"
    "v_mfma_f32_16x16x32_bf16 v[164:167], a[148:151], v[32:35], v[164:167]\n"
    "v_add_f32 v6, v6, v59\n"
    "v_cvt_pk_bf16_f32 v57, v58, v59\n"
    "v_mfma_f32_16x16x32_bf16 v[196:199], a[148:151], v[40:43], v[196:199]\n"
    "v_mul_f32 v98, v98, v12\n"
    "v_mul_f32 v99, v99, v12\n"
    "v_mfma_f32_16x16x32_bf16 v[228:231], a[148:151], v[16:19], 0\n"
    "v_mul_f32 v100, v100, v12\n"
    "v_mul_f32 v101, v101, v12\n"
    "v_mfma_f32_16x16x32_bf16 v[136:139], a[152:155], v[24:27], v[136:139]\n"
    "v_mul_f32 v102, v102, v12\n"
    "v_mul_f32 v103, v103, v12\n"
    "v_mfma_f32_16x16x32_bf16 v[168:171], a[152:155], v[32:35], v[168:171]\n"
    "v_cvt_pk_bf16_f32 v96, v96, v97\n"
    "v_cvt_pk_bf16_f32 v97, v98, v99\n"
    "v_mfma_f32_16x16x32_bf16 v[200:203], a[152:155], v[40:43], v[200:203]\n"
    "v_cvt_pk_bf16_f32 v98, v100, v101\n"
    "v_cvt_pk_bf16_f32 v99, v102, v103\n"
    "v_mfma_f32_16x16x32_bf16 v[232:235], a[152:155], v[16:19], 0\n"
    "v_mul_f32 v104, v104, v12\n"
    "v_mul_f32 v105, v105, v12\n"
    "v_mfma_f32_16x16x32_bf16 v[140:143], a[156:159], v[24:27], v[140:143]\n"
    "v_mul_f32 v106, v106, v12\n"
    "v_mul_f32 v107, v107, v12\n"
    "v_mfma_f32_16x16x32_bf16 v[172:175], a[156:159], v[32:35], v[172:175]\n"
    "v_mul_f32 v108, v108, v12\n"
    "v_mul_f32 v109, v109, v12\n"
    "v_mfma_f32_16x16x32_bf16 v[204:207], a[156:159], v[40:43], v[204:207]\n"
    "v_mul_f32 v110, v110, v12\n"
    "v_mul_f32 v111, v111, v12\n"
    "v_mfma_f32_16x16x32_bf16 v[236:239], a[156:159], v[16:19], 0\n"
    "s_waitcnt lgkmcnt(5)\n"
    "v_cvt_pk_bf16_f32 v104, v104, v105\n"
    "v_cvt_pk_bf16_f32 v105, v106, v107\n"
    "v_mfma_f32_16x16x32_bf16 v[24:27], a[96:99], a[16:19], v[244:247]\n"
    "v_cvt_pk_bf16_f32 v106, v108, v109\n"
    "v_cvt_pk_bf16_f32 v107, v110, v111\n"
    "v_mfma_f32_16x16x32_bf16 v[32:35], a[96:99], a[32:35], v[248:251]\n"
    "v_exp_f32 v64, v64\n"
    "v_mfma_f32_16x16x32_bf16 v[40:43], a[96:99], a[48:51], v[252:255]\n"
    "v_add_f32 v7, v7, v64\n"
    "v_mfma_f32_16x16x32_bf16 v[16:19], a[96:99], a[64:67], v[240:243]\n"
    "s_waitcnt lgkmcnt(4)\n"
    "v_exp_f32 v65, v65\n"
    "v_mfma_f32_16x16x32_bf16 v[24:27], a[100:103], a[20:23], v[24:27]\n"
    "ds_read_b128 a[96:99], v1 offset:21504\n"
    "v_add_f32 v7, v7, v65\n"
    "v_mfma_f32_16x16x32_bf16 v[32:35], a[100:103], a[36:39], v[32:35]\n"
    "v_cvt_pk_bf16_f32 v64, v64, v65\n"
    "v_mfma_f32_16x16x32_bf16 v[40:43], a[100:103], a[52:55], v[40:43]\n"
    "v_exp_f32 v66, v66\n"
    "v_mfma_f32_16x16x32_bf16 v[16:19], a[100:103], a[68:71], v[16:19]\n"
    "s_waitcnt lgkmcnt(4)\n"
    "ds_read_b128 a[100:103], v1 offset:22016\n"
    "v_add_f32 v7, v7, v66\n"
    "v_mfma_f32_16x16x32_bf16 v[24:27], a[80:83], a[24:27], v[24:27]\n"
    "v_exp_f32 v67, v67\n"
    "v_mfma_f32_16x16x32_bf16 v[32:35], a[80:83], a[40:43], v[32:35]\n"
    "v_add_f32 v7, v7, v67\n"
    "v_cvt_pk_bf16_f32 v65, v66, v67\n"
    "v_mfma_f32_16x16x32_bf16 v[40:43], a[80:83], a[56:59], v[40:43]\n"
    "v_exp_f32 v72, v72\n"
    "v_mfma_f32_16x16x32_bf16 v[16:19], a[80:83], a[72:75], v[16:19]\n"
    "s_waitcnt lgkmcnt(4)\n"
    "ds_read_b128 a[80:83], v1 offset:24576\n"
    "v_add_f32 v8, v8, v72\n"
    "v_mfma_f32_16x16x32_bf16 v[24:27], a[84:87], a[28:31], v[24:27]\n"
    "v_exp_f32 v73, v73\n"
    "v_mfma_f32_16x16x32_bf16 v[32:35], a[84:87], a[44:47], v[32:35]\n"
    "v_add_f32 v8, v8, v73\n"
    "v_cvt_pk_bf16_f32 v72, v72, v73\n"
    "v_mfma_f32_16x16x32_bf16 v[40:43], a[84:87], a[60:63], v[40:43]\n"
    "v_exp_f32 v74, v74\n"
    "v_mfma_f32_16x16x32_bf16 v[16:19], a[84:87], a[76:79], v[16:19]\n"
    "s_waitcnt lgkmcnt(4)\n"
    "ds_read_b128 a[84:87], v1 offset:25088\n"
    "v_add_f32 v8, v8, v74\n"
    "v_mfma_f32_16x16x32_bf16 v[28:31], a[88:91], a[16:19], v[244:247]\n"
    "v_exp_f32 v75, v75\n"
    "v_mfma_f32_16x16x32_bf16 v[36:39], a[88:91], a[32:35], v[248:251]\n"
    "v_add_f32 v8, v8, v75\n"
    "v_cvt_pk_bf16_f32 v73, v74, v75\n"
    "v_mfma_f32_16x16x32_bf16 v[44:47], a[88:91], a[48:51], v[252:255]\n"
    "v_exp_f32 v48, v48\n"
    "v_mfma_f32_16x16x32_bf16 v[20:23], a[88:91], a[64:67], v[240:243]\n"
    "s_waitcnt lgkmcnt(4)\n"
    "ds_read_b128 a[88:91], v1 offset:25600\n"
    "v_add_f32 v9, v9, v48\n"
    "v_mfma_f32_16x16x32_bf16 v[28:31], a[92:95], a[20:23], v[28:31]\n"
    "v_exp_f32 v49, v49\n"
    "v_mfma_f32_16x16x32_bf16 v[36:39], a[92:95], a[36:39], v[36:39]\n"
    "v_add_f32 v9, v9, v49\n"
    "v_cvt_pk_bf16_f32 v48, v48, v49\n"
    "v_mfma_f32_16x16x32_bf16 v[44:47], a[92:95], a[52:55], v[44:47]\n"
    "v_exp_f32 v50, v50\n"
    "v_mfma_f32_16x16x32_bf16 v[20:23], a[92:95], a[68:71], v[20:23]\n"
    "s_waitcnt lgkmcnt(4)\n"
    "ds_read_b128 a[92:95], v1 offset:26112\n"
    "v_add_f32 v9, v9, v50\n"
    "v_mfma_f32_16x16x32_bf16 v[28:31], a[96:99], a[24:27], v[28:31]\n"
    "v_exp_f32 v51, v51\n"
    "v_mfma_f32_16x16x32_bf16 v[36:39], a[96:99], a[40:43], v[36:39]\n"
    "v_add_f32 v9, v9, v51\n"
    "v_cvt_pk_bf16_f32 v49, v50, v51\n"
    "v_mfma_f32_16x16x32_bf16 v[44:47], a[96:99], a[56:59], v[44:47]\n"
    "v_exp_f32 v60, v60\n"
    "v_mfma_f32_16x16x32_bf16 v[20:23], a[96:99], a[72:75], v[20:23]\n"
    "s_waitcnt lgkmcnt(4)\n"
    "ds_read_b128 a[96:99], v1 offset:28672\n"
    "v_add_f32 v6, v6, v60\n"
    "v_mfma_f32_16x16x32_bf16 v[28:31], a[100:103], a[28:31], v[28:31]\n"
    "v_exp_f32 v61, v61\n"
    "v_mfma_f32_16x16x32_bf16 v[36:39], a[100:103], a[44:47], v[36:39]\n"
    "buffer_load_dwordx4 a[16:19], v4, s[40:43], s60 offen offset:0\n"
    "buffer_load_dwordx4 a[20:23], v4, s[40:43], s60 offen offset:64\n"
    "v_mfma_f32_16x16x32_bf16 v[44:47], a[100:103], a[60:63], v[44:47]\n"
    "v_add_f32 v6, v6, v61\n"
    "v_cvt_pk_bf16_f32 v58, v60, v61\n"
    "v_mfma_f32_16x16x32_bf16 v[20:23], a[100:103], a[76:79], v[20:23]\n"
    "ds_read_b128 a[100:103], v1 offset:29184\n"
    "v_exp_f32 v62, v62\n"
    "v_exp_f32 v63, v63\n"
    "v_add_f32 v6, v6, v62\n"
    "v_add_f32 v6, v6, v63\n"
    "v_cvt_pk_bf16_f32 v59, v62, v63\n"
    "v_exp_f32 v68, v68\n"
    "s_nop 0\n"
    "v_mfma_f32_16x16x32_bf16 v[112:115], a[160:163], v[56:59], v[112:115]\n"
    "v_add_f32 v7, v7, v68\n"
    "v_exp_f32 v69, v69\n"
    "v_exp_f32 v70, v70\n"
    "v_add_f32 v7, v7, v69\n"
    "v_cvt_pk_bf16_f32 v66, v68, v69\n"
    "v_add_f32 v7, v7, v70\n"
    "v_exp_f32 v71, v71\n"
    "v_exp_f32 v76, v76\n"
    "v_add_f32 v7, v7, v71\n"
    "v_cvt_pk_bf16_f32 v67, v70, v71\n"
    "v_add_f32 v8, v8, v76\n"
    "s_nop 0\n"
    "v_mfma_f32_16x16x32_bf16 v[144:147], a[160:163], v[64:67], v[144:147]\n"
    "buffer_load_dwordx4 a[24:27], v4, s[40:43], s60 offen offset:128\n"
    "v_exp_f32 v77, v77\n"
    "v_exp_f32 v78, v78\n"
    "v_add_f32 v8, v8, v77\n"
    "v_cvt_pk_bf16_f32 v74, v76, v77\n"
    "v_add_f32 v8, v8, v78\n"
    "v_exp_f32 v79, v79\n"
    "v_exp_f32 v52, v52\n"
    "v_add_f32 v8, v8, v79\n"
    "v_cvt_pk_bf16_f32 v75, v78, v79\n"
    "v_add_f32 v9, v9, v52\n"
    "s_nop 0\n"
    "v_mfma_f32_16x16x32_bf16 v[176:179], a[160:163], v[72:75], v[176:179]\n"
    "buffer_load_dwordx4 a[28:31], v4, s[40:43], s60 offen offset:192\n"
    "v_exp_f32 v53, v53\n"
    "s_mov_b32 s60, 131072\n"
    "v_add_f32 v9, v9, v53\n"
    "v_cvt_pk_bf16_f32 v50, v52, v53\n"
    "v_exp_f32 v54, v54\n"
    "v_exp_f32 v55, v55\n"
    "v_add_f32 v9, v9, v54\n"
    "v_add_f32 v9, v9, v55\n"
    "v_cvt_pk_bf16_f32 v51, v54, v55\n"
    "v_exp_f32 v24, v24\n"
    "s_nop 0\n"
    "v_mfma_f32_16x16x32_bf16 v[208:211], a[160:163], v[48:51], v[208:211]\n"
    "buffer_store_dwordx4 v[80:83], v4, s[52:55], s62 offen offset:0 sc0 sc1\n"
    "v_add_f32 v6, v6, v24\n"
    "v_mfma_f32_16x16x32_bf16 v[116:119], a[164:167], v[56:59], v[116:119]\n"
    "v_exp_f32 v25, v25\n"
    "v_mfma_f32_16x16x32_bf16 v[148:151], a[164:167], v[64:67], v[148:151]\n"
    "v_add_f32 v6, v6, v25\n"
    "v_cvt_pk_bf16_f32 v24, v24, v25\n"
    "v_mfma_f32_16x16x32_bf16 v[180:183], a[164:167], v[72:75], v[180:183]\n"
    "v_exp_f32 v26, v26\n"
    "v_mfma_f32_16x16x32_bf16 v[212:215], a[164:167], v[48:51], v[212:215]\n"
    "v_add_f32 v6, v6, v26\n"
    "v_mfma_f32_16x16x32_bf16 v[120:123], a[168:171], v[56:59], v[120:123]\n"
    "v_exp_f32 v27, v27\n"
    "v_mfma_f32_16x16x32_bf16 v[152:155], a[168:171], v[64:67], v[152:155]\n"
    "buffer_store_dwordx4 v[88:91], v4, s[52:55], s62 offen offset:64 sc0 sc1\n"
    "v_add_f32 v6, v6, v27\n"
    "v_mfma_f32_16x16x32_bf16 v[184:187], a[168:171], v[72:75], v[184:187]\n"
    "v_cvt_pk_bf16_f32 v25, v26, v27\n"
    "v_mfma_f32_16x16x32_bf16 v[216:219], a[168:171], v[48:51], v[216:219]\n"
    "v_exp_f32 v32, v32\n"
    "v_mfma_f32_16x16x32_bf16 v[124:127], a[172:175], v[56:59], v[124:127]\n"
    "v_add_f32 v7, v7, v32\n"
    "v_mfma_f32_16x16x32_bf16 v[156:159], a[172:175], v[64:67], v[156:159]\n"
    "v_exp_f32 v33, v33\n"
    "v_mfma_f32_16x16x32_bf16 v[188:191], a[172:175], v[72:75], v[188:191]\n"
    "v_add_f32 v7, v7, v33\n"
    "v_cvt_pk_bf16_f32 v32, v32, v33\n"
    "v_mfma_f32_16x16x32_bf16 v[220:223], a[172:175], v[48:51], v[220:223]\n"
    "buffer_store_dwordx4 v[96:99], v4, s[52:55], s62 offen offset:128 sc0 sc1\n"
    "v_mfma_f32_16x16x32_bf16 v[128:131], a[176:179], v[56:59], v[128:131]\n"
    "v_exp_f32 v34, v34\n"
    "v_mfma_f32_16x16x32_bf16 v[160:163], a[176:179], v[64:67], v[160:163]\n"
    "v_add_f32 v7, v7, v34\n"
    "v_mfma_f32_16x16x32_bf16 v[192:195], a[176:179], v[72:75], v[192:195]\n"
    "v_exp_f32 v35, v35\n"
    "v_mfma_f32_16x16x32_bf16 v[224:227], a[176:179], v[48:51], v[224:227]\n"
    "v_add_f32 v7, v7, v35\n"
    "v_cvt_pk_bf16_f32 v33, v34, v35\n"
    "v_mfma_f32_16x16x32_bf16 v[132:135], a[180:183], v[56:59], v[132:135]\n"
    "v_exp_f32 v40, v40\n"
    "v_mfma_f32_16x16x32_bf16 v[164:167], a[180:183], v[64:67], v[164:167]\n"
    "buffer_store_dwordx4 v[104:107], v4, s[52:55], s62 offen offset:192 sc0 sc1\n"
    "v_add_f32 v8, v8, v40\n"
    "v_mfma_f32_16x16x32_bf16 v[196:199], a[180:183], v[72:75], v[196:199]\n"
    "v_exp_f32 v41, v41\n"
    "v_mfma_f32_16x16x32_bf16 v[228:231], a[180:183], v[48:51], v[228:231]\n"
    "v_add_f32 v8, v8, v41\n"
    "v_cvt_pk_bf16_f32 v40, v40, v41\n"
    "v_mfma_f32_16x16x32_bf16 v[136:139], a[184:187], v[56:59], v[136:139]\n"
    "v_exp_f32 v42, v42\n"
    "v_mfma_f32_16x16x32_bf16 v[168:171], a[184:187], v[64:67], v[168:171]\n"
    "v_add_f32 v8, v8, v42\n"
    "v_mfma_f32_16x16x32_bf16 v[200:203], a[184:187], v[72:75], v[200:203]\n"
    "v_exp_f32 v43, v43\n"
    "v_mfma_f32_16x16x32_bf16 v[232:235], a[184:187], v[48:51], v[232:235]\n"
    "v_add_f32 v8, v8, v43\n"
    "v_cvt_pk_bf16_f32 v41, v42, v43\n"
    "v_mfma_f32_16x16x32_bf16 v[140:143], a[188:191], v[56:59], v[140:143]\n"
    "v_exp_f32 v16, v16\n"
    "v_mfma_f32_16x16x32_bf16 v[172:175], a[188:191], v[64:67], v[172:175]\n"
    "v_add_f32 v9, v9, v16\n"
    "v_mfma_f32_16x16x32_bf16 v[204:207], a[188:191], v[72:75], v[204:207]\n"
    "v_exp_f32 v17, v17\n"
    "v_mfma_f32_16x16x32_bf16 v[236:239], a[188:191], v[48:51], v[236:239]\n"
    "s_waitcnt lgkmcnt(5)\n"
    "v_add_f32 v9, v9, v17\n"
    "v_cvt_pk_bf16_f32 v16, v16, v17\n"
    "v_mfma_f32_16x16x32_bf16 v[64:67], a[80:83], a[32:35], v[248:251]\n"
    "v_exp_f32 v18, v18\n"
    "v_mfma_f32_16x16x32_bf16 v[72:75], a[80:83], a[48:51], v[252:255]\n"
    "v_add_f32 v9, v9, v18\n"
    "v_mfma_f32_16x16x32_bf16 v[48:51], a[80:83], a[64:67], v[240:243]\n"
    "s_waitcnt vmcnt(12)\n"
    "v_exp_f32 v19, v19\n"
    "v_mfma_f32_16x16x32_bf16 v[56:59], a[80:83], a[0:3], 0\n"
    "s_waitcnt lgkmcnt(4)\n"
    "ds_read_b128 a[80:83], v1 offset:29696\n"
    "v_add_f32 v9, v9, v19\n"
    "v_mfma_f32_16x16x32_bf16 v[64:67], a[84:87], a[36:39], v[64:67]\n"
    "v_cvt_pk_bf16_f32 v17, v18, v19\n"
    "v_mfma_f32_16x16x32_bf16 v[72:75], a[84:87], a[52:55], v[72:75]\n"
    "v_exp_f32 v28, v28\n"
    "v_mfma_f32_16x16x32_bf16 v[48:51], a[84:87], a[68:71], v[48:51]\n"
    "s_waitcnt vmcnt(11)\n"
    "v_add_f32 v6, v6, v28\n"
    "v_mfma_f32_16x16x32_bf16 v[56:59], a[84:87], a[4:7], v[56:59]\n"
    "s_waitcnt lgkmcnt(4)\n"
    "v_exp_f32 v29, v29\n"
    "v_mfma_f32_16x16x32_bf16 v[64:67], a[88:91], a[40:43], v[64:67]\n"
    "ds_read_b128 a[84:87], v1 offset:30208\n"
    "v_add_f32 v6, v6, v29\n"
    "v_mfma_f32_16x16x32_bf16 v[72:75], a[88:91], a[56:59], v[72:75]\n"
    "v_cvt_pk_bf16_f32 v26, v28, v29\n"
    "v_mfma_f32_16x16x32_bf16 v[48:51], a[88:91], a[72:75], v[48:51]\n"
    "s_waitcnt vmcnt(10)\n"
    "v_exp_f32 v30, v30\n"
    "v_mfma_f32_16x16x32_bf16 v[56:59], a[88:91], a[8:11], v[56:59]\n"
    "s_waitcnt lgkmcnt(4)\n"
    "ds_read_b128 a[88:91], v1 offset:32768\n"
    "v_add_f32 v6, v6, v30\n"
    "v_mfma_f32_16x16x32_bf16 v[64:67], a[92:95], a[44:47], v[64:67]\n"
    "v_exp_f32 v31, v31\n"
    "v_mfma_f32_16x16x32_bf16 v[72:75], a[92:95], a[60:63], v[72:75]\n"
    "v_add_f32 v6, v6, v31\n"
    "v_cvt_pk_bf16_f32 v27, v30, v31\n"
    "v_mfma_f32_16x16x32_bf16 v[48:51], a[92:95], a[76:79], v[48:51]\n"
    "s_waitcnt vmcnt(9)\n"
    "v_exp_f32 v36, v36\n"
    "v_mfma_f32_16x16x32_bf16 v[56:59], a[92:95], a[12:15], v[56:59]\n"
    "s_waitcnt lgkmcnt(4)\n"
    "ds_read_b128 a[92:95], v1 offset:33280\n"
    "v_add_f32 v7, v7, v36\n"
    "v_mfma_f32_16x16x32_bf16 v[68:71], a[96:99], a[32:35], v[248:251]\n"
    "v_exp_f32 v37, v37\n"
    "v_mfma_f32_16x16x32_bf16 v[76:79], a[96:99], a[48:51], v[252:255]\n"
    "v_add_f32 v7, v7, v37\n"
    "v_cvt_pk_bf16_f32 v34, v36, v37\n"
    "v_mfma_f32_16x16x32_bf16 v[52:55], a[96:99], a[64:67], v[240:243]\n"
    "v_exp_f32 v38, v38\n"
    "v_mfma_f32_16x16x32_bf16 v[60:63], a[96:99], a[0:3], 0\n"
    "s_waitcnt lgkmcnt(4)\n"
    "ds_read_b128 a[96:99], v1 offset:33792\n"
    "v_add_f32 v7, v7, v38\n"
    "v_mfma_f32_16x16x32_bf16 v[68:71], a[100:103], a[36:39], v[68:71]\n"
    "v_exp_f32 v39, v39\n"
    "v_mfma_f32_16x16x32_bf16 v[76:79], a[100:103], a[52:55], v[76:79]\n"
    "v_add_f32 v7, v7, v39\n"
    "v_cvt_pk_bf16_f32 v35, v38, v39\n"
    "v_mfma_f32_16x16x32_bf16 v[52:55], a[100:103], a[68:71], v[52:55]\n"
    "v_exp_f32 v44, v44\n"
    "v_mfma_f32_16x16x32_bf16 v[60:63], a[100:103], a[4:7], v[60:63]\n"
    "s_waitcnt lgkmcnt(4)\n"
    "ds_read_b128 a[100:103], v1 offset:34304\n"
    "v_add_f32 v8, v8, v44\n"
    "v_mfma_f32_16x16x32_bf16 v[68:71], a[80:83], a[40:43], v[68:71]\n"
    "v_exp_f32 v45, v45\n"
    "v_mfma_f32_16x16x32_bf16 v[76:79], a[80:83], a[56:59], v[76:79]\n"
    "v_add_f32 v8, v8, v45\n"
    "v_cvt_pk_bf16_f32 v42, v44, v45\n"
    "v_mfma_f32_16x16x32_bf16 v[52:55], a[80:83], a[72:75], v[52:55]\n"
    "v_exp_f32 v46, v46\n"
    "v_mfma_f32_16x16x32_bf16 v[60:63], a[80:83], a[8:11], v[60:63]\n"
    "s_waitcnt lgkmcnt(4)\n"
    "ds_read_b128 a[80:83], v1 offset:36864\n"
    "v_add_f32 v8, v8, v46\n"
    "v_mfma_f32_16x16x32_bf16 v[68:71], a[84:87], a[44:47], v[68:71]\n"
    "v_exp_f32 v47, v47\n"
    "v_mfma_f32_16x16x32_bf16 v[76:79], a[84:87], a[60:63], v[76:79]\n"
    "v_add_f32 v8, v8, v47\n"
    "v_cvt_pk_bf16_f32 v43, v46, v47\n"
    "v_mfma_f32_16x16x32_bf16 v[52:55], a[84:87], a[76:79], v[52:55]\n"
    "v_max3_f32 v10, v56, v57, v58\n"
    "v_mov_b32 v12, v6\n"
    "v_mfma_f32_16x16x32_bf16 v[60:63], a[84:87], a[12:15], v[60:63]\n"
    "s_mov_b32 s63, 16384\n"
    "v_exp_f32 v20, v20\n"
    "v_mfma_f32_16x16x32_bf16 v[112:115], a[192:195], v[24:27], v[112:115]\n"
    "ds_read_b128 a[84:87], v1 offset:37376\n"
    "v_add_f32 v9, v9, v20\n"
    "v_mfma_f32_16x16x32_bf16 v[144:147], a[192:195], v[32:35], v[144:147]\n"
    "v_exp_f32 v21, v21\n"
    "v_mfma_f32_16x16x32_bf16 v[176:179], a[192:195], v[40:43], v[176:179]\n"
    "v_max3_f32 v11, v60, v61, v62\n"
    "v_max3_f32 v10, v10, v59, v63\n"
    "v_max_f32 v10, v10, v11\n"
    "v_mov_b32 v11, v10\n"
    "v_add_f32 v9, v9, v21\n"
    "v_cvt_pk_bf16_f32 v18, v20, v21\n"
    "v_permlane32_swap_b32 v10, v11\n"
    "v_exp_f32 v22, v22\n"
    "v_max_f32 v10, v10, v11\n"
    "v_mov_b32 v11, v10\n"
    "v_add_f32 v9, v9, v22\n"
    "v_exp_f32 v23, v23\n"
    "v_permlane16_swap_b32 v10, v11\n"
    "v_add_f32 v9, v9, v23\n"
    "v_min_f32 v244, -v10, -v11\n"
    "v_min_f32 v245, -v10, -v11\n"
    "v_min_f32 v246, -v10, -v11\n"
    "v_min_f32 v247, -v10, -v11\n"
    "v_add_f32 v56, v56, v244\n"
    "v_add_f32 v57, v57, v244\n"
    "v_add_f32 v58, v58, v244\n"
    "v_add_f32 v59, v59, v244\n"
    "v_add_f32 v60, v60, v244\n"
    "v_add_f32 v61, v61, v244\n"
    "v_add_f32 v62, v62, v244\n"
    "v_add_f32 v63, v63, v244\n"
    "v_cvt_pk_bf16_f32 v19, v22, v23\n"
    "v_exp_f32 v64, v64\n"
    "s_nop 0\n"
    "v_mfma_f32_16x16x32_bf16 v[208:211], a[192:195], v[16:19], v[208:211]\n"
    "v_add_f32 v7, v7, v64\n"
    "v_permlane32_swap_b32 v6, v12\n"
    "v_mfma_f32_16x16x32_bf16 v[116:119], a[196:199], v[24:27], v[116:119]\n"
    "v_exp_f32 v65, v65\n"
    "v_mfma_f32_16x16x32_bf16 v[148:151], a[196:199], v[32:35], v[148:151]\n"
    "v_add_f32 v7, v7, v65\n"
    "v_cvt_pk_bf16_f32 v64, v64, v65\n"
    "v_mfma_f32_16x16x32_bf16 v[180:183], a[196:199], v[40:43], v[180:183]\n"
    "v_exp_f32 v66, v66\n"
    "v_mfma_f32_16x16x32_bf16 v[212:215], a[196:199], v[16:19], v[212:215]\n"
    "v_add_f32 v7, v7, v66\n"
    "v_add_f32 v6, v6, v12\n"
    "v_mfma_f32_16x16x32_bf16 v[120:123], a[200:203], v[24:27], v[120:123]\n"
    "v_exp_f32 v67, v67\n"
    "v_mfma_f32_16x16x32_bf16 v[152:155], a[200:203], v[32:35], v[152:155]\n"
    "v_add_f32 v7, v7, v67\n"
    "v_cvt_pk_bf16_f32 v65, v66, v67\n"
    "v_mfma_f32_16x16x32_bf16 v[184:187], a[200:203], v[40:43], v[184:187]\n"
    "v_exp_f32 v72, v72\n"
    "v_mfma_f32_16x16x32_bf16 v[216:219], a[200:203], v[16:19], v[216:219]\n"
    "v_add_f32 v8, v8, v72\n"
    "v_mov_b32 v12, v6\n"
    "v_mfma_f32_16x16x32_bf16 v[124:127], a[204:207], v[24:27], v[124:127]\n"
    "v_exp_f32 v73, v73\n"
    "v_mfma_f32_16x16x32_bf16 v[156:159], a[204:207], v[32:35], v[156:159]\n"
    "v_add_f32 v8, v8, v73\n"
    "v_cvt_pk_bf16_f32 v72, v72, v73\n"
    "v_mfma_f32_16x16x32_bf16 v[188:191], a[204:207], v[40:43], v[188:191]\n"
    "v_exp_f32 v74, v74\n"
    "v_mfma_f32_16x16x32_bf16 v[220:223], a[204:207], v[16:19], v[220:223]\n"
    "v_add_f32 v8, v8, v74\n"
    "v_permlane16_swap_b32 v6, v12\n"
    "v_mfma_f32_16x16x32_bf16 v[128:131], a[208:211], v[24:27], v[128:131]\n"
    "v_exp_f32 v75, v75\n"
    "v_mfma_f32_16x16x32_bf16 v[160:163], a[208:211], v[32:35], v[160:163]\n"
    "v_add_f32 v8, v8, v75\n"
    "v_cvt_pk_bf16_f32 v73, v74, v75\n"
    "v_mfma_f32_16x16x32_bf16 v[192:195], a[208:211], v[40:43], v[192:195]\n"
    "v_exp_f32 v48, v48\n"
    "v_mfma_f32_16x16x32_bf16 v[224:227], a[208:211], v[16:19], v[224:227]\n"
    "v_add_f32 v9, v9, v48\n"
    "v_add_f32 v12, v6, v12\n"
    "v_mfma_f32_16x16x32_bf16 v[132:135], a[212:215], v[24:27], v[132:135]\n"
    "v_exp_f32 v49, v49\n"
    "v_mfma_f32_16x16x32_bf16 v[164:167], a[212:215], v[32:35], v[164:167]\n"
    "v_add_f32 v9, v9, v49\n"
    "v_cvt_pk_bf16_f32 v48, v48, v49\n"
    "v_mfma_f32_16x16x32_bf16 v[196:199], a[212:215], v[40:43], v[196:199]\n"
    "v_exp_f32 v50, v50\n"
    "v_mfma_f32_16x16x32_bf16 v[228:231], a[212:215], v[16:19], v[228:231]\n"
    "v_add_f32 v9, v9, v50\n"
    "v_cmp_ngt_f32 vcc, 0x5d800000, v12\n"
    "v_mfma_f32_16x16x32_bf16 v[136:139], a[216:219], v[24:27], v[136:139]\n"
    "v_exp_f32 v51, v51\n"
    "v_mfma_f32_16x16x32_bf16 v[168:171], a[216:219], v[32:35], v[168:171]\n"
    "s_or_b64 s[64:65], s[64:65], vcc\n"
    "v_add_f32 v9, v9, v51\n"
    "v_cvt_pk_bf16_f32 v49, v50, v51\n"
    "v_mfma_f32_16x16x32_bf16 v[200:203], a[216:219], v[40:43], v[200:203]\n"
    "v_exp_f32 v56, v56\n"
    "v_mfma_f32_16x16x32_bf16 v[232:235], a[216:219], v[16:19], v[232:235]\n"
    "v_exp_f32 v57, v57\n"
    "v_mfma_f32_16x16x32_bf16 v[140:143], a[220:223], v[24:27], v[140:143]\n"
    "v_add_f32 v5, v56, v57\n"
    "v_cvt_pk_bf16_f32 v56, v56, v57\n"
    "v_mfma_f32_16x16x32_bf16 v[172:175], a[220:223], v[32:35], v[172:175]\n"
    "v_exp_f32 v58, v58\n"
    "v_mfma_f32_16x16x32_bf16 v[204:207], a[220:223], v[40:43], v[204:207]\n"
    "v_add_f32 v5, v5, v58\n"
    "v_mfma_f32_16x16x32_bf16 v[236:239], a[220:223], v[16:19], v[236:239]\n"
    "s_waitcnt lgkmcnt(5)\n"
    "s_mov_b32 s76, 24576\n"
    "v_exp_f32 v59, v59\n"
    "v_mfma_f32_16x16x32_bf16 v[32:35], a[88:91], a[32:35], v[248:251]\n"
    "buffer_load_dword v15, v14, s[72:75], s76 offen\n"
    "v_add_f32 v5, v5, v59\n"
    "v_mfma_f32_16x16x32_bf16 v[40:43], a[88:91], a[48:51], v[252:255]\n"
    "v_cvt_pk_bf16_f32 v57, v58, v59\n"
    "v_mfma_f32_16x16x32_bf16 v[16:19], a[88:91], a[64:67], v[240:243]\n"
    "v_exp_f32 v68, v68\n"
    "v_mfma_f32_16x16x32_bf16 v[24:27], a[88:91], a[0:3], v[244:247]\n"
    "s_waitcnt lgkmcnt(4)\n"
    "ds_read_b128 a[88:91], v1 offset:37888\n"
    "v_add_f32 v7, v7, v68\n"
    "v_mfma_f32_16x16x32_bf16 v[32:35], a[92:95], a[36:39], v[32:35]\n"
    "v_exp_f32 v69, v69\n"
    "v_mfma_f32_16x16x32_bf16 v[40:43], a[92:95], a[52:55], v[40:43]\n"
    "v_add_f32 v7, v7, v69\n"
    "v_cvt_pk_bf16_f32 v66, v68, v69\n"
    "v_mfma_f32_16x16x32_bf16 v[16:19], a[92:95], a[68:71], v[16:19]\n"
    "v_exp_f32 v70, v70\n"
    "v_mfma_f32_16x16x32_bf16 v[24:27], a[92:95], a[4:7], v[24:27]\n"
    "s_waitcnt lgkmcnt(4)\n"
    "ds_read_b128 a[92:95], v1 offset:38400\n"
    "v_add_f32 v7, v7, v70\n"
    "v_mfma_f32_16x16x32_bf16 v[32:35], a[96:99], a[40:43], v[32:35]\n"
    "v_exp_f32 v71, v71\n"
    "v_mfma_f32_16x16x32_bf16 v[40:43], a[96:99], a[56:59], v[40:43]\n"
    "v_add_f32 v7, v7, v71\n"
    "v_cvt_pk_bf16_f32 v67, v70, v71\n"
    "v_mfma_f32_16x16x32_bf16 v[16:19], a[96:99], a[72:75], v[16:19]\n"
    "v_exp_f32 v76, v76\n"
    "v_mfma_f32_16x16x32_bf16 v[24:27], a[96:99], a[8:11], v[24:27]\n"
    "s_waitcnt lgkmcnt(4)\n"
    "ds_read_b128 a[96:99], v1 offset:40960\n"
    "v_add_f32 v8, v8, v76\n"
    "v_mfma_f32_16x16x32_bf16 v[32:35], a[100:103], a[44:47], v[32:35]\n"
    "v_exp_f32 v77, v77\n"
    "v_mfma_f32_16x16x32_bf16 v[40:43], a[100:103], a[60:63], v[40:43]\n"
    "v_add_f32 v8, v8, v77\n"
    "v_cvt_pk_bf16_f32 v74, v76, v77\n"
    "v_mfma_f32_16x16x32_bf16 v[16:19], a[100:103], a[76:79], v[16:19]\n"
    "v_exp_f32 v78, v78\n"
    "v_mfma_f32_16x16x32_bf16 v[24:27], a[100:103], a[12:15], v[24:27]\n"
    "s_waitcnt lgkmcnt(4)\n"
    "ds_read_b128 a[100:103], v1 offset:41472\n"
    "v_add_f32 v8, v8, v78\n"
    "v_mfma_f32_16x16x32_bf16 v[36:39], a[80:83], a[32:35], v[248:251]\n"
    "v_exp_f32 v79, v79\n"
    "v_mfma_f32_16x16x32_bf16 v[44:47], a[80:83], a[48:51], v[252:255]\n"
    "v_add_f32 v8, v8, v79\n"
    "v_cvt_pk_bf16_f32 v75, v78, v79\n"
    "v_mfma_f32_16x16x32_bf16 v[20:23], a[80:83], a[64:67], v[240:243]\n"
    "v_exp_f32 v52, v52\n"
    "v_mfma_f32_16x16x32_bf16 v[28:31], a[80:83], a[0:3], v[244:247]\n"
    "s_waitcnt lgkmcnt(4)\n"
    "ds_read_b128 a[80:83], v1 offset:41984\n"
    "v_add_f32 v9, v9, v52\n"
    "v_mfma_f32_16x16x32_bf16 v[36:39], a[84:87], a[36:39], v[36:39]\n"
    "v_exp_f32 v53, v53\n"
    "v_mfma_f32_16x16x32_bf16 v[44:47], a[84:87], a[52:55], v[44:47]\n"
    "v_add_f32 v9, v9, v53\n"
    "v_cvt_pk_bf16_f32 v50, v52, v53\n"
    "v_mfma_f32_16x16x32_bf16 v[20:23], a[84:87], a[68:71], v[20:23]\n"
    "v_exp_f32 v54, v54\n"
    "v_mfma_f32_16x16x32_bf16 v[28:31], a[84:87], a[4:7], v[28:31]\n"
    "s_waitcnt lgkmcnt(4)\n"
    "ds_read_b128 a[84:87], v1 offset:42496\n"
    "v_add_f32 v9, v9, v54\n"
    "v_mfma_f32_16x16x32_bf16 v[36:39], a[88:91], a[40:43], v[36:39]\n"
    "v_exp_f32 v55, v55\n"
    "v_mfma_f32_16x16x32_bf16 v[44:47], a[88:91], a[56:59], v[44:47]\n"
    "v_add_f32 v9, v9, v55\n"
    "v_cvt_pk_bf16_f32 v51, v54, v55\n"
    "v_mfma_f32_16x16x32_bf16 v[20:23], a[88:91], a[72:75], v[20:23]\n"
    "v_exp_f32 v60, v60\n"
    "v_mfma_f32_16x16x32_bf16 v[28:31], a[88:91], a[8:11], v[28:31]\n"
    "s_waitcnt lgkmcnt(4)\n"
    "ds_read_b128 a[88:91], v1 offset:45056\n"
    "v_add_f32 v5, v5, v60\n"
    "v_mfma_f32_16x16x32_bf16 v[36:39], a[92:95], a[44:47], v[36:39]\n"
    "v_exp_f32 v61, v61\n"
    "v_mfma_f32_16x16x32_bf16 v[44:47], a[92:95], a[60:63], v[44:47]\n"
    "v_add_f32 v5, v5, v61\n"
    "v_cvt_pk_bf16_f32 v58, v60, v61\n"
    "v_mfma_f32_16x16x32_bf16 v[20:23], a[92:95], a[76:79], v[20:23]\n"
    "v_exp_f32 v62, v62\n"
    "v_mfma_f32_16x16x32_bf16 v[28:31], a[92:95], a[12:15], v[28:31]\n"
    "ds_read_b128 a[92:95], v1 offset:45568\n"
    "v_add_f32 v5, v5, v62\n"
    "v_mfma_f32_16x16x32_bf16 v[144:147], a[224:227], v[64:67], v[144:147]\n"
    "v_exp_f32 v63, v63\n"
    "v_mfma_f32_16x16x32_bf16 v[176:179], a[224:227], v[72:75], v[176:179]\n"
    "v_add_f32 v5, v5, v63\n"
    "v_cvt_pk_bf16_f32 v59, v62, v63\n"
    "v_mfma_f32_16x16x32_bf16 v[208:211], a[224:227], v[48:51], v[208:211]\n"
    "v_rcp_f32 v12, v12\n"
    "v_mfma_f32_16x16x32_bf16 v[80:83], a[224:227], v[56:59], 0\n"
    "v_mul_f32 v112, v112, v12\n"
    "v_mul_f32 v113, v113, v12\n"
    "v_mfma_f32_16x16x32_bf16 v[148:151], a[228:231], v[64:67], v[148:151]\n"
    "v_mul_f32 v114, v114, v12\n"
    "v_mul_f32 v115, v115, v12\n"
    "v_mfma_f32_16x16x32_bf16 v[180:183], a[228:231], v[72:75], v[180:183]\n"
    "v_mul_f32 v116, v116, v12\n"
    "v_mul_f32 v117, v117, v12\n"
    "v_mfma_f32_16x16x32_bf16 v[212:215], a[228:231], v[48:51], v[212:215]\n"
    "v_mul_f32 v118, v118, v12\n"
    "v_mul_f32 v119, v119, v12\n"
    "v_mfma_f32_16x16x32_bf16 v[84:87], a[228:231], v[56:59], 0\n"
    "v_cvt_pk_bf16_f32 v112, v112, v113\n"
    "v_cvt_pk_bf16_f32 v113, v114, v115\n"
    "v_mfma_f32_16x16x32_bf16 v[152:155], a[232:235], v[64:67], v[152:155]\n"
    "v_cvt_pk_bf16_f32 v114, v116, v117\n"
    "v_cvt_pk_bf16_f32 v115, v118, v119\n"
    "v_mfma_f32_16x16x32_bf16 v[184:187], a[232:235], v[72:75], v[184:187]\n"
    "v_mul_f32 v120, v120, v12\n"
    "v_mul_f32 v121, v121, v12\n"
    "v_mfma_f32_16x16x32_bf16 v[216:219], a[232:235], v[48:51], v[216:219]\n"
    "v_mul_f32 v122, v122, v12\n"
    "v_mul_f32 v123, v123, v12\n"
    "v_mfma_f32_16x16x32_bf16 v[88:91], a[232:235], v[56:59], 0\n"
    "v_mul_f32 v124, v124, v12\n"
    "v_mul_f32 v125, v125, v12\n"
    "v_mfma_f32_16x16x32_bf16 v[156:159], a[236:239], v[64:67], v[156:159]\n"
    "v_mul_f32 v126, v126, v12\n"
    "v_mul_f32 v127, v127, v12\n"
    "v_mfma_f32_16x16x32_bf16 v[188:191], a[236:239], v[72:75], v[188:191]\n"
    "v_cvt_pk_bf16_f32 v120, v120, v121\n"
    "v_cvt_pk_bf16_f32 v121, v122, v123\n"
    "v_mfma_f32_16x16x32_bf16 v[220:223], a[236:239], v[48:51], v[220:223]\n"
    "v_cvt_pk_bf16_f32 v122, v124, v125\n"
    "v_cvt_pk_bf16_f32 v123, v126, v127\n"
    "v_mfma_f32_16x16x32_bf16 v[92:95], a[236:239], v[56:59], 0\n"
    "v_exp_f32 v32, v32\n"
    "v_mfma_f32_16x16x32_bf16 v[160:163], a[240:243], v[64:67], v[160:163]\n"
    "v_add_f32 v7, v7, v32\n"
    "v_mul_f32 v128, v128, v12\n"
    "v_mfma_f32_16x16x32_bf16 v[192:195], a[240:243], v[72:75], v[192:195]\n"
    "v_exp_f32 v33, v33\n"
    "v_mfma_f32_16x16x32_bf16 v[224:227], a[240:243], v[48:51], v[224:227]\n"
    "v_add_f32 v7, v7, v33\n"
    "v_cvt_pk_bf16_f32 v32, v32, v33\n"
    "v_mfma_f32_16x16x32_bf16 v[96:99], a[240:243], v[56:59], 0\n"
    "v_exp_f32 v34, v34\n"
    "v_mfma_f32_16x16x32_bf16 v[164:167], a[244:247], v[64:67], v[164:167]\n"
    "v_add_f32 v7, v7, v34\n"
    "v_mul_f32 v129, v129, v12\n"
    "v_mfma_f32_16x16x32_bf16 v[196:199], a[244:247], v[72:75], v[196:199]\n"
    "v_exp_f32 v35, v35\n"
    "v_mfma_f32_16x16x32_bf16 v[228:231], a[244:247], v[48:51], v[228:231]\n"
    "v_add_f32 v7, v7, v35\n"
    "v_cvt_pk_bf16_f32 v33, v34, v35\n"
    "v_mfma_f32_16x16x32_bf16 v[100:103], a[244:247], v[56:59], 0\n"
    "v_mul_f32 v130, v130, v12\n"
    "v_mul_f32 v131, v131, v12\n"
    "v_mfma_f32_16x16x32_bf16 v[168:171], a[248:251], v[64:67], v[168:171]\n"
    "v_mul_f32 v132, v132, v12\n"
    "v_mul_f32 v133, v133, v12\n"
    "v_mfma_f32_16x16x32_bf16 v[200:203], a[248:251], v[72:75], v[200:203]\n"
    "v_mul_f32 v134, v134, v12\n"
    "v_mul_f32 v135, v135, v12\n"
    "v_mfma_f32_16x16x32_bf16 v[232:235], a[248:251], v[48:51], v[232:235]\n"
    "v_cvt_pk_bf16_f32 v128, v128, v129\n"
    "v_cvt_pk_bf16_f32 v129, v130, v131\n"
    "v_mfma_f32_16x16x32_bf16 v[104:107], a[248:251], v[56:59], 0\n"
    "v_cvt_pk_bf16_f32 v130, v132, v133\n"
    "v_cvt_pk_bf16_f32 v131, v134, v135\n"
    "v_mfma_f32_16x16x32_bf16 v[172:175], a[252:255], v[64:67], v[172:175]\n"
    "v_mul_f32 v136, v136, v12\n"
    "v_mul_f32 v137, v137, v12\n"
    "v_mfma_f32_16x16x32_bf16 v[204:207], a[252:255], v[72:75], v[204:207]\n"
    "v_mul_f32 v138, v138, v12\n"
    "v_mul_f32 v139, v139, v12\n"
    "v_mfma_f32_16x16x32_bf16 v[236:239], a[252:255], v[48:51], v[236:239]\n"
    "v_mul_f32 v140, v140, v12\n"
    "v_mul_f32 v141, v141, v12\n"
    "v_mfma_f32_16x16x32_bf16 v[108:111], a[252:255], v[56:59], 0\n"
    "s_waitcnt lgkmcnt(5)\n"
    "v_mul_f32 v142, v142, v12\n"
    "v_mul_f32 v143, v143, v12\n"
    "v_mfma_f32_16x16x32_bf16 v[64:67], a[96:99], a[32:35], v[248:251]\n"
    "v_cvt_pk_bf16_f32 v136, v136, v137\n"
    "v_cvt_pk_bf16_f32 v137, v138, v139\n"
    "v_mfma_f32_16x16x32_bf16 v[72:75], a[96:99], a[48:51], v[252:255]\n"
    "v_cvt_pk_bf16_f32 v138, v140, v141\n"
    "v_cvt_pk_bf16_f32 v139, v142, v143\n"
    "v_mfma_f32_16x16x32_bf16 v[48:51], a[96:99], a[64:67], v[240:243]\n"
    "v_exp_f32 v40, v40\n"
    "v_mfma_f32_16x16x32_bf16 v[56:59], a[96:99], a[0:3], v[244:247]\n"
    "s_waitcnt lgkmcnt(4)\n"
    "ds_read_b128 a[96:99], v1 offset:46080\n"
    "v_add_f32 v8, v8, v40\n"
    "v_mfma_f32_16x16x32_bf16 v[64:67], a[100:103], a[36:39], v[64:67]\n"
    "v_exp_f32 v41, v41\n"
    "v_mfma_f32_16x16x32_bf16 v[72:75], a[100:103], a[52:55], v[72:75]\n"
    "v_add_f32 v8, v8, v41\n"
    "v_cvt_pk_bf16_f32 v40, v40, v41\n"
    "v_mfma_f32_16x16x32_bf16 v[48:51], a[100:103], a[68:71], v[48:51]\n"
    "v_exp_f32 v42, v42\n"
    "v_mfma_f32_16x16x32_bf16 v[56:59], a[100:103], a[4:7], v[56:59]\n"
    "s_waitcnt lgkmcnt(4)\n"
    "ds_read_b128 a[100:103], v1 offset:46592\n"
    "v_add_f32 v8, v8, v42\n"
    "v_mfma_f32_16x16x32_bf16 v[64:67], a[80:83], a[40:43], v[64:67]\n"
    "v_exp_f32 v43, v43\n"
    "v_mfma_f32_16x16x32_bf16 v[72:75], a[80:83], a[56:59], v[72:75]\n"
    "v_add_f32 v8, v8, v43\n"
    "v_cvt_pk_bf16_f32 v41, v42, v43\n"
    "v_mfma_f32_16x16x32_bf16 v[48:51], a[80:83], a[72:75], v[48:51]\n"
    "v_exp_f32 v16, v16\n"
    "v_mfma_f32_16x16x32_bf16 v[56:59], a[80:83], a[8:11], v[56:59]\n"
    "s_waitcnt lgkmcnt(4)\n"
    "ds_read_b128 a[80:83], v1 offset:49152\n"
    "v_add_f32 v9, v9, v16\n"
    "v_mfma_f32_16x16x32_bf16 v[64:67], a[84:87], a[44:47], v[64:67]\n"
    "v_exp_f32 v17, v17\n"
    "v_mfma_f32_16x16x32_bf16 v[72:75], a[84:87], a[60:63], v[72:75]\n"
    "v_add_f32 v9, v9, v17\n"
    "v_cvt_pk_bf16_f32 v16, v16, v17\n"
    "v_mfma_f32_16x16x32_bf16 v[48:51], a[84:87], a[76:79], v[48:51]\n"
    "v_exp_f32 v18, v18\n"
    "v_mfma_f32_16x16x32_bf16 v[56:59], a[84:87], a[12:15], v[56:59]\n"
    "s_waitcnt lgkmcnt(4)\n"
    "ds_read_b128 a[84:87], v1 offset:49664\n"
    "v_add_f32 v9, v9, v18\n"
    "v_mfma_f32_16x16x32_bf16 v[68:71], a[88:91], a[32:35], v[248:251]\n"
    "v_exp_f32 v19, v19\n"
    "v_mfma_f32_16x16x32_bf16 v[76:79], a[88:91], a[48:51], v[252:255]\n"
    "v_add_f32 v9, v9, v19\n"
    "v_cvt_pk_bf16_f32 v17, v18, v19\n"
    "v_mfma_f32_16x16x32_bf16 v[52:55], a[88:91], a[64:67], v[240:243]\n"
    "v_exp_f32 v24, v24\n"
    "v_mfma_f32_16x16x32_bf16 v[60:63], a[88:91], a[0:3], v[244:247]\n"
    "s_waitcnt lgkmcnt(4)\n"
    "ds_read_b128 a[88:91], v1 offset:50176\n"
    "v_add_f32 v5, v5, v24\n"
    "v_mfma_f32_16x16x32_bf16 v[68:71], a[92:95], a[36:39], v[68:71]\n"
    "v_exp_f32 v25, v25\n"
    "v_mfma_f32_16x16x32_bf16 v[76:79], a[92:95], a[52:55], v[76:79]\n"
    "v_add_f32 v5, v5, v25\n"
    "v_cvt_pk_bf16_f32 v24, v24, v25\n"
    "v_mfma_f32_16x16x32_bf16 v[52:55], a[92:95], a[68:71], v[52:55]\n"
    "v_exp_f32 v26, v26\n"
    "v_mfma_f32_16x16x32_bf16 v[60:63], a[92:95], a[4:7], v[60:63]\n"
    "s_waitcnt lgkmcnt(4)\n"
    "ds_read_b128 a[92:95], v1 offset:50688\n"
    "v_add_f32 v5, v5, v26\n"
    "v_mfma_f32_16x16x32_bf16 v[68:71], a[96:99], a[40:43], v[68:71]\n"
    "v_exp_f32 v27, v27\n"
    "v_mfma_f32_16x16x32_bf16 v[76:79], a[96:99], a[56:59], v[76:79]\n"
    "v_add_f32 v5, v5, v27\n"
    "v_cvt_pk_bf16_f32 v25, v26, v27\n"
    "v_mfma_f32_16x16x32_bf16 v[52:55], a[96:99], a[72:75], v[52:55]\n"
    "v_exp_f32 v36, v36\n"
    "v_mfma_f32_16x16x32_bf16 v[60:63], a[96:99], a[8:11], v[60:63]\n"
    "s_waitcnt lgkmcnt(4)\n"
    "ds_read_b128 a[96:99], v1 offset:53248\n"
    "v_add_f32 v7, v7, v36\n"
    "v_mfma_f32_16x16x32_bf16 v[68:71], a[100:103], a[44:47], v[68:71]\n"
    "v_exp_f32 v37, v37\n"
    "v_mfma_f32_16x16x32_bf16 v[76:79], a[100:103], a[60:63], v[76:79]\n"
    "buffer_load_dwordx4 a[32:35], v4, s[40:43], s61 offen offset:0\n"
    "buffer_load_dwordx4 a[36:39], v4, s[40:43], s61 offen offset:64\n"
    "v_mfma_f32_16x16x32_bf16 v[52:55], a[100:103], a[76:79], v[52:55]\n"
    "v_add_f32 v7, v7, v37\n"
    "v_cvt_pk_bf16_f32 v34, v36, v37\n"
    "v_mfma_f32_16x16x32_bf16 v[60:63], a[100:103], a[12:15], v[60:63]\n"
    "ds_read_b128 a[100:103], v1 offset:53760\n"
    "v_exp_f32 v38, v38\n"
    "v_exp_f32 v39, v39\n"
    "v_add_f32 v7, v7, v38\n"
    "v_add_f32 v7, v7, v39\n"
    "v_cvt_pk_bf16_f32 v35, v38, v39\n"
    "v_exp_f32 v44, v44\n"
    "s_nop 0\n"
    "v_mfma_f32_16x16x32_bf16 v[144:147], a[120:123], v[32:35], v[144:147]\n"
    "v_add_f32 v8, v8, v44\n"
    "v_exp_f32 v45, v45\n"
    "v_exp_f32 v46, v46\n"
    "v_add_f32 v8, v8, v45\n"
    "v_cvt_pk_bf16_f32 v42, v44, v45\n"
    "v_add_f32 v8, v8, v46\n"
    "v_exp_f32 v47, v47\n"
    "v_exp_f32 v20, v20\n"
    "v_add_f32 v8, v8, v47\n"
    "v_cvt_pk_bf16_f32 v43, v46, v47\n"
    "v_add_f32 v9, v9, v20\n"
    "s_nop 0\n"
    "v_mfma_f32_16x16x32_bf16 v[176:179], a[120:123], v[40:43], v[176:179]\n"
    "buffer_load_dwordx4 a[40:43], v4, s[40:43], s61 offen offset:128\n"
    "v_exp_f32 v21, v21\n"
    "v_exp_f32 v22, v22\n"
    "v_add_f32 v9, v9, v21\n"
    "v_cvt_pk_bf16_f32 v18, v20, v21\n"
    "v_add_f32 v9, v9, v22\n"
    "v_exp_f32 v23, v23\n"
    "v_exp_f32 v28, v28\n"
    "v_add_f32 v9, v9, v23\n"
    "v_cvt_pk_bf16_f32 v19, v22, v23\n"
    "v_add_f32 v5, v5, v28\n"
    "s_nop 0\n"
    "v_mfma_f32_16x16x32_bf16 v[208:211], a[120:123], v[16:19], v[208:211]\n"
    "buffer_load_dwordx4 a[44:47], v4, s[40:43], s61 offen offset:192\n"
    "v_exp_f32 v29, v29\n"
    "s_mov_b32 s61, 147456\n"
    "v_add_f32 v5, v5, v29\n"
    "v_cvt_pk_bf16_f32 v26, v28, v29\n"
    "v_exp_f32 v30, v30\n"
    "v_exp_f32 v31, v31\n"
    "v_add_f32 v5, v5, v30\n"
    "v_add_f32 v5, v5, v31\n"
    "v_cvt_pk_bf16_f32 v27, v30, v31\n"
    "v_exp_f32 v64, v64\n"
    "s_nop 0\n"
    "v_mfma_f32_16x16x32_bf16 v[80:83], a[120:123], v[24:27], v[80:83]\n"
    "buffer_store_dwordx4 v[112:115], v4, s[52:55], s63 offen offset:0 sc0 sc1\n"
    "ds_read_b64_tr_b16 a[120:121], v3 offset:1536\n"
    "v_mfma_f32_16x16x32_bf16 v[148:151], a[124:127], v[32:35], v[148:151]\n"
    "ds_read_b64_tr_b16 a[122:123], v3 offset:5632\n"
    "v_add_f32 v7, v7, v64\n"
    "v_mfma_f32_16x16x32_bf16 v[180:183], a[124:127], v[40:43], v[180:183]\n"
    "v_exp_f32 v65, v65\n"
    "v_mfma_f32_16x16x32_bf16 v[212:215], a[124:127], v[16:19], v[212:215]\n"
    "v_add_f32 v7, v7, v65\n"
    "v_cvt_pk_bf16_f32 v64, v64, v65\n"
    "v_mfma_f32_16x16x32_bf16 v[84:87], a[124:127], v[24:27], v[84:87]\n"
    "v_exp_f32 v66, v66\n"
    "v_mfma_f32_16x16x32_bf16 v[152:155], a[104:107], v[32:35], v[152:155]\n"
    "ds_read_b64_tr_b16 a[124:125], v3 offset:1544\n"
    "ds_read_b64_tr_b16 a[126:127], v3 offset:5640\n"
    "v_mfma_f32_16x16x32_bf16 v[184:187], a[104:107], v[40:43], v[184:187]\n"
    "buffer_store_dwordx4 v[120:123], v4, s[52:55], s63 offen offset:64 sc0 sc1\n"
    "v_add_f32 v7, v7, v66\n"
    "v_mfma_f32_16x16x32_bf16 v[216:219], a[104:107], v[16:19], v[216:219]\n"
    "v_exp_f32 v67, v67\n"
    "v_mfma_f32_16x16x32_bf16 v[88:91], a[104:107], v[24:27], v[88:91]\n"
    "ds_read_b64_tr_b16 a[104:105], v3 offset:8192\n"
    "ds_read_b64_tr_b16 a[106:107], v3 offset:12288\n"
    "v_mfma_f32_16x16x32_bf16 v[156:159], a[108:111], v[32:35], v[156:159]\n"
    "v_add_f32 v7, v7, v67\n"
    "v_cvt_pk_bf16_f32 v65, v66, v67\n"
    "v_mfma_f32_16x16x32_bf16 v[188:191], a[108:111], v[40:43], v[188:191]\n"
    "v_exp_f32 v72, v72\n"
    "v_mfma_f32_16x16x32_bf16 v[220:223], a[108:111], v[16:19], v[220:223]\n"
    "v_add_f32 v8, v8, v72\n"
    "v_mfma_f32_16x16x32_bf16 v[92:95], a[108:111], v[24:27], v[92:95]\n"
    "buffer_store_dwordx4 v[128:131], v4, s[52:55], s63 offen offset:128 sc0 sc1\n"
    "ds_read_b64_tr_b16 a[108:109], v3 offset:8200\n"
    "v_mfma_f32_16x16x32_bf16 v[160:163], a[112:115], v[32:35], v[160:163]\n"
    "v_exp_f32 v73, v73\n"
    "v_mfma_f32_16x16x32_bf16 v[192:195], a[112:115], v[40:43], v[192:195]\n"
    "ds_read_b64_tr_b16 a[110:111], v3 offset:12296\n"
    "v_add_f32 v8, v8, v73\n"
    "v_mfma_f32_16x16x32_bf16 v[224:227], a[112:115], v[16:19], v[224:227]\n"
    "v_cvt_pk_bf16_f32 v72, v72, v73\n"
    "v_mfma_f32_16x16x32_bf16 v[96:99], a[112:115], v[24:27], v[96:99]\n"
    "v_exp_f32 v74, v74\n"
    "v_mfma_f32_16x16x32_bf16 v[164:167], a[116:119], v[32:35], v[164:167]\n"
    "ds_read_b64_tr_b16 a[112:113], v3 offset:8704\n"
    "ds_read_b64_tr_b16 a[114:115], v3 offset:12800\n"
    "v_mfma_f32_16x16x32_bf16 v[196:199], a[116:119], v[40:43], v[196:199]\n"
    "buffer_store_dwordx4 v[136:139], v4, s[52:55], s63 offen offset:192 sc0 sc1\n"
    "v_add_f32 v8, v8, v74\n"
    "v_mfma_f32_16x16x32_bf16 v[228:231], a[116:119], v[16:19], v[228:231]\n"
    "v_exp_f32 v75, v75\n"
    "v_mfma_f32_16x16x32_bf16 v[100:103], a[116:119], v[24:27], v[100:103]\n"
    "s_waitcnt lgkmcnt(8)\n"
    "ds_read_b64_tr_b16 a[116:117], v3 offset:8712\n"
    "ds_read_b64_tr_b16 a[118:119], v3 offset:12808\n"
    "v_mfma_f32_16x16x32_bf16 v[168:171], a[120:123], v[32:35], v[168:171]\n"
    "v_add_f32 v8, v8, v75\n"
    "v_cvt_pk_bf16_f32 v73, v74, v75\n"
    "v_mfma_f32_16x16x32_bf16 v[200:203], a[120:123], v[40:43], v[200:203]\n"
    "v_exp_f32 v48, v48\n"
    "v_mfma_f32_16x16x32_bf16 v[232:235], a[120:123], v[16:19], v[232:235]\n"
    "v_add_f32 v9, v9, v48\n"
    "v_mfma_f32_16x16x32_bf16 v[104:107], a[120:123], v[24:27], v[104:107]\n"
    "s_waitcnt lgkmcnt(8)\n"
    "v_exp_f32 v49, v49\n"
    "v_mfma_f32_16x16x32_bf16 v[172:175], a[124:127], v[32:35], v[172:175]\n"
    "ds_read_b64_tr_b16 a[120:121], v3 offset:9216\n"
    "ds_read_b64_tr_b16 a[122:123], v3 offset:13312\n"
    "v_mfma_f32_16x16x32_bf16 v[204:207], a[124:127], v[40:43], v[204:207]\n"
    "v_add_f32 v9, v9, v49\n"
    "v_cvt_pk_bf16_f32 v48, v48, v49\n"
    "v_mfma_f32_16x16x32_bf16 v[236:239], a[124:127], v[16:19], v[236:239]\n"
    "v_exp_f32 v50, v50\n"
    "v_mfma_f32_16x16x32_bf16 v[108:111], a[124:127], v[24:27], v[108:111]\n"
    "ds_read_b64_tr_b16 a[124:125], v3 offset:9224\n"
    "ds_read_b64_tr_b16 a[126:127], v3 offset:13320\n"
    "v_mfma_f32_16x16x32_bf16 v[40:43], a[80:83], a[48:51], v[252:255]\n"
    "v_add_f32 v9, v9, v50\n"
    "v_mfma_f32_16x16x32_bf16 v[16:19], a[80:83], a[64:67], v[240:243]\n"
    "v_exp_f32 v51, v51\n"
    "v_mfma_f32_16x16x32_bf16 v[24:27], a[80:83], a[0:3], v[244:247]\n"
    "s_waitcnt vmcnt(16)\n"
    "v_add_f32 v9, v9, v51\n"
    "v_cvt_pk_bf16_f32 v49, v50, v51\n"
    "v_mfma_f32_16x16x32_bf16 v[32:35], a[80:83], a[16:19], 0\n"
    "v_exp_f32 v56, v56\n"
    "v_mfma_f32_16x16x32_bf16 v[40:43], a[84:87], a[52:55], v[40:43]\n"
    "ds_read_b128 a[80:83], v1 offset:54272\n"
    "v_add_f32 v5, v5, v56\n"
    "v_mfma_f32_16x16x32_bf16 v[16:19], a[84:87], a[68:71], v[16:19]\n"
    "v_exp_f32 v57, v57\n"
    "v_mfma_f32_16x16x32_bf16 v[24:27], a[84:87], a[4:7], v[24:27]\n"
    "s_waitcnt vmcnt(15)\n"
    "v_add_f32 v5, v5, v57\n"
    "v_cvt_pk_bf16_f32 v56, v56, v57\n"
    "v_mfma_f32_16x16x32_bf16 v[32:35], a[84:87], a[20:23], v[32:35]\n"
    "v_exp_f32 v58, v58\n"
    "v_mfma_f32_16x16x32_bf16 v[40:43], a[88:91], a[56:59], v[40:43]\n"
    "ds_read_b128 a[84:87], v1 offset:54784\n"
    "v_add_f32 v5, v5, v58\n"
    "v_mfma_f32_16x16x32_bf16 v[16:19], a[88:91], a[72:75], v[16:19]\n"
    "v_exp_f32 v59, v59\n"
    "v_mfma_f32_16x16x32_bf16 v[24:27], a[88:91], a[8:11], v[24:27]\n"
    "s_waitcnt vmcnt(14)\n"
    "v_add_f32 v5, v5, v59\n"
    "v_cvt_pk_bf16_f32 v57, v58, v59\n"
    "v_mfma_f32_16x16x32_bf16 v[32:35], a[88:91], a[24:27], v[32:35]\n"
    "v_exp_f32 v68, v68\n"
    "v_mfma_f32_16x16x32_bf16 v[40:43], a[92:95], a[60:63], v[40:43]\n"
    "ds_read_b128 a[88:91], v1 offset:57344\n"
    "v_add_f32 v7, v7, v68\n"
    "v_mfma_f32_16x16x32_bf16 v[16:19], a[92:95], a[76:79], v[16:19]\n"
    "v_exp_f32 v69, v69\n"
    "v_mfma_f32_16x16x32_bf16 v[24:27], a[92:95], a[12:15], v[24:27]\n"
    "s_waitcnt vmcnt(13)\n"
    "v_add_f32 v7, v7, v69\n"
    "v_cvt_pk_bf16_f32 v66, v68, v69\n"
    "v_mfma_f32_16x16x32_bf16 v[32:35], a[92:95], a[28:31], v[32:35]\n"
    "v_exp_f32 v70, v70\n"
    "v_mfma_f32_16x16x32_bf16 v[44:47], a[96:99], a[48:51], v[252:255]\n"
    "ds_read_b128 a[92:95], v1 offset:57856\n"
    "v_add_f32 v7, v7, v70\n"
    "v_mfma_f32_16x16x32_bf16 v[20:23], a[96:99], a[64:67], v[240:243]\n"
    "v_exp_f32 v71, v71\n"
    "v_mfma_f32_16x16x32_bf16 v[28:31], a[96:99], a[0:3], v[244:247]\n"
    "v_add_f32 v7, v7, v71\n"
    "v_cvt_pk_bf16_f32 v67, v70, v71\n"
    "v_mfma_f32_16x16x32_bf16 v[36:39], a[96:99], a[16:19], 0\n"
    "v_exp_f32 v76, v76\n"
    "v_mfma_f32_16x16x32_bf16 v[44:47], a[100:103], a[52:55], v[44:47]\n"
    "ds_read_b128 a[96:99], v1 offset:58368\n"
    "v_add_f32 v8, v8, v76\n"
    "v_mfma_f32_16x16x32_bf16 v[20:23], a[100:103], a[68:71], v[20:23]\n"
    "v_exp_f32 v77, v77\n"
    "v_mfma_f32_16x16x32_bf16 v[28:31], a[100:103], a[4:7], v[28:31]\n"
    "v_add_f32 v8, v8, v77\n"
    "v_cvt_pk_bf16_f32 v74, v76, v77\n"
    "v_mfma_f32_16x16x32_bf16 v[36:39], a[100:103], a[20:23], v[36:39]\n"
    "s_waitcnt lgkmcnt(4)\n"
    "v_exp_f32 v78, v78\n"
    "v_mfma_f32_16x16x32_bf16 v[44:47], a[80:83], a[56:59], v[44:47]\n"
    "ds_read_b128 a[100:103], v1 offset:58880\n"
    "v_add_f32 v8, v8, v78\n"
    "v_mfma_f32_16x16x32_bf16 v[20:23], a[80:83], a[72:75], v[20:23]\n"
    "v_exp_f32 v79, v79\n"
    "v_mfma_f32_16x16x32_bf16 v[28:31], a[80:83], a[8:11], v[28:31]\n"
    "v_add_f32 v8, v8, v79\n"
    "v_cvt_pk_bf16_f32 v75, v78, v79\n"
    "v_mfma_f32_16x16x32_bf16 v[36:39], a[80:83], a[24:27], v[36:39]\n"
    "s_waitcnt lgkmcnt(4)\n"
    "v_exp_f32 v52, v52\n"
    "v_mfma_f32_16x16x32_bf16 v[44:47], a[84:87], a[60:63], v[44:47]\n"
    "ds_read_b128 a[80:83], v1 offset:61440\n"
    "v_add_f32 v9, v9, v52\n"
    "v_mfma_f32_16x16x32_bf16 v[20:23], a[84:87], a[76:79], v[20:23]\n"
    "v_exp_f32 v53, v53\n"
    "v_mfma_f32_16x16x32_bf16 v[28:31], a[84:87], a[12:15], v[28:31]\n"
    "v_add_f32 v9, v9, v53\n"
    "v_cvt_pk_bf16_f32 v50, v52, v53\n"
    "v_mfma_f32_16x16x32_bf16 v[36:39], a[84:87], a[28:31], v[36:39]\n"
    "v_exp_f32 v54, v54\n"
    "v_mfma_f32_16x16x32_bf16 v[144:147], a[104:107], v[64:67], v[144:147]\n"
    "ds_read_b128 a[84:87], v1 offset:61952\n"
    "v_add_f32 v9, v9, v54\n"
    "v_mfma_f32_16x16x32_bf16 v[176:179], a[104:107], v[72:75], v[176:179]\n"
    "v_exp_f32 v55, v55\n"
    "v_max3_f32 v10, v32, v33, v34\n"
    "v_add_f32 v9, v9, v55\n"
    "v_cvt_pk_bf16_f32 v51, v54, v55\n"
    "v_max3_f32 v11, v36, v37, v38\n"
    "v_max3_f32 v10, v10, v35, v39\n"
    "v_mfma_f32_16x16x32_bf16 v[208:211], a[104:107], v[48:51], v[208:211]\n"
    "v_max_f32 v10, v10, v11\n"
    "v_mov_b32 v11, v10\n"
    "v_exp_f32 v60, v60\n"
    "s_nop 0\n"
    "v_permlane32_swap_b32 v10, v11\n"
    "v_add_f32 v5, v5, v60\n"
    "v_max_f32 v10, v10, v11\n"
    "v_mov_b32 v11, v10\n"
    "v_exp_f32 v61, v61\n"
    "s_nop 0\n"
    "v_permlane16_swap_b32 v10, v11\n"
    "v_add_f32 v5, v5, v61\n"
    "v_min_f32 v248, -v10, -v11\n"
    "v_min_f32 v249, -v10, -v11\n"
    "v_min_f32 v250, -v10, -v11\n"
    "v_min_f32 v251, -v10, -v11\n"
    "v_add_f32 v32, v32, v248\n"
    "v_add_f32 v33, v33, v248\n"
    "v_add_f32 v34, v34, v248\n"
    "v_add_f32 v35, v35, v248\n"
    "v_add_f32 v36, v36, v248\n"
    "v_add_f32 v37, v37, v248\n"
    "v_add_f32 v38, v38, v248\n"
    "v_add_f32 v39, v39, v248\n"
    "v_cvt_pk_bf16_f32 v58, v60, v61\n"
    "v_exp_f32 v62, v62\n"
    "v_exp_f32 v63, v63\n"
    "v_add_f32 v5, v5, v62\n"
    "v_add_f32 v5, v5, v63\n"
    "v_cvt_pk_bf16_f32 v59, v62, v63\n"
    "v_exp_f32 v40, v40\n"
    "s_nop 0\n"
    "v_mfma_f32_16x16x32_bf16 v[80:83], a[104:107], v[56:59], v[80:83]\n"
    "ds_read_b64_tr_b16 a[104:105], v3 offset:9728\n"
    "ds_read_b64_tr_b16 a[106:107], v3 offset:13824\n"
    "v_mfma_f32_16x16x32_bf16 v[148:151], a[108:111], v[64:67], v[148:151]\n"
    "v_add_f32 v8, v8, v40\n"
    "v_mov_b32 v12, v7\n"
    "v_mfma_f32_16x16x32_bf16 v[180:183], a[108:111], v[72:75], v[180:183]\n"
    "s_mov_b32 s62, 32768\n"
    "v_exp_f32 v41, v41\n"
    "v_mfma_f32_16x16x32_bf16 v[212:215], a[108:111], v[48:51], v[212:215]\n"
    "v_add_f32 v8, v8, v41\n"
    "v_cvt_pk_bf16_f32 v40, v40, v41\n"
    "v_mfma_f32_16x16x32_bf16 v[84:87], a[108:111], v[56:59], v[84:87]\n"
    "v_exp_f32 v42, v42\n"
    "v_mfma_f32_16x16x32_bf16 v[152:155], a[112:115], v[64:67], v[152:155]\n"
    "ds_read_b64_tr_b16 a[108:109], v3 offset:9736\n"
    "ds_read_b64_tr_b16 a[110:111], v3 offset:13832\n"
    "v_mfma_f32_16x16x32_bf16 v[184:187], a[112:115], v[72:75], v[184:187]\n"
    "v_add_f32 v8, v8, v42\n"
    "v_permlane32_swap_b32 v7, v12\n"
    "v_mfma_f32_16x16x32_bf16 v[216:219], a[112:115], v[48:51], v[216:219]\n"
    "v_exp_f32 v43, v43\n"
    "v_mfma_f32_16x16x32_bf16 v[88:91], a[112:115], v[56:59], v[88:91]\n"
    "ds_read_b64_tr_b16 a[112:113], v3 offset:16384\n"
    "ds_read_b64_tr_b16 a[114:115], v3 offset:20480\n"
    "v_mfma_f32_16x16x32_bf16 v[156:159], a[116:119], v[64:67], v[156:159]\n"
    "v_add_f32 v8, v8, v43\n"
    "v_cvt_pk_bf16_f32 v41, v42, v43\n"
    "v_mfma_f32_16x16x32_bf16 v[188:191], a[116:119], v[72:75], v[188:191]\n"
    "v_exp_f32 v16, v16\n"
    "v_mfma_f32_16x16x32_bf16 v[220:223], a[116:119], v[48:51], v[220:223]\n"
    "v_add_f32 v9, v9, v16\n"
    "v_add_f32 v7, v7, v12\n"
    "v_mfma_f32_16x16x32_bf16 v[92:95], a[116:119], v[56:59], v[92:95]\n"
    "v_exp_f32 v17, v17\n"
    "v_mfma_f32_16x16x32_bf16 v[160:163], a[120:123], v[64:67], v[160:163]\n"
    "ds_read_b64_tr_b16 a[116:117], v3 offset:16392\n"
    "ds_read_b64_tr_b16 a[118:119], v3 offset:20488\n"
    "v_mfma_f32_16x16x32_bf16 v[192:195], a[120:123], v[72:75], v[192:195]\n"
    "v_add_f32 v9, v9, v17\n"
    "v_cvt_pk_bf16_f32 v16, v16, v17\n"
    "v_mfma_f32_16x16x32_bf16 v[224:227], a[120:123], v[48:51], v[224:227]\n"
    "v_exp_f32 v18, v18\n"
    "v_mfma_f32_16x16x32_bf16 v[96:99], a[120:123], v[56:59], v[96:99]\n"
    "ds_read_b64_tr_b16 a[120:121], v3 offset:16896\n"
    "ds_read_b64_tr_b16 a[122:123], v3 offset:20992\n"
    "v_mfma_f32_16x16x32_bf16 v[164:167], a[124:127], v[64:67], v[164:167]\n"
    "v_add_f32 v9, v9, v18\n"
    "v_mov_b32 v12, v7\n"
    "v_mfma_f32_16x16x32_bf16 v[196:199], a[124:127], v[72:75], v[196:199]\n"
    "v_exp_f32 v19, v19\n"
    "v_mfma_f32_16x16x32_bf16 v[228:231], a[124:127], v[48:51], v[228:231]\n"
    "v_add_f32 v9, v9, v19\n"
    "v_cvt_pk_bf16_f32 v17, v18, v19\n"
    "v_mfma_f32_16x16x32_bf16 v[100:103], a[124:127], v[56:59], v[100:103]\n"
    "s_waitcnt lgkmcnt(8)\n"
    "v_exp_f32 v24, v24\n"
    "v_mfma_f32_16x16x32_bf16 v[168:171], a[104:107], v[64:67], v[168:171]\n"
    "ds_read_b64_tr_b16 a[124:125], v3 offset:16904\n"
    "ds_read_b64_tr_b16 a[126:127], v3 offset:21000\n"
    "v_mfma_f32_16x16x32_bf16 v[200:203], a[104:107], v[72:75], v[200:203]\n"
    "v_add_f32 v5, v5, v24\n"
    "v_permlane16_swap_b32 v7, v12\n"
    "v_mfma_f32_16x16x32_bf16 v[232:235], a[104:107], v[48:51], v[232:235]\n"
    "v_exp_f32 v25, v25\n"
    "v_mfma_f32_16x16x32_bf16 v[104:107], a[104:107], v[56:59], v[104:107]\n"
    "s_waitcnt lgkmcnt(8)\n"
    "ds_read_b64_tr_b16 a[104:105], v3 offset:17408\n"
    "ds_read_b64_tr_b16 a[106:107], v3 offset:21504\n"
    "v_mfma_f32_16x16x32_bf16 v[172:175], a[108:111], v[64:67], v[172:175]\n"
    "v_add_f32 v5, v5, v25\n"
    "v_cvt_pk_bf16_f32 v24, v24, v25\n"
    "v_mfma_f32_16x16x32_bf16 v[204:207], a[108:111], v[72:75], v[204:207]\n"
    "v_exp_f32 v26, v26\n"
    "v_mfma_f32_16x16x32_bf16 v[236:239], a[108:111], v[48:51], v[236:239]\n"
    "v_add_f32 v5, v5, v26\n"
    "v_add_f32 v12, v7, v12\n"
    "v_mfma_f32_16x16x32_bf16 v[108:111], a[108:111], v[56:59], v[108:111]\n"
    "v_exp_f32 v27, v27\n"
    "v_mfma_f32_16x16x32_bf16 v[72:75], a[88:91], a[48:51], v[252:255]\n"
    "ds_read_b64_tr_b16 a[108:109], v3 offset:17416\n"
    "ds_read_b64_tr_b16 a[110:111], v3 offset:21512\n"
    "v_mfma_f32_16x16x32_bf16 v[48:51], a[88:91], a[64:67], v[240:243]\n"
    "v_add_f32 v5, v5, v27\n"
    "v_cvt_pk_bf16_f32 v25, v26, v27\n"
    "v_mfma_f32_16x16x32_bf16 v[56:59], a[88:91], a[0:3], v[244:247]\n"
    "v_exp_f32 v32, v32\n"
    "v_mfma_f32_16x16x32_bf16 v[64:67], a[88:91], a[16:19], v[248:251]\n"
    "v_exp_f32 v33, v33\n"
    "v_mfma_f32_16x16x32_bf16 v[72:75], a[92:95], a[52:55], v[72:75]\n"
    "ds_read_b128 a[88:91], v1 offset:62464\n"
    "v_add_f32 v6, v32, v33\n"
    "v_mfma_f32_16x16x32_bf16 v[48:51], a[92:95], a[68:71], v[48:51]\n"
    "v_cvt_pk_bf16_f32 v32, v32, v33\n"
    "v_cmp_ngt_f32 vcc, 0x5d800000, v12\n"
    "v_mfma_f32_16x16x32_bf16 v[56:59], a[92:95], a[4:7], v[56:59]\n"
    "v_exp_f32 v34, v34\n"
    "v_mfma_f32_16x16x32_bf16 v[64:67], a[92:95], a[20:23], v[64:67]\n"
    "s_or_b64 s[64:65], s[64:65], vcc\n"
    "ds_read_b128 a[92:95], v1 offset:62976\n"
    "v_add_f32 v6, v6, v34\n"
    "v_mfma_f32_16x16x32_bf16 v[72:75], a[96:99], a[56:59], v[72:75]\n"
    "v_exp_f32 v35, v35\n"
    "v_mfma_f32_16x16x32_bf16 v[48:51], a[96:99], a[72:75], v[48:51]\n"
    "v_add_f32 v6, v6, v35\n"
    "v_cvt_pk_bf16_f32 v33, v34, v35\n"
    "v_mfma_f32_16x16x32_bf16 v[56:59], a[96:99], a[8:11], v[56:59]\n"
    "v_exp_f32 v44, v44\n"
    "v_mfma_f32_16x16x32_bf16 v[64:67], a[96:99], a[24:27], v[64:67]\n"
    "ds_read_b128 a[96:99], v2 offset:0\n"
    "v_add_f32 v8, v8, v44\n"
    "v_mfma_f32_16x16x32_bf16 v[72:75], a[100:103], a[60:63], v[72:75]\n"
    "v_exp_f32 v45, v45\n"
    "v_mfma_f32_16x16x32_bf16 v[48:51], a[100:103], a[76:79], v[48:51]\n"
    "v_add_f32 v8, v8, v45\n"
    "v_cvt_pk_bf16_f32 v42, v44, v45\n"
    "v_mfma_f32_16x16x32_bf16 v[56:59], a[100:103], a[12:15], v[56:59]\n"
    "v_exp_f32 v46, v46\n"
    "v_mfma_f32_16x16x32_bf16 v[64:67], a[100:103], a[28:31], v[64:67]\n"
    "ds_read_b128 a[100:103], v2 offset:512\n"
    "v_add_f32 v8, v8, v46\n"
    "v_mfma_f32_16x16x32_bf16 v[76:79], a[80:83], a[48:51], v[252:255]\n"
    "v_exp_f32 v47, v47\n"
    "v_mfma_f32_16x16x32_bf16 v[52:55], a[80:83], a[64:67], v[240:243]\n"
    "v_add_f32 v8, v8, v47\n"
    "v_cvt_pk_bf16_f32 v43, v46, v47\n"
    "v_mfma_f32_16x16x32_bf16 v[60:63], a[80:83], a[0:3], v[244:247]\n"
    "v_exp_f32 v20, v20\n"
    "v_mfma_f32_16x16x32_bf16 v[68:71], a[80:83], a[16:19], v[248:251]\n"
    "ds_read_b128 a[80:83], v2 offset:1024\n"
    "v_add_f32 v9, v9, v20\n"
    "v_mfma_f32_16x16x32_bf16 v[76:79], a[84:87], a[52:55], v[76:79]\n"
    "v_exp_f32 v21, v21\n"
    "v_mfma_f32_16x16x32_bf16 v[52:55], a[84:87], a[68:71], v[52:55]\n"
    "v_add_f32 v9, v9, v21\n"
    "v_cvt_pk_bf16_f32 v18, v20, v21\n"
    "v_mfma_f32_16x16x32_bf16 v[60:63], a[84:87], a[4:7], v[60:63]\n"
    "v_exp_f32 v22, v22\n"
    "v_mfma_f32_16x16x32_bf16 v[68:71], a[84:87], a[20:23], v[68:71]\n"
    "s_waitcnt lgkmcnt(4)\n"
    "ds_read_b128 a[84:87], v2 offset:1536\n"
    "v_add_f32 v9, v9, v22\n"
    "v_mfma_f32_16x16x32_bf16 v[76:79], a[88:91], a[56:59], v[76:79]\n"
    "v_exp_f32 v23, v23\n"
    "v_mfma_f32_16x16x32_bf16 v[52:55], a[88:91], a[72:75], v[52:55]\n"
    "v_add_f32 v9, v9, v23\n"
    "v_cvt_pk_bf16_f32 v19, v22, v23\n"
    "v_mfma_f32_16x16x32_bf16 v[60:63], a[88:91], a[8:11], v[60:63]\n"
    "v_exp_f32 v28, v28\n"
    "v_mfma_f32_16x16x32_bf16 v[68:71], a[88:91], a[24:27], v[68:71]\n"
    "s_waitcnt lgkmcnt(4)\n"
    "ds_read_b128 a[88:91], v2 offset:4096\n"
    "v_add_f32 v5, v5, v28\n"
    "v_mfma_f32_16x16x32_bf16 v[76:79], a[92:95], a[60:63], v[76:79]\n"
    "v_exp_f32 v29, v29\n"
    "v_mfma_f32_16x16x32_bf16 v[52:55], a[92:95], a[76:79], v[52:55]\n"
    "v_add_f32 v5, v5, v29\n"
    "v_cvt_pk_bf16_f32 v26, v28, v29\n"
    "v_mfma_f32_16x16x32_bf16 v[60:63], a[92:95], a[12:15], v[60:63]\n"
    "v_exp_f32 v30, v30\n"
    "v_mfma_f32_16x16x32_bf16 v[68:71], a[92:95], a[28:31], v[68:71]\n"
    "ds_read_b128 a[92:95], v2 offset:4608\n"
    "v_add_f32 v5, v5, v30\n"
    "v_mfma_f32_16x16x32_bf16 v[176:179], a[112:115], v[40:43], v[176:179]\n"
    "v_exp_f32 v31, v31\n"
    "v_mfma_f32_16x16x32_bf16 v[208:211], a[112:115], v[16:19], v[208:211]\n"
    "v_add_f32 v5, v5, v31\n"
    "v_cvt_pk_bf16_f32 v27, v30, v31\n"
    "v_exp_f32 v36, v36\n"
    "s_nop 0\n"
    "v_mfma_f32_16x16x32_bf16 v[80:83], a[112:115], v[24:27], v[80:83]\n"
    "v_add_f32 v6, v6, v36\n"
    "v_exp_f32 v37, v37\n"
    "v_exp_f32 v38, v38\n"
    "v_add_f32 v6, v6, v37\n"
    "v_cvt_pk_bf16_f32 v34, v36, v37\n"
    "v_add_f32 v6, v6, v38\n"
    "v_exp_f32 v39, v39\n"
    "v_rcp_f32 v12, v12\n"
    "v_add_f32 v6, v6, v39\n"
    "v_cvt_pk_bf16_f32 v35, v38, v39\n"
    "v_mul_f32 v144, v144, v12\n"
    "v_mul_f32 v145, v145, v12\n"
    "v_mfma_f32_16x16x32_bf16 v[112:115], a[112:115], v[32:35], 0\n"
    "ds_read_b64_tr_b16 a[112:113], v3 offset:17920\n"
    "ds_read_b64_tr_b16 a[114:115], v3 offset:22016\n"
    "v_mfma_f32_16x16x32_bf16 v[180:183], a[116:119], v[40:43], v[180:183]\n"
    "v_mul_f32 v146, v146, v12\n"
    "v_mul_f32 v147, v147, v12\n"
    "v_mfma_f32_16x16x32_bf16 v[212:215], a[116:119], v[16:19], v[212:215]\n"
    "v_mul_f32 v148, v148, v12\n"
    "v_mul_f32 v149, v149, v12\n"
    "v_mfma_f32_16x16x32_bf16 v[84:87], a[116:119], v[24:27], v[84:87]\n"
    "v_mul_f32 v150, v150, v12\n"
    "v_mul_f32 v151, v151, v12\n"
    "v_mfma_f32_16x16x32_bf16 v[116:119], a[116:119], v[32:35], 0\n"
    "ds_read_b64_tr_b16 a[116:117], v3 offset:17928\n"
    "ds_read_b64_tr_b16 a[118:119], v3 offset:22024\n"
    "v_mfma_f32_16x16x32_bf16 v[184:187], a[120:123], v[40:43], v[184:187]\n"
    "v_cvt_pk_bf16_f32 v144, v144, v145\n"
    "v_cvt_pk_bf16_f32 v145, v146, v147\n"
    "v_mfma_f32_16x16x32_bf16 v[216:219], a[120:123], v[16:19], v[216:219]\n"
    "v_cvt_pk_bf16_f32 v146, v148, v149\n"
    "v_cvt_pk_bf16_f32 v147, v150, v151\n"
    "v_mfma_f32_16x16x32_bf16 v[88:91], a[120:123], v[24:27], v[88:91]\n"
    "v_mul_f32 v152, v152, v12\n"
    "v_mul_f32 v153, v153, v12\n"
    "v_mfma_f32_16x16x32_bf16 v[120:123], a[120:123], v[32:35], 0\n"
    "ds_read_b64_tr_b16 a[120:121], v3 offset:24576\n"
    "ds_read_b64_tr_b16 a[122:123], v3 offset:28672\n"
    "v_mfma_f32_16x16x32_bf16 v[188:191], a[124:127], v[40:43], v[188:191]\n"
    "v_mul_f32 v154, v154, v12\n"
    "v_mul_f32 v155, v155, v12\n"
    "v_mfma_f32_16x16x32_bf16 v[220:223], a[124:127], v[16:19], v[220:223]\n"
    "v_mul_f32 v156, v156, v12\n"
    "v_mul_f32 v157, v157, v12\n"
    "v_mfma_f32_16x16x32_bf16 v[92:95], a[124:127], v[24:27], v[92:95]\n"
    "v_mul_f32 v158, v158, v12\n"
    "v_mul_f32 v159, v159, v12\n"
    "v_mfma_f32_16x16x32_bf16 v[124:127], a[124:127], v[32:35], 0\n"
    "ds_read_b64_tr_b16 a[124:125], v3 offset:24584\n"
    "ds_read_b64_tr_b16 a[126:127], v3 offset:28680\n"
    "v_mfma_f32_16x16x32_bf16 v[192:195], a[104:107], v[40:43], v[192:195]\n"
    "v_cvt_pk_bf16_f32 v152, v152, v153\n"
    "v_cvt_pk_bf16_f32 v153, v154, v155\n"
    "v_mfma_f32_16x16x32_bf16 v[224:227], a[104:107], v[16:19], v[224:227]\n"
    "v_cvt_pk_bf16_f32 v154, v156, v157\n"
    "v_cvt_pk_bf16_f32 v155, v158, v159\n"
    "v_mfma_f32_16x16x32_bf16 v[96:99], a[104:107], v[24:27], v[96:99]\n"
    "v_exp_f32 v72, v72\n"
    "v_mfma_f32_16x16x32_bf16 v[128:131], a[104:107], v[32:35], 0\n"
    "ds_read_b64_tr_b16 a[104:105], v3 offset:25088\n"
    "ds_read_b64_tr_b16 a[106:107], v3 offset:29184\n"
    "v_mfma_f32_16x16x32_bf16 v[196:199], a[108:111], v[40:43], v[196:199]\n"
    "v_add_f32 v8, v8, v72\n"
    "v_mul_f32 v160, v160, v12\n"
    "v_mfma_f32_16x16x32_bf16 v[228:231], a[108:111], v[16:19], v[228:231]\n"
    "v_exp_f32 v73, v73\n"
    "v_mfma_f32_16x16x32_bf16 v[100:103], a[108:111], v[24:27], v[100:103]\n"
    "v_add_f32 v8, v8, v73\n"
    "v_cvt_pk_bf16_f32 v72, v72, v73\n"
    "v_mfma_f32_16x16x32_bf16 v[132:135], a[108:111], v[32:35], 0\n"
    "s_waitcnt lgkmcnt(8)\n"
    "v_exp_f32 v74, v74\n"
    "v_mfma_f32_16x16x32_bf16 v[200:203], a[112:115], v[40:43], v[200:203]\n"
    "ds_read_b64_tr_b16 a[108:109], v3 offset:25096\n"
    "ds_read_b64_tr_b16 a[110:111], v3 offset:29192\n"
    "v_mfma_f32_16x16x32_bf16 v[232:235], a[112:115], v[16:19], v[232:235]\n"
    "v_add_f32 v8, v8, v74\n"
    "v_mul_f32 v161, v161, v12\n"
    "v_mfma_f32_16x16x32_bf16 v[104:107], a[112:115], v[24:27], v[104:107]\n"
    "v_exp_f32 v75, v75\n"
    "v_mfma_f32_16x16x32_bf16 v[136:139], a[112:115], v[32:35], 0\n"
    "s_waitcnt lgkmcnt(8)\n"
    "ds_read_b64_tr_b16 a[112:113], v3 offset:25600\n"
    "ds_read_b64_tr_b16 a[114:115], v3 offset:29696\n"
    "v_mfma_f32_16x16x32_bf16 v[204:207], a[116:119], v[40:43], v[204:207]\n"
    "v_add_f32 v8, v8, v75\n"
    "v_cvt_pk_bf16_f32 v73, v74, v75\n"
    "v_mfma_f32_16x16x32_bf16 v[236:239], a[116:119], v[16:19], v[236:239]\n"
    "v_mul_f32 v162, v162, v12\n"
    "v_mul_f32 v163, v163, v12\n"
    "v_mfma_f32_16x16x32_bf16 v[108:111], a[116:119], v[24:27], v[108:111]\n"
    "v_mul_f32 v164, v164, v12\n"
    "v_mul_f32 v165, v165, v12\n"
    "v_mfma_f32_16x16x32_bf16 v[140:143], a[116:119], v[32:35], 0\n"
    "s_mov_b32 s77, 28672\n"
    "ds_read_b64_tr_b16 a[116:117], v3 offset:25608\n"
    "ds_read_b64_tr_b16 a[118:119], v3 offset:29704\n"
    "v_mfma_f32_16x16x32_bf16 v[40:43], a[96:99], a[48:51], v[252:255]\n"
    "buffer_load_dword v15, v14, s[72:75], s77 offen\n"
    "v_mul_f32 v166, v166, v12\n"
    "v_mfma_f32_16x16x32_bf16 v[16:19], a[96:99], a[64:67], v[240:243]\n"
    "v_mul_f32 v167, v167, v12\n"
    "v_cvt_pk_bf16_f32 v160, v160, v161\n"
    "v_mfma_f32_16x16x32_bf16 v[24:27], a[96:99], a[0:3], v[244:247]\n"
    "v_cvt_pk_bf16_f32 v161, v162, v163\n"
    "v_cvt_pk_bf16_f32 v162, v164, v165\n"
    "v_mfma_f32_16x16x32_bf16 v[32:35], a[96:99], a[16:19], v[248:251]\n"
    "ds_read_b128 a[96:99], v2 offset:5120\n"
    "v_cvt_pk_bf16_f32 v163, v166, v167\n"
    "v_mfma_f32_16x16x32_bf16 v[40:43], a[100:103], a[52:55], v[40:43]\n"
    "v_mul_f32 v168, v168, v12\n"
    "v_mul_f32 v169, v169, v12\n"
    "v_mfma_f32_16x16x32_bf16 v[16:19], a[100:103], a[68:71], v[16:19]\n"
    "v_mul_f32 v170, v170, v12\n"
    "v_mul_f32 v171, v171, v12\n"
    "v_mfma_f32_16x16x32_bf16 v[24:27], a[100:103], a[4:7], v[24:27]\n"
    "v_mul_f32 v172, v172, v12\n"
    "v_mul_f32 v173, v173, v12\n"
    "v_mfma_f32_16x16x32_bf16 v[32:35], a[100:103], a[20:23], v[32:35]\n"
    "ds_read_b128 a[100:103], v2 offset:5632\n"
    "v_mul_f32 v174, v174, v12\n"
    "v_mfma_f32_16x16x32_bf16 v[40:43], a[80:83], a[56:59], v[40:43]\n"
    "v_mul_f32 v175, v175, v12\n"
    "v_cvt_pk_bf16_f32 v168, v168, v169\n"
    "v_mfma_f32_16x16x32_bf16 v[16:19], a[80:83], a[72:75], v[16:19]\n"
    "v_cvt_pk_bf16_f32 v169, v170, v171\n"
    "v_cvt_pk_bf16_f32 v170, v172, v173\n"
    "v_mfma_f32_16x16x32_bf16 v[24:27], a[80:83], a[8:11], v[24:27]\n"
    "v_cvt_pk_bf16_f32 v171, v174, v175\n"
    "v_mfma_f32_16x16x32_bf16 v[32:35], a[80:83], a[24:27], v[32:35]\n"
    "v_exp_f32 v48, v48\n"
    "v_mfma_f32_16x16x32_bf16 v[40:43], a[84:87], a[60:63], v[40:43]\n"
    "ds_read_b128 a[80:83], v2 offset:8192\n"
    "v_add_f32 v9, v9, v48\n"
    "v_mfma_f32_16x16x32_bf16 v[16:19], a[84:87], a[76:79], v[16:19]\n"
    "v_exp_f32 v49, v49\n"
    "v_mfma_f32_16x16x32_bf16 v[24:27], a[84:87], a[12:15], v[24:27]\n"
    "v_add_f32 v9, v9, v49\n"
    "v_cvt_pk_bf16_f32 v48, v48, v49\n"
    "v_mfma_f32_16x16x32_bf16 v[32:35], a[84:87], a[28:31], v[32:35]\n"
    "v_exp_f32 v50, v50\n"
    "v_mfma_f32_16x16x32_bf16 v[44:47], a[88:91], a[48:51], v[252:255]\n"
    "ds_read_b128 a[84:87], v2 offset:8704\n"
    "v_add_f32 v9, v9, v50\n"
    "v_mfma_f32_16x16x32_bf16 v[20:23], a[88:91], a[64:67], v[240:243]\n"
    "v_exp_f32 v51, v51\n"
    "v_mfma_f32_16x16x32_bf16 v[28:31], a[88:91], a[0:3], v[244:247]\n"
    "v_add_f32 v9, v9, v51\n"
    "v_cvt_pk_bf16_f32 v49, v50, v51\n"
    "v_mfma_f32_16x16x32_bf16 v[36:39], a[88:91], a[16:19], v[248:251]\n"
    "v_exp_f32 v56, v56\n"
    "v_mfma_f32_16x16x32_bf16 v[44:47], a[92:95], a[52:55], v[44:47]\n"
    "ds_read_b128 a[88:91], v2 offset:9216\n"
    "v_add_f32 v5, v5, v56\n"
    "v_mfma_f32_16x16x32_bf16 v[20:23], a[92:95], a[68:71], v[20:23]\n"
    "v_exp_f32 v57, v57\n"
    "v_mfma_f32_16x16x32_bf16 v[28:31], a[92:95], a[4:7], v[28:31]\n"
    "v_add_f32 v5, v5, v57\n"
    "v_cvt_pk_bf16_f32 v56, v56, v57\n"
    "v_mfma_f32_16x16x32_bf16 v[36:39], a[92:95], a[20:23], v[36:39]\n"
    "s_waitcnt lgkmcnt(4)\n"
    "v_exp_f32 v58, v58\n"
    "v_mfma_f32_16x16x32_bf16 v[44:47], a[96:99], a[56:59], v[44:47]\n"
    "ds_read_b128 a[92:95], v2 offset:9728\n"
    "v_add_f32 v5, v5, v58\n"
    "v_mfma_f32_16x16x32_bf16 v[20:23], a[96:99], a[72:75], v[20:23]\n"
    "v_exp_f32 v59, v59\n"
    "v_mfma_f32_16x16x32_bf16 v[28:31], a[96:99], a[8:11], v[28:31]\n"
    "v_add_f32 v5, v5, v59\n"
    "v_cvt_pk_bf16_f32 v57, v58, v59\n"
    "v_mfma_f32_16x16x32_bf16 v[36:39], a[96:99], a[24:27], v[36:39]\n"
    "s_waitcnt lgkmcnt(4)\n"
    "v_exp_f32 v64, v64\n"
    "v_mfma_f32_16x16x32_bf16 v[44:47], a[100:103], a[60:63], v[44:47]\n"
    "ds_read_b128 a[96:99], v2 offset:12288\n"
    "v_add_f32 v6, v6, v64\n"
    "v_mfma_f32_16x16x32_bf16 v[20:23], a[100:103], a[76:79], v[20:23]\n"
    "v_exp_f32 v65, v65\n"
    "v_mfma_f32_16x16x32_bf16 v[28:31], a[100:103], a[12:15], v[28:31]\n"
    "buffer_load_dwordx4 a[48:51], v4, s[40:43], s60 offen offset:0\n"
    "buffer_load_dwordx4 a[52:55], v4, s[40:43], s60 offen offset:64\n"
    "v_mfma_f32_16x16x32_bf16 v[36:39], a[100:103], a[28:31], v[36:39]\n"
    "ds_read_b128 a[100:103], v2 offset:12800\n"
    "v_add_f32 v6, v6, v65\n"
    "v_cvt_pk_bf16_f32 v64, v64, v65\n"
    "v_exp_f32 v66, v66\n"
    "v_exp_f32 v67, v67\n"
    "v_add_f32 v6, v6, v66\n"
    "v_add_f32 v6, v6, v67\n"
    "v_cvt_pk_bf16_f32 v65, v66, v67\n"
    "v_exp_f32 v76, v76\n"
    "v_exp_f32 v77, v77\n"
    "v_add_f32 v8, v8, v76\n"
    "v_add_f32 v8, v8, v77\n"
    "v_cvt_pk_bf16_f32 v74, v76, v77\n"
    "v_exp_f32 v78, v78\n"
    "v_exp_f32 v79, v79\n"
    "v_add_f32 v8, v8, v78\n"
    "v_add_f32 v8, v8, v79\n"
    "v_cvt_pk_bf16_f32 v75, v78, v79\n"
    "v_exp_f32 v52, v52\n"
    "s_nop 0\n"
    "v_mfma_f32_16x16x32_bf16 v[176:179], a[120:123], v[72:75], v[176:179]\n"
    "buffer_load_dwordx4 a[56:59], v4, s[40:43], s60 offen offset:128\n"
    "v_add_f32 v9, v9, v52\n"
    "v_exp_f32 v53, v53\n"
    "v_exp_f32 v54, v54\n"
    "v_add_f32 v9, v9, v53\n"
    "v_cvt_pk_bf16_f32 v50, v52, v53\n"
    "v_add_f32 v9, v9, v54\n"
    "v_exp_f32 v55, v55\n"
    "v_exp_f32 v60, v60\n"
    "v_add_f32 v9, v9, v55\n"
    "v_cvt_pk_bf16_f32 v51, v54, v55\n"
    "v_add_f32 v5, v5, v60\n"
    "s_nop 0\n"
    "v_mfma_f32_16x16x32_bf16 v[208:211], a[120:123], v[48:51], v[208:211]\n"
    "buffer_load_dwordx4 a[60:63], v4, s[40:43], s60 offen offset:192\n"
    "v_exp_f32 v61, v61\n"
    "s_mov_b32 s60, 163840\n"
    "v_add_f32 v5, v5, v61\n"
    "v_cvt_pk_bf16_f32 v58, v60, v61\n"
    "v_exp_f32 v62, v62\n"
    "v_exp_f32 v63, v63\n"
    "v_add_f32 v5, v5, v62\n"
    "v_add_f32 v5, v5, v63\n"
    "v_cvt_pk_bf16_f32 v59, v62, v63\n"
    "v_exp_f32 v68, v68\n"
    "s_nop 0\n"
    "v_mfma_f32_16x16x32_bf16 v[80:83], a[120:123], v[56:59], v[80:83]\n"
    "v_add_f32 v6, v6, v68\n"
    "v_exp_f32 v69, v69\n"
    "v_exp_f32 v70, v70\n"
    "buffer_store_dwordx4 v[144:147], v4, s[52:55], s62 offen offset:0 sc0 sc1\n"
    "v_add_f32 v6, v6, v69\n"
    "v_cvt_pk_bf16_f32 v66, v68, v69\n"
    "v_add_f32 v6, v6, v70\n"
    "v_exp_f32 v71, v71\n"
    "v_exp_f32 v40, v40\n"
    "v_add_f32 v6, v6, v71\n"
    "v_cvt_pk_bf16_f32 v67, v70, v71\n"
    "v_add_f32 v8, v8, v40\n"
    "s_nop 0\n"
    "v_mfma_f32_16x16x32_bf16 v[112:115], a[120:123], v[64:67], v[112:115]\n"
    "v_exp_f32 v41, v41\n"
    "v_mfma_f32_16x16x32_bf16 v[180:183], a[124:127], v[72:75], v[180:183]\n"
    "ds_read_b64_tr_b16 a[120:121], v3 offset:26112\n"
    "ds_read_b64_tr_b16 a[122:123], v3 offset:30208\n"
    "v_mfma_f32_16x16x32_bf16 v[212:215], a[124:127], v[48:51], v[212:215]\n"
    "buffer_store_dwordx4 v[152:155], v4, s[52:55], s62 offen offset:64 sc0 sc1\n"
    "v_add_f32 v8, v8, v41\n"
    "v_mfma_f32_16x16x32_bf16 v[84:87], a[124:127], v[56:59], v[84:87]\n"
    "v_cvt_pk_bf16_f32 v40, v40, v41\n"
    "v_mfma_f32_16x16x32_bf16 v[116:119], a[124:127], v[64:67], v[116:119]\n"
    "v_exp_f32 v42, v42\n"
    "v_mfma_f32_16x16x32_bf16 v[184:187], a[104:107], v[72:75], v[184:187]\n"
    "ds_read_b64_tr_b16 a[124:125], v3 offset:26120\n"
    "ds_read_b64_tr_b16 a[126:127], v3 offset:30216\n"
    "v_mfma_f32_16x16x32_bf16 v[216:219], a[104:107], v[48:51], v[216:219]\n"
    "v_add_f32 v8, v8, v42\n"
    "v_mfma_f32_16x16x32_bf16 v[88:91], a[104:107], v[56:59], v[88:91]\n"
    "v_exp_f32 v43, v43\n"
    "v_mfma_f32_16x16x32_bf16 v[120:123], a[104:107], v[64:67], v[120:123]\n"
    "buffer_store_dwordx4 v[160:163], v4, s[52:55], s62 offen offset:128 sc0 sc1\n"
    "ds_read_b64_tr_b16 a[104:105], v3 offset:32768\n"
    "v_mfma_f32_16x16x32_bf16 v[188:191], a[108:111], v[72:75], v[188:191]\n"
    "ds_read_b64_tr_b16 a[106:107], v3 offset:36864\n"
    "v_add_f32 v8, v8, v43\n"
    "v_mfma_f32_16x16x32_bf16 v[220:223], a[108:111], v[48:51], v[220:223]\n"
    "v_cvt_pk_bf16_f32 v41, v42, v43\n"
    "v_mfma_f32_16x16x32_bf16 v[92:95], a[108:111], v[56:59], v[92:95]\n"
    "v_exp_f32 v16, v16\n"
    "v_mfma_f32_16x16x32_bf16 v[124:127], a[108:111], v[64:67], v[124:127]\n"
    "ds_read_b64_tr_b16 a[108:109], v3 offset:32776\n"
    "ds_read_b64_tr_b16 a[110:111], v3 offset:36872\n"
    "v_mfma_f32_16x16x32_bf16 v[192:195], a[112:115], v[72:75], v[192:195]\n"
    "v_add_f32 v9, v9, v16\n"
    "v_mfma_f32_16x16x32_bf16 v[224:227], a[112:115], v[48:51], v[224:227]\n"
    "buffer_store_dwordx4 v[168:171], v4, s[52:55], s62 offen offset:192 sc0 sc1\n"
    "v_mfma_f32_16x16x32_bf16 v[96:99], a[112:115], v[56:59], v[96:99]\n"
    "v_exp_f32 v17, v17\n"
    "v_mfma_f32_16x16x32_bf16 v[128:131], a[112:115], v[64:67], v[128:131]\n"
    "ds_read_b64_tr_b16 a[112:113], v3 offset:33280\n"
    "ds_read_b64_tr_b16 a[114:115], v3 offset:37376\n"
    "v_mfma_f32_16x16x32_bf16 v[196:199], a[116:119], v[72:75], v[196:199]\n"
    "v_add_f32 v9, v9, v17\n"
    "v_cvt_pk_bf16_f32 v16, v16, v17\n"
    "v_mfma_f32_16x16x32_bf16 v[228:231], a[116:119], v[48:51], v[228:231]\n"
    "v_exp_f32 v18, v18\n"
    "v_mfma_f32_16x16x32_bf16 v[100:103], a[116:119], v[56:59], v[100:103]\n"
    "v_add_f32 v9, v9, v18\n"
    "v_mfma_f32_16x16x32_bf16 v[132:135], a[116:119], v[64:67], v[132:135]\n"
    "s_waitcnt lgkmcnt(8)\n"
    "v_exp_f32 v19, v19\n"
    "v_mfma_f32_16x16x32_bf16 v[200:203], a[120:123], v[72:75], v[200:203]\n"
    "ds_read_b64_tr_b16 a[116:117], v3 offset:33288\n"
    "ds_read_b64_tr_b16 a[118:119], v3 offset:37384\n"
    "v_mfma_f32_16x16x32_bf16 v[232:235], a[120:123], v[48:51], v[232:235]\n"
    "v_add_f32 v9, v9, v19\n"
    "v_cvt_pk_bf16_f32 v17, v18, v19\n"
    "v_mfma_f32_16x16x32_bf16 v[104:107], a[120:123], v[56:59], v[104:107]\n"
    "v_exp_f32 v24, v24\n"
    "v_mfma_f32_16x16x32_bf16 v[136:139], a[120:123], v[64:67], v[136:139]\n"
    "s_waitcnt lgkmcnt(8)\n"
    "ds_read_b64_tr_b16 a[120:121], v3 offset:33792\n"
    "ds_read_b64_tr_b16 a[122:123], v3 offset:37888\n"
    "v_mfma_f32_16x16x32_bf16 v[204:207], a[124:127], v[72:75], v[204:207]\n"
    "v_add_f32 v5, v5, v24\n"
    "v_mfma_f32_16x16x32_bf16 v[236:239], a[124:127], v[48:51], v[236:239]\n"
    "v_exp_f32 v25, v25\n"
    "v_mfma_f32_16x16x32_bf16 v[108:111], a[124:127], v[56:59], v[108:111]\n"
    "v_add_f32 v5, v5, v25\n"
    "v_cvt_pk_bf16_f32 v24, v24, v25\n"
    "v_mfma_f32_16x16x32_bf16 v[140:143], a[124:127], v[64:67], v[140:143]\n"
    "v_exp_f32 v26, v26\n"
    "v_mfma_f32_16x16x32_bf16 v[48:51], a[80:83], a[64:67], v[240:243]\n"
    "ds_read_b64_tr_b16 a[124:125], v3 offset:33800\n"
    "ds_read_b64_tr_b16 a[126:127], v3 offset:37896\n"
    "v_mfma_f32_16x16x32_bf16 v[56:59], a[80:83], a[0:3], v[244:247]\n"
    "v_add_f32 v5, v5, v26\n"
    "v_mfma_f32_16x16x32_bf16 v[64:67], a[80:83], a[16:19], v[248:251]\n"
    "s_waitcnt vmcnt(16)\n"
    "v_exp_f32 v27, v27\n"
    "v_mfma_f32_16x16x32_bf16 v[72:75], a[80:83], a[32:35], 0\n"
    "ds_read_b128 a[80:83], v2 offset:13312\n"
    "v_add_f32 v5, v5, v27\n"
    "v_mfma_f32_16x16x32_bf16 v[48:51], a[84:87], a[68:71], v[48:51]\n"
    "v_cvt_pk_bf16_f32 v25, v26, v27\n"
    "v_mfma_f32_16x16x32_bf16 v[56:59], a[84:87], a[4:7], v[56:59]\n"
    "v_exp_f32 v32, v32\n"
    "v_mfma_f32_16x16x32_bf16 v[64:67], a[84:87], a[20:23], v[64:67]\n"
    "s_waitcnt vmcnt(15)\n"
    "v_add_f32 v6, v6, v32\n"
    "v_mfma_f32_16x16x32_bf16 v[72:75], a[84:87], a[36:39], v[72:75]\n"
    "v_exp_f32 v33, v33\n"
    "v_mfma_f32_16x16x32_bf16 v[48:51], a[88:91], a[72:75], v[48:51]\n"
    "ds_read_b128 a[84:87], v2 offset:13824\n"
    "v_add_f32 v6, v6, v33\n"
    "v_mfma_f32_16x16x32_bf16 v[56:59], a[88:91], a[8:11], v[56:59]\n"
    "v_cvt_pk_bf16_f32 v32, v32, v33\n"
    "v_mfma_f32_16x16x32_bf16 v[64:67], a[88:91], a[24:27], v[64:67]\n"
    "s_waitcnt vmcnt(14)\n"
    "v_exp_f32 v34, v34\n"
    "v_mfma_f32_16x16x32_bf16 v[72:75], a[88:91], a[40:43], v[72:75]\n"
    "ds_read_b128 a[88:91], v2 offset:16384\n"
    "v_add_f32 v6, v6, v34\n"
    "v_mfma_f32_16x16x32_bf16 v[48:51], a[92:95], a[76:79], v[48:51]\n"
    "v_exp_f32 v35, v35\n"
    "v_mfma_f32_16x16x32_bf16 v[56:59], a[92:95], a[12:15], v[56:59]\n"
    "v_add_f32 v6, v6, v35\n"
    "v_cvt_pk_bf16_f32 v33, v34, v35\n"
    "v_mfma_f32_16x16x32_bf16 v[64:67], a[92:95], a[28:31], v[64:67]\n"
    "s_waitcnt vmcnt(13)\n"
    "v_exp_f32 v44, v44\n"
    "v_mfma_f32_16x16x32_bf16 v[72:75], a[92:95], a[44:47], v[72:75]\n"
    "ds_read_b128 a[92:95], v2 offset:16896\n"
    "v_add_f32 v8, v8, v44\n"
    "v_mfma_f32_16x16x32_bf16 v[52:55], a[96:99], a[64:67], v[240:243]\n"
    "v_exp_f32 v45, v45\n"
    "v_mfma_f32_16x16x32_bf16 v[60:63], a[96:99], a[0:3], v[244:247]\n"
    "v_add_f32 v8, v8, v45\n"
    "v_cvt_pk_bf16_f32 v42, v44, v45\n"
    "v_mfma_f32_16x16x32_bf16 v[68:71], a[96:99], a[16:19], v[248:251]\n"
    "v_exp_f32 v46, v46\n"
    "v_mfma_f32_16x16x32_bf16 v[76:79], a[96:99], a[32:35], 0\n"
    "ds_read_b128 a[96:99], v2 offset:17408\n"
    "v_add_f32 v8, v8, v46\n"
    "v_mfma_f32_16x16x32_bf16 v[52:55], a[100:103], a[68:71], v[52:55]\n"
    "v_exp_f32 v47, v47\n"
    "v_mfma_f32_16x16x32_bf16 v[60:63], a[100:103], a[4:7], v[60:63]\n"
    "v_add_f32 v8, v8, v47\n"
    "v_cvt_pk_bf16_f32 v43, v46, v47\n"
    "v_mfma_f32_16x16x32_bf16 v[68:71], a[100:103], a[20:23], v[68:71]\n"
    "v_exp_f32 v20, v20\n"
    "v_mfma_f32_16x16x32_bf16 v[76:79], a[100:103], a[36:39], v[76:79]\n"
    "s_waitcnt lgkmcnt(4)\n"
    "ds_read_b128 a[100:103], v2 offset:17920\n"
    "v_add_f32 v9, v9, v20\n"
    "v_mfma_f32_16x16x32_bf16 v[52:55], a[80:83], a[72:75], v[52:55]\n"
    "v_exp_f32 v21, v21\n"
    "v_mfma_f32_16x16x32_bf16 v[60:63], a[80:83], a[8:11], v[60:63]\n"
    "v_add_f32 v9, v9, v21\n"
    "v_cvt_pk_bf16_f32 v18, v20, v21\n"
    "v_mfma_f32_16x16x32_bf16 v[68:71], a[80:83], a[24:27], v[68:71]\n"
    "v_exp_f32 v22, v22\n"
    "v_mfma_f32_16x16x32_bf16 v[76:79], a[80:83], a[40:43], v[76:79]\n"
    "s_waitcnt lgkmcnt(4)\n"
    "ds_read_b128 a[80:83], v2 offset:20480\n"
    "v_add_f32 v9, v9, v22\n"
    "v_mfma_f32_16x16x32_bf16 v[52:55], a[84:87], a[76:79], v[52:55]\n"
    "v_exp_f32 v23, v23\n"
    "v_mfma_f32_16x16x32_bf16 v[60:63], a[84:87], a[12:15], v[60:63]\n"
    "v_add_f32 v9, v9, v23\n"
    "v_cvt_pk_bf16_f32 v19, v22, v23\n"
    "v_mfma_f32_16x16x32_bf16 v[68:71], a[84:87], a[28:31], v[68:71]\n"
    "v_exp_f32 v28, v28\n"
    "v_mfma_f32_16x16x32_bf16 v[76:79], a[84:87], a[44:47], v[76:79]\n"
    "ds_read_b128 a[84:87], v2 offset:20992\n"
    "v_add_f32 v5, v5, v28\n"
    "v_mfma_f32_16x16x32_bf16 v[176:179], a[104:107], v[40:43], v[176:179]\n"
    "v_exp_f32 v29, v29\n"
    "v_mfma_f32_16x16x32_bf16 v[208:211], a[104:107], v[16:19], v[208:211]\n"
    "v_add_f32 v5, v5, v29\n"
    "v_cvt_pk_bf16_f32 v26, v28, v29\n"
    "v_exp_f32 v30, v30\n"
    "v_exp_f32 v31, v31\n"
    "v_add_f32 v5, v5, v30\n"
    "v_add_f32 v5, v5, v31\n"
    "v_cvt_pk_bf16_f32 v27, v30, v31\n"
    "v_max3_f32 v10, v72, v73, v74\n"
    "v_max3_f32 v11, v76, v77, v78\n"
    "v_mfma_f32_16x16x32_bf16 v[80:83], a[104:107], v[24:27], v[80:83]\n"
    "v_max3_f32 v10, v10, v75, v79\n"
    "v_max_f32 v10, v10, v11\n"
    "v_mov_b32 v11, v10\n"
    "v_exp_f32 v36, v36\n"
    "s_nop 0\n"
    "v_permlane32_swap_b32 v10, v11\n"
    "v_add_f32 v6, v6, v36\n"
    "v_max_f32 v10, v10, v11\n"
    "v_mov_b32 v11, v10\n"
    "v_exp_f32 v37, v37\n"
    "s_nop 0\n"
    "v_permlane16_swap_b32 v10, v11\n"
    "v_add_f32 v6, v6, v37\n"
    "v_min_f32 v252, -v10, -v11\n"
    "v_min_f32 v253, -v10, -v11\n"
    "v_min_f32 v254, -v10, -v11\n"
    "v_min_f32 v255, -v10, -v11\n"
    "v_add_f32 v72, v72, v252\n"
    "v_add_f32 v73, v73, v252\n"
    "v_add_f32 v74, v74, v252\n"
    "v_add_f32 v75, v75, v252\n"
    "v_add_f32 v76, v76, v252\n"
    "v_add_f32 v77, v77, v252\n"
    "v_add_f32 v78, v78, v252\n"
    "v_add_f32 v79, v79, v252\n"
    "v_cvt_pk_bf16_f32 v34, v36, v37\n"
    "v_exp_f32 v38, v38\n"
    "v_exp_f32 v39, v39\n"
    "v_add_f32 v6, v6, v38\n"
    "v_add_f32 v6, v6, v39\n"
    "v_cvt_pk_bf16_f32 v35, v38, v39\n"
    "v_exp_f32 v48, v48\n"
    "s_nop 0\n"
    "v_mfma_f32_16x16x32_bf16 v[112:115], a[104:107], v[32:35], v[112:115]\n"
    "ds_read_b64_tr_b16 a[104:105], v3 offset:34304\n"
    "ds_read_b64_tr_b16 a[106:107], v3 offset:38400\n"
    "v_mfma_f32_16x16x32_bf16 v[180:183], a[108:111], v[40:43], v[180:183]\n"
    "v_add_f32 v9, v9, v48\n"
    "v_mov_b32 v12, v8\n"
    "v_mfma_f32_16x16x32_bf16 v[212:215], a[108:111], v[16:19], v[212:215]\n"
    "s_mov_b32 s63, 49152\n"
    "v_exp_f32 v49, v49\n"
    "v_mfma_f32_16x16x32_bf16 v[84:87], a[108:111], v[24:27], v[84:87]\n"
    "v_add_f32 v9, v9, v49\n"
    "v_cvt_pk_bf16_f32 v48, v48, v49\n"
    "v_mfma_f32_16x16x32_bf16 v[116:119], a[108:111], v[32:35], v[116:119]\n"
    "v_exp_f32 v50, v50\n"
    "v_mfma_f32_16x16x32_bf16 v[184:187], a[112:115], v[40:43], v[184:187]\n"
    "ds_read_b64_tr_b16 a[108:109], v3 offset:34312\n"
    "ds_read_b64_tr_b16 a[110:111], v3 offset:38408\n"
    "v_mfma_f32_16x16x32_bf16 v[216:219], a[112:115], v[16:19], v[216:219]\n"
    "v_add_f32 v9, v9, v50\n"
    "v_permlane32_swap_b32 v8, v12\n"
    "v_mfma_f32_16x16x32_bf16 v[88:91], a[112:115], v[24:27], v[88:91]\n"
    "v_exp_f32 v51, v51\n"
    "v_mfma_f32_16x16x32_bf16 v[120:123], a[112:115], v[32:35], v[120:123]\n"
    "ds_read_b64_tr_b16 a[112:113], v3 offset:40960\n"
    "ds_read_b64_tr_b16 a[114:115], v3 offset:45056\n"
    "v_mfma_f32_16x16x32_bf16 v[188:191], a[116:119], v[40:43], v[188:191]\n"
    "v_add_f32 v9, v9, v51\n"
    "v_cvt_pk_bf16_f32 v49, v50, v51\n"
    "v_mfma_f32_16x16x32_bf16 v[220:223], a[116:119], v[16:19], v[220:223]\n"
    "v_exp_f32 v56, v56\n"
    "v_mfma_f32_16x16x32_bf16 v[92:95], a[116:119], v[24:27], v[92:95]\n"
    "v_add_f32 v5, v5, v56\n"
    "v_add_f32 v8, v8, v12\n"
    "v_mfma_f32_16x16x32_bf16 v[124:127], a[116:119], v[32:35], v[124:127]\n"
    "v_exp_f32 v57, v57\n"
    "v_mfma_f32_16x16x32_bf16 v[192:195], a[120:123], v[40:43], v[192:195]\n"
    "ds_read_b64_tr_b16 a[116:117], v3 offset:40968\n"
    "ds_read_b64_tr_b16 a[118:119], v3 offset:45064\n"
    "v_mfma_f32_16x16x32_bf16 v[224:227], a[120:123], v[16:19], v[224:227]\n"
    "v_add_f32 v5, v5, v57\n"
    "v_cvt_pk_bf16_f32 v56, v56, v57\n"
    "v_mfma_f32_16x16x32_bf16 v[96:99], a[120:123], v[24:27], v[96:99]\n"
    "v_exp_f32 v58, v58\n"
    "v_mfma_f32_16x16x32_bf16 v[128:131], a[120:123], v[32:35], v[128:131]\n"
    "ds_read_b64_tr_b16 a[120:121], v3 offset:41472\n"
    "ds_read_b64_tr_b16 a[122:123], v3 offset:45568\n"
    "v_mfma_f32_16x16x32_bf16 v[196:199], a[124:127], v[40:43], v[196:199]\n"
    "v_add_f32 v5, v5, v58\n"
    "v_mov_b32 v12, v8\n"
    "v_mfma_f32_16x16x32_bf16 v[228:231], a[124:127], v[16:19], v[228:231]\n"
    "v_exp_f32 v59, v59\n"
    "v_mfma_f32_16x16x32_bf16 v[100:103], a[124:127], v[24:27], v[100:103]\n"
    "v_add_f32 v5, v5, v59\n"
    "v_cvt_pk_bf16_f32 v57, v58, v59\n"
    "v_mfma_f32_16x16x32_bf16 v[132:135], a[124:127], v[32:35], v[132:135]\n"
    "s_waitcnt lgkmcnt(8)\n"
    "v_exp_f32 v64, v64\n"
    "v_mfma_f32_16x16x32_bf16 v[200:203], a[104:107], v[40:43], v[200:203]\n"
    "ds_read_b64_tr_b16 a[124:125], v3 offset:41480\n"
    "ds_read_b64_tr_b16 a[126:127], v3 offset:45576\n"
    "v_mfma_f32_16x16x32_bf16 v[232:235], a[104:107], v[16:19], v[232:235]\n"
    "v_add_f32 v6, v6, v64\n"
    "v_permlane16_swap_b32 v8, v12\n"
    "v_mfma_f32_16x16x32_bf16 v[104:107], a[104:107], v[24:27], v[104:107]\n"
    "v_exp_f32 v65, v65\n"
    "v_mfma_f32_16x16x32_bf16 v[136:139], a[104:107], v[32:35], v[136:139]\n"
    "s_waitcnt lgkmcnt(8)\n"
    "ds_read_b64_tr_b16 a[104:105], v3 offset:41984\n"
    "ds_read_b64_tr_b16 a[106:107], v3 offset:46080\n"
    "v_mfma_f32_16x16x32_bf16 v[204:207], a[108:111], v[40:43], v[204:207]\n"
    "v_add_f32 v6, v6, v65\n"
    "v_cvt_pk_bf16_f32 v64, v64, v65\n"
    "v_mfma_f32_16x16x32_bf16 v[236:239], a[108:111], v[16:19], v[236:239]\n"
    "v_exp_f32 v66, v66\n"
    "v_mfma_f32_16x16x32_bf16 v[108:111], a[108:111], v[24:27], v[108:111]\n"
    "v_add_f32 v6, v6, v66\n"
    "v_add_f32 v12, v8, v12\n"
    "v_mfma_f32_16x16x32_bf16 v[140:143], a[108:111], v[32:35], v[140:143]\n"
    "v_exp_f32 v67, v67\n"
    "v_mfma_f32_16x16x32_bf16 v[16:19], a[88:91], a[64:67], v[240:243]\n"
    "ds_read_b64_tr_b16 a[108:109], v3 offset:41992\n"
    "ds_read_b64_tr_b16 a[110:111], v3 offset:46088\n"
    "v_mfma_f32_16x16x32_bf16 v[24:27], a[88:91], a[0:3], v[244:247]\n"
    "v_add_f32 v6, v6, v67\n"
    "v_cvt_pk_bf16_f32 v65, v66, v67\n"
    "v_mfma_f32_16x16x32_bf16 v[32:35], a[88:91], a[16:19], v[248:251]\n"
    "v_exp_f32 v72, v72\n"
    "v_mfma_f32_16x16x32_bf16 v[40:43], a[88:91], a[32:35], v[252:255]\n"
    "v_exp_f32 v73, v73\n"
    "v_mfma_f32_16x16x32_bf16 v[16:19], a[92:95], a[68:71], v[16:19]\n"
    "ds_read_b128 a[88:91], v2 offset:21504\n"
    "v_add_f32 v7, v72, v73\n"
    "v_mfma_f32_16x16x32_bf16 v[24:27], a[92:95], a[4:7], v[24:27]\n"
    "v_cvt_pk_bf16_f32 v72, v72, v73\n"
    "v_cmp_ngt_f32 vcc, 0x5d800000, v12\n"
    "v_mfma_f32_16x16x32_bf16 v[32:35], a[92:95], a[20:23], v[32:35]\n"
    "v_exp_f32 v74, v74\n"
    "v_mfma_f32_16x16x32_bf16 v[40:43], a[92:95], a[36:39], v[40:43]\n"
    "s_or_b64 s[64:65], s[64:65], vcc\n"
    "ds_read_b128 a[92:95], v2 offset:22016\n"
    "v_add_f32 v7, v7, v74\n"
    "v_mfma_f32_16x16x32_bf16 v[16:19], a[96:99], a[72:75], v[16:19]\n"
    "v_exp_f32 v75, v75\n"
    "v_mfma_f32_16x16x32_bf16 v[24:27], a[96:99], a[8:11], v[24:27]\n"
    "v_add_f32 v7, v7, v75\n"
    "v_cvt_pk_bf16_f32 v73, v74, v75\n"
    "v_mfma_f32_16x16x32_bf16 v[32:35], a[96:99], a[24:27], v[32:35]\n"
    "v_exp_f32 v52, v52\n"
    "v_mfma_f32_16x16x32_bf16 v[40:43], a[96:99], a[40:43], v[40:43]\n"
    "ds_read_b128 a[96:99], v2 offset:24576\n"
    "v_add_f32 v9, v9, v52\n"
    "v_mfma_f32_16x16x32_bf16 v[16:19], a[100:103], a[76:79], v[16:19]\n"
    "v_exp_f32 v53, v53\n"
    "v_mfma_f32_16x16x32_bf16 v[24:27], a[100:103], a[12:15], v[24:27]\n"
    "v_add_f32 v9, v9, v53\n"
    "v_cvt_pk_bf16_f32 v50, v52, v53\n"
    "v_mfma_f32_16x16x32_bf16 v[32:35], a[100:103], a[28:31], v[32:35]\n"
    "v_exp_f32 v54, v54\n"
    "v_mfma_f32_16x16x32_bf16 v[40:43], a[100:103], a[44:47], v[40:43]\n"
    "ds_read_b128 a[100:103], v2 offset:25088\n"
    "v_add_f32 v9, v9, v54\n"
    "v_mfma_f32_16x16x32_bf16 v[20:23], a[80:83], a[64:67], v[240:243]\n"
    "v_exp_f32 v55, v55\n"
    "v_mfma_f32_16x16x32_bf16 v[28:31], a[80:83], a[0:3], v[244:247]\n"
    "v_add_f32 v9, v9, v55\n"
    "v_cvt_pk_bf16_f32 v51, v54, v55\n"
    "v_mfma_f32_16x16x32_bf16 v[36:39], a[80:83], a[16:19], v[248:251]\n"
    "v_exp_f32 v60, v60\n"
    "v_mfma_f32_16x16x32_bf16 v[44:47], a[80:83], a[32:35], v[252:255]\n"
    "ds_read_b128 a[80:83], v2 offset:25600\n"
    "v_add_f32 v5, v5, v60\n"
    "v_mfma_f32_16x16x32_bf16 v[20:23], a[84:87], a[68:71], v[20:23]\n"
    "v_exp_f32 v61, v61\n"
    "v_mfma_f32_16x16x32_bf16 v[28:31], a[84:87], a[4:7], v[28:31]\n"
    "v_add_f32 v5, v5, v61\n"
    "v_cvt_pk_bf16_f32 v58, v60, v61\n"
    "v_mfma_f32_16x16x32_bf16 v[36:39], a[84:87], a[20:23], v[36:39]\n"
    "v_exp_f32 v62, v62\n"
    "v_mfma_f32_16x16x32_bf16 v[44:47], a[84:87], a[36:39], v[44:47]\n"
    "s_waitcnt lgkmcnt(4)\n"
    "ds_read_b128 a[84:87], v2 offset:26112\n"
    "v_add_f32 v5, v5, v62\n"
    "v_mfma_f32_16x16x32_bf16 v[20:23], a[88:91], a[72:75], v[20:23]\n"
    "v_exp_f32 v63, v63\n"
    "v_mfma_f32_16x16x32_bf16 v[28:31], a[88:91], a[8:11], v[28:31]\n"
    "v_add_f32 v5, v5, v63\n"
    "v_cvt_pk_bf16_f32 v59, v62, v63\n"
    "v_mfma_f32_16x16x32_bf16 v[36:39], a[88:91], a[24:27], v[36:39]\n"
    "v_exp_f32 v68, v68\n"
    "v_mfma_f32_16x16x32_bf16 v[44:47], a[88:91], a[40:43], v[44:47]\n"
    "s_waitcnt lgkmcnt(4)\n"
    "ds_read_b128 a[88:91], v2 offset:28672\n"
    "v_add_f32 v6, v6, v68\n"
    "v_mfma_f32_16x16x32_bf16 v[20:23], a[92:95], a[76:79], v[20:23]\n"
    "v_exp_f32 v69, v69\n"
    "v_mfma_f32_16x16x32_bf16 v[28:31], a[92:95], a[12:15], v[28:31]\n"
    "v_add_f32 v6, v6, v69\n"
    "v_cvt_pk_bf16_f32 v66, v68, v69\n"
    "v_mfma_f32_16x16x32_bf16 v[36:39], a[92:95], a[28:31], v[36:39]\n"
    "v_exp_f32 v70, v70\n"
    "v_mfma_f32_16x16x32_bf16 v[44:47], a[92:95], a[44:47], v[44:47]\n"
    "ds_read_b128 a[92:95], v2 offset:29184\n"
    "v_add_f32 v6, v6, v70\n"
    "v_mfma_f32_16x16x32_bf16 v[208:211], a[112:115], v[48:51], v[208:211]\n"
    "v_exp_f32 v71, v71\n"
    "v_mfma_f32_16x16x32_bf16 v[80:83], a[112:115], v[56:59], v[80:83]\n"
    "v_add_f32 v6, v6, v71\n"
    "v_cvt_pk_bf16_f32 v67, v70, v71\n"
    "v_exp_f32 v76, v76\n"
    "s_nop 0\n"
    "v_mfma_f32_16x16x32_bf16 v[112:115], a[112:115], v[64:67], v[112:115]\n"
    "v_add_f32 v7, v7, v76\n"
    "v_exp_f32 v77, v77\n"
    "v_exp_f32 v78, v78\n"
    "v_add_f32 v7, v7, v77\n"
    "v_cvt_pk_bf16_f32 v74, v76, v77\n"
    "v_add_f32 v7, v7, v78\n"
    "v_exp_f32 v79, v79\n"
    "v_rcp_f32 v12, v12\n"
    "v_add_f32 v7, v7, v79\n"
    "v_cvt_pk_bf16_f32 v75, v78, v79\n"
    "v_mul_f32 v176, v176, v12\n"
    "v_mul_f32 v177, v177, v12\n"
    "v_mfma_f32_16x16x32_bf16 v[144:147], a[112:115], v[72:75], 0\n"
    "ds_read_b64_tr_b16 a[112:113], v3 offset:42496\n"
    "ds_read_b64_tr_b16 a[114:115], v3 offset:46592\n"
    "v_mfma_f32_16x16x32_bf16 v[212:215], a[116:119], v[48:51], v[212:215]\n"
    "v_mul_f32 v178, v178, v12\n"
    "v_mul_f32 v179, v179, v12\n"
    "v_mfma_f32_16x16x32_bf16 v[84:87], a[116:119], v[56:59], v[84:87]\n"
    "v_mul_f32 v180, v180, v12\n"
    "v_mul_f32 v181, v181, v12\n"
    "v_mfma_f32_16x16x32_bf16 v[116:119], a[116:119], v[64:67], v[116:119]\n"
    "v_mul_f32 v182, v182, v12\n"
    "v_mul_f32 v183, v183, v12\n"
    "v_mfma_f32_16x16x32_bf16 v[148:151], a[116:119], v[72:75], 0\n"
    "ds_read_b64_tr_b16 a[116:117], v3 offset:42504\n"
    "ds_read_b64_tr_b16 a[118:119], v3 offset:46600\n"
    "v_mfma_f32_16x16x32_bf16 v[216:219], a[120:123], v[48:51], v[216:219]\n"
    "v_cvt_pk_bf16_f32 v176, v176, v177\n"
    "v_cvt_pk_bf16_f32 v177, v178, v179\n"
    "v_mfma_f32_16x16x32_bf16 v[88:91], a[120:123], v[56:59], v[88:91]\n"
    "v_cvt_pk_bf16_f32 v178, v180, v181\n"
    "v_cvt_pk_bf16_f32 v179, v182, v183\n"
    "v_mfma_f32_16x16x32_bf16 v[120:123], a[120:123], v[64:67], v[120:123]\n"
    "v_mul_f32 v184, v184, v12\n"
    "v_mul_f32 v185, v185, v12\n"
    "v_mfma_f32_16x16x32_bf16 v[152:155], a[120:123], v[72:75], 0\n"
    "ds_read_b64_tr_b16 a[120:121], v3 offset:49152\n"
    "ds_read_b64_tr_b16 a[122:123], v3 offset:53248\n"
    "v_mfma_f32_16x16x32_bf16 v[220:223], a[124:127], v[48:51], v[220:223]\n"
    "v_mul_f32 v186, v186, v12\n"
    "v_mul_f32 v187, v187, v12\n"
    "v_mfma_f32_16x16x32_bf16 v[92:95], a[124:127], v[56:59], v[92:95]\n"
    "v_mul_f32 v188, v188, v12\n"
    "v_mul_f32 v189, v189, v12\n"
    "v_mfma_f32_16x16x32_bf16 v[124:127], a[124:127], v[64:67], v[124:127]\n"
    "v_mul_f32 v190, v190, v12\n"
    "v_mul_f32 v191, v191, v12\n"
    "v_mfma_f32_16x16x32_bf16 v[156:159], a[124:127], v[72:75], 0\n"
    "ds_read_b64_tr_b16 a[124:125], v3 offset:49160\n"
    "ds_read_b64_tr_b16 a[126:127], v3 offset:53256\n"
    "v_mfma_f32_16x16x32_bf16 v[224:227], a[104:107], v[48:51], v[224:227]\n"
    "v_cvt_pk_bf16_f32 v184, v184, v185\n"
    "v_cvt_pk_bf16_f32 v185, v186, v187\n"
    "v_mfma_f32_16x16x32_bf16 v[96:99], a[104:107], v[56:59], v[96:99]\n"
    "v_cvt_pk_bf16_f32 v186, v188, v189\n"
    "v_cvt_pk_bf16_f32 v187, v190, v191\n"
    "v_mfma_f32_16x16x32_bf16 v[128:131], a[104:107], v[64:67], v[128:131]\n"
    "v_exp_f32 v16, v16\n"
    "v_mfma_f32_16x16x32_bf16 v[160:163], a[104:107], v[72:75], 0\n"
    "ds_read_b64_tr_b16 a[104:105], v3 offset:49664\n"
    "ds_read_b64_tr_b16 a[106:107], v3 offset:53760\n"
    "v_mfma_f32_16x16x32_bf16 v[228:231], a[108:111], v[48:51], v[228:231]\n"
    "v_add_f32 v9, v9, v16\n"
    "v_mul_f32 v192, v192, v12\n"
    "v_mfma_f32_16x16x32_bf16 v[100:103], a[108:111], v[56:59], v[100:103]\n"
    "v_exp_f32 v17, v17\n"
    "v_mfma_f32_16x16x32_bf16 v[132:135], a[108:111], v[64:67], v[132:135]\n"
    "v_add_f32 v9, v9, v17\n"
    "v_cvt_pk_bf16_f32 v16, v16, v17\n"
    "v_mfma_f32_16x16x32_bf16 v[164:167], a[108:111], v[72:75], 0\n"
    "s_waitcnt lgkmcnt(8)\n"
    "v_exp_f32 v18, v18\n"
    "v_mfma_f32_16x16x32_bf16 v[232:235], a[112:115], v[48:51], v[232:235]\n"
    "ds_read_b64_tr_b16 a[108:109], v3 offset:49672\n"
    "ds_read_b64_tr_b16 a[110:111], v3 offset:53768\n"
    "v_mfma_f32_16x16x32_bf16 v[104:107], a[112:115], v[56:59], v[104:107]\n"
    "v_add_f32 v9, v9, v18\n"
    "v_mul_f32 v193, v193, v12\n"
    "v_mfma_f32_16x16x32_bf16 v[136:139], a[112:115], v[64:67], v[136:139]\n"
    "v_exp_f32 v19, v19\n"
    "v_mfma_f32_16x16x32_bf16 v[168:171], a[112:115], v[72:75], 0\n"
    "s_waitcnt lgkmcnt(8)\n"
    "ds_read_b64_tr_b16 a[112:113], v3 offset:50176\n"
    "ds_read_b64_tr_b16 a[114:115], v3 offset:54272\n"
    "v_mfma_f32_16x16x32_bf16 v[236:239], a[116:119], v[48:51], v[236:239]\n"
    "v_add_f32 v9, v9, v19\n"
    "v_cvt_pk_bf16_f32 v17, v18, v19\n"
    "v_mfma_f32_16x16x32_bf16 v[108:111], a[116:119], v[56:59], v[108:111]\n"
    "v_mul_f32 v194, v194, v12\n"
    "v_mul_f32 v195, v195, v12\n"
    "v_mfma_f32_16x16x32_bf16 v[140:143], a[116:119], v[64:67], v[140:143]\n"
    "v_mul_f32 v196, v196, v12\n"
    "v_mul_f32 v197, v197, v12\n"
    "v_mfma_f32_16x16x32_bf16 v[172:175], a[116:119], v[72:75], 0\n"
    "s_mov_b32 s76, 32768\n"
    "ds_read_b64_tr_b16 a[116:117], v3 offset:50184\n"
    "ds_read_b64_tr_b16 a[118:119], v3 offset:54280\n"
    "v_mfma_f32_16x16x32_bf16 v[48:51], a[96:99], a[64:67], v[240:243]\n"
    "buffer_load_dword v15, v14, s[72:75], s76 offen\n"
    "v_mul_f32 v198, v198, v12\n"
    "v_mfma_f32_16x16x32_bf16 v[56:59], a[96:99], a[0:3], v[244:247]\n"
    "v_mul_f32 v199, v199, v12\n"
    "v_cvt_pk_bf16_f32 v192, v192, v193\n"
    "v_mfma_f32_16x16x32_bf16 v[64:67], a[96:99], a[16:19], v[248:251]\n"
    "v_cvt_pk_bf16_f32 v193, v194, v195\n"
    "v_cvt_pk_bf16_f32 v194, v196, v197\n"
    "v_mfma_f32_16x16x32_bf16 v[72:75], a[96:99], a[32:35], v[252:255]\n"
    "ds_read_b128 a[96:99], v2 offset:29696\n"
    "v_cvt_pk_bf16_f32 v195, v198, v199\n"
    "v_mfma_f32_16x16x32_bf16 v[48:51], a[100:103], a[68:71], v[48:51]\n"
    "v_mul_f32 v200, v200, v12\n"
    "v_mul_f32 v201, v201, v12\n"
    "v_mfma_f32_16x16x32_bf16 v[56:59], a[100:103], a[4:7], v[56:59]\n"
    "v_mul_f32 v202, v202, v12\n"
    "v_mul_f32 v203, v203, v12\n"
    "v_mfma_f32_16x16x32_bf16 v[64:67], a[100:103], a[20:23], v[64:67]\n"
    "v_mul_f32 v204, v204, v12\n"
    "v_mul_f32 v205, v205, v12\n"
    "v_mfma_f32_16x16x32_bf16 v[72:75], a[100:103], a[36:39], v[72:75]\n"
    "ds_read_b128 a[100:103], v2 offset:30208\n"
    "v_mul_f32 v206, v206, v12\n"
    "v_mfma_f32_16x16x32_bf16 v[48:51], a[80:83], a[72:75], v[48:51]\n"
    "v_mul_f32 v207, v207, v12\n"
    "v_cvt_pk_bf16_f32 v200, v200, v201\n"
    "v_mfma_f32_16x16x32_bf16 v[56:59], a[80:83], a[8:11], v[56:59]\n"
    "v_cvt_pk_bf16_f32 v201, v202, v203\n"
    "v_cvt_pk_bf16_f32 v202, v204, v205\n"
    "v_mfma_f32_16x16x32_bf16 v[64:67], a[80:83], a[24:27], v[64:67]\n"
    "v_cvt_pk_bf16_f32 v203, v206, v207\n"
    "v_mfma_f32_16x16x32_bf16 v[72:75], a[80:83], a[40:43], v[72:75]\n"
    "v_exp_f32 v24, v24\n"
    "v_mfma_f32_16x16x32_bf16 v[48:51], a[84:87], a[76:79], v[48:51]\n"
    "ds_read_b128 a[80:83], v1 offset:0\n"
    "v_add_f32 v5, v5, v24\n"
    "v_mfma_f32_16x16x32_bf16 v[56:59], a[84:87], a[12:15], v[56:59]\n"
    "v_exp_f32 v25, v25\n"
    "v_mfma_f32_16x16x32_bf16 v[64:67], a[84:87], a[28:31], v[64:67]\n"
    "v_add_f32 v5, v5, v25\n"
    "v_cvt_pk_bf16_f32 v24, v24, v25\n"
    "v_mfma_f32_16x16x32_bf16 v[72:75], a[84:87], a[44:47], v[72:75]\n"
    "v_exp_f32 v26, v26\n"
    "v_mfma_f32_16x16x32_bf16 v[52:55], a[88:91], a[64:67], v[240:243]\n"
    "ds_read_b128 a[84:87], v1 offset:512\n"
    "v_add_f32 v5, v5, v26\n"
    "v_mfma_f32_16x16x32_bf16 v[60:63], a[88:91], a[0:3], v[244:247]\n"
    "v_exp_f32 v27, v27\n"
    "v_mfma_f32_16x16x32_bf16 v[68:71], a[88:91], a[16:19], v[248:251]\n"
    "v_add_f32 v5, v5, v27\n"
    "v_cvt_pk_bf16_f32 v25, v26, v27\n"
    "v_mfma_f32_16x16x32_bf16 v[76:79], a[88:91], a[32:35], v[252:255]\n"
    "v_exp_f32 v32, v32\n"
    "v_mfma_f32_16x16x32_bf16 v[52:55], a[92:95], a[68:71], v[52:55]\n"
    "ds_read_b128 a[88:91], v1 offset:1024\n"
    "v_add_f32 v6, v6, v32\n"
    "v_mfma_f32_16x16x32_bf16 v[60:63], a[92:95], a[4:7], v[60:63]\n"
    "v_exp_f32 v33, v33\n"
    "v_mfma_f32_16x16x32_bf16 v[68:71], a[92:95], a[20:23], v[68:71]\n"
    "v_add_f32 v6, v6, v33\n"
    "v_cvt_pk_bf16_f32 v32, v32, v33\n"
    "v_mfma_f32_16x16x32_bf16 v[76:79], a[92:95], a[36:39], v[76:79]\n"
    "s_waitcnt lgkmcnt(4)\n"
    "v_exp_f32 v34, v34\n"
    "v_mfma_f32_16x16x32_bf16 v[52:55], a[96:99], a[72:75], v[52:55]\n"
    "ds_read_b128 a[92:95], v1 offset:1536\n"
    "v_add_f32 v6, v6, v34\n"
    "v_mfma_f32_16x16x32_bf16 v[60:63], a[96:99], a[8:11], v[60:63]\n"
    "v_exp_f32 v35, v35\n"
    "v_mfma_f32_16x16x32_bf16 v[68:71], a[96:99], a[24:27], v[68:71]\n"
    "v_add_f32 v6, v6, v35\n"
    "v_cvt_pk_bf16_f32 v33, v34, v35\n"
    "v_mfma_f32_16x16x32_bf16 v[76:79], a[96:99], a[40:43], v[76:79]\n"
    "s_waitcnt lgkmcnt(4)\n"
    "v_exp_f32 v40, v40\n"
    "v_mfma_f32_16x16x32_bf16 v[52:55], a[100:103], a[76:79], v[52:55]\n"
    "ds_read_b128 a[96:99], v1 offset:4096\n"
    "v_add_f32 v7, v7, v40\n"
    "v_mfma_f32_16x16x32_bf16 v[60:63], a[100:103], a[12:15], v[60:63]\n"
    "v_exp_f32 v41, v41\n"
    "v_mfma_f32_16x16x32_bf16 v[68:71], a[100:103], a[28:31], v[68:71]\n"
    "buffer_load_dwordx4 a[64:67], v4, s[40:43], s61 offen offset:0\n"
    "buffer_load_dwordx4 a[68:71], v4, s[40:43], s61 offen offset:64\n"
    "v_mfma_f32_16x16x32_bf16 v[76:79], a[100:103], a[44:47], v[76:79]\n"
    "ds_read_b128 a[100:103], v1 offset:4608\n"
    "v_add_f32 v7, v7, v41\n"
    "v_cvt_pk_bf16_f32 v40, v40, v41\n"
    "v_exp_f32 v42, v42\n"
    "v_exp_f32 v43, v43\n"
    "v_add_f32 v7, v7, v42\n"
    "v_add_f32 v7, v7, v43\n"
    "v_cvt_pk_bf16_f32 v41, v42, v43\n"
    "v_exp_f32 v20, v20\n"
    "v_exp_f32 v21, v21\n"
    "v_add_f32 v9, v9, v20\n"
    "v_add_f32 v9, v9, v21\n"
    "v_cvt_pk_bf16_f32 v18, v20, v21\n"
    "v_exp_f32 v22, v22\n"
    "v_exp_f32 v23, v23\n"
    "v_add_f32 v9, v9, v22\n"
    "v_add_f32 v9, v9, v23\n"
    "v_cvt_pk_bf16_f32 v19, v22, v23\n"
    "v_exp_f32 v28, v28\n"
    "s_nop 0\n"
    "v_mfma_f32_16x16x32_bf16 v[208:211], a[120:123], v[16:19], v[208:211]\n"
    "buffer_load_dwordx4 a[72:75], v4, s[40:43], s61 offen offset:128\n"
    "v_add_f32 v5, v5, v28\n"
    "v_exp_f32 v29, v29\n"
    "v_exp_f32 v30, v30\n"
    "v_add_f32 v5, v5, v29\n"
    "v_cvt_pk_bf16_f32 v26, v28, v29\n"
    "v_add_f32 v5, v5, v30\n"
    "v_exp_f32 v31, v31\n"
    "v_exp_f32 v36, v36\n"
    "v_add_f32 v5, v5, v31\n"
    "v_cvt_pk_bf16_f32 v27, v30, v31\n"
    "v_add_f32 v6, v6, v36\n"
    "s_nop 0\n"
    "v_mfma_f32_16x16x32_bf16 v[80:83], a[120:123], v[24:27], v[80:83]\n"
    "buffer_load_dwordx4 a[76:79], v4, s[40:43], s61 offen offset:192\n"
    "v_exp_f32 v37, v37\n"
    "s_mov_b32 s61, 180224\n"
    "v_add_f32 v6, v6, v37\n"
    "v_cvt_pk_bf16_f32 v34, v36, v37\n"
    "v_exp_f32 v38, v38\n"
    "v_exp_f32 v39, v39\n"
    "v_add_f32 v6, v6, v38\n"
    "v_add_f32 v6, v6, v39\n"
    "v_cvt_pk_bf16_f32 v35, v38, v39\n"
    "v_exp_f32 v44, v44\n"
    "s_nop 0\n"
    "v_mfma_f32_16x16x32_bf16 v[112:115], a[120:123], v[32:35], v[112:115]\n"
    "v_add_f32 v7, v7, v44\n"
    "v_exp_f32 v45, v45\n"
    "v_exp_f32 v46, v46\n"
    "buffer_store_dwordx4 v[176:179], v4, s[52:55], s63 offen offset:0 sc0 sc1\n"
    "v_add_f32 v7, v7, v45\n"
    "v_cvt_pk_bf16_f32 v42, v44, v45\n"
    "v_add_f32 v7, v7, v46\n"
    "v_exp_f32 v47, v47\n"
    "v_exp_f32 v48, v48\n"
    "v_add_f32 v7, v7, v47\n"
    "v_cvt_pk_bf16_f32 v43, v46, v47\n"
    "v_add_f32 v9, v9, v48\n"
    "s_nop 0\n"
    "v_mfma_f32_16x16x32_bf16 v[144:147], a[120:123], v[40:43], v[144:147]\n"
    "v_exp_f32 v49, v49\n"
    "v_mfma_f32_16x16x32_bf16 v[212:215], a[124:127], v[16:19], v[212:215]\n"
    "ds_read_b64_tr_b16 a[120:121], v3 offset:50688\n"
    "ds_read_b64_tr_b16 a[122:123], v3 offset:54784\n"
    "v_mfma_f32_16x16x32_bf16 v[84:87], a[124:127], v[24:27], v[84:87]\n"
    "buffer_store_dwordx4 v[184:187], v4, s[52:55], s63 offen offset:64 sc0 sc1\n"
    "v_add_f32 v9, v9, v49\n"
    "v_mfma_f32_16x16x32_bf16 v[116:119], a[124:127], v[32:35], v[116:119]\n"
    "v_cvt_pk_bf16_f32 v48, v48, v49\n"
    "v_mfma_f32_16x16x32_bf16 v[148:151], a[124:127], v[40:43], v[148:151]\n"
    "v_exp_f32 v50, v50\n"
    "v_mfma_f32_16x16x32_bf16 v[216:219], a[104:107], v[16:19], v[216:219]\n"
    "ds_read_b64_tr_b16 a[124:125], v3 offset:50696\n"
    "ds_read_b64_tr_b16 a[126:127], v3 offset:54792\n"
    "v_mfma_f32_16x16x32_bf16 v[88:91], a[104:107], v[24:27], v[88:91]\n"
    "v_add_f32 v9, v9, v50\n"
    "v_mfma_f32_16x16x32_bf16 v[120:123], a[104:107], v[32:35], v[120:123]\n"
    "v_exp_f32 v51, v51\n"
    "v_mfma_f32_16x16x32_bf16 v[152:155], a[104:107], v[40:43], v[152:155]\n"
    "buffer_store_dwordx4 v[192:195], v4, s[52:55], s63 offen offset:128 sc0 sc1\n"
    "ds_read_b64_tr_b16 a[104:105], v3 offset:57344\n"
    "v_mfma_f32_16x16x32_bf16 v[220:223], a[108:111], v[16:19], v[220:223]\n"
    "ds_read_b64_tr_b16 a[106:107], v3 offset:61440\n"
    "v_add_f32 v9, v9, v51\n"
    "v_mfma_f32_16x16x32_bf16 v[92:95], a[108:111], v[24:27], v[92:95]\n"
    "v_cvt_pk_bf16_f32 v49, v50, v51\n"
    "v_mfma_f32_16x16x32_bf16 v[124:127], a[108:111], v[32:35], v[124:127]\n"
    "v_exp_f32 v56, v56\n"
    "v_mfma_f32_16x16x32_bf16 v[156:159], a[108:111], v[40:43], v[156:159]\n"
    "ds_read_b64_tr_b16 a[108:109], v3 offset:57352\n"
    "ds_read_b64_tr_b16 a[110:111], v3 offset:61448\n"
    "v_mfma_f32_16x16x32_bf16 v[224:227], a[112:115], v[16:19], v[224:227]\n"
    "v_add_f32 v5, v5, v56\n"
    "v_mfma_f32_16x16x32_bf16 v[96:99], a[112:115], v[24:27], v[96:99]\n"
    "buffer_store_dwordx4 v[200:203], v4, s[52:55], s63 offen offset:192 sc0 sc1\n"
    "v_mfma_f32_16x16x32_bf16 v[128:131], a[112:115], v[32:35], v[128:131]\n"
    "v_exp_f32 v57, v57\n"
    "v_mfma_f32_16x16x32_bf16 v[160:163], a[112:115], v[40:43], v[160:163]\n"
    "ds_read_b64_tr_b16 a[112:113], v3 offset:57856\n"
    "ds_read_b64_tr_b16 a[114:115], v3 offset:61952\n"
    "v_mfma_f32_16x16x32_bf16 v[228:231], a[116:119], v[16:19], v[228:231]\n"
    "v_add_f32 v5, v5, v57\n"
    "v_cvt_pk_bf16_f32 v56, v56, v57\n"
    "v_mfma_f32_16x16x32_bf16 v[100:103], a[116:119], v[24:27], v[100:103]\n"
    "v_exp_f32 v58, v58\n"
    "v_mfma_f32_16x16x32_bf16 v[132:135], a[116:119], v[32:35], v[132:135]\n"
    "v_add_f32 v5, v5, v58\n"
    "v_mfma_f32_16x16x32_bf16 v[164:167], a[116:119], v[40:43], v[164:167]\n"
    "s_waitcnt lgkmcnt(8)\n"
    "v_exp_f32 v59, v59\n"
    "v_mfma_f32_16x16x32_bf16 v[232:235], a[120:123], v[16:19], v[232:235]\n"
    "ds_read_b64_tr_b16 a[116:117], v3 offset:57864\n"
    "ds_read_b64_tr_b16 a[118:119], v3 offset:61960\n"
    "v_mfma_f32_16x16x32_bf16 v[104:107], a[120:123], v[24:27], v[104:107]\n"
    "v_add_f32 v5, v5, v59\n"
    "v_cvt_pk_bf16_f32 v57, v58, v59\n"
    "v_mfma_f32_16x16x32_bf16 v[136:139], a[120:123], v[32:35], v[136:139]\n"
    "v_exp_f32 v64, v64\n"
    "v_mfma_f32_16x16x32_bf16 v[168:171], a[120:123], v[40:43], v[168:171]\n"
    "s_waitcnt lgkmcnt(8)\n"
    "ds_read_b64_tr_b16 a[120:121], v3 offset:58368\n"
    "ds_read_b64_tr_b16 a[122:123], v3 offset:62464\n"
    "v_mfma_f32_16x16x32_bf16 v[236:239], a[124:127], v[16:19], v[236:239]\n"
    "v_add_f32 v6, v6, v64\n"
    "v_mfma_f32_16x16x32_bf16 v[108:111], a[124:127], v[24:27], v[108:111]\n"
    "v_exp_f32 v65, v65\n"
    "v_mfma_f32_16x16x32_bf16 v[140:143], a[124:127], v[32:35], v[140:143]\n"
    "v_add_f32 v6, v6, v65\n"
    "v_cvt_pk_bf16_f32 v64, v64, v65\n"
    "v_mfma_f32_16x16x32_bf16 v[172:175], a[124:127], v[40:43], v[172:175]\n"
    "v_exp_f32 v66, v66\n"
    "v_mfma_f32_16x16x32_bf16 v[24:27], a[80:83], a[0:3], v[244:247]\n"
    "ds_read_b64_tr_b16 a[124:125], v3 offset:58376\n"
    "ds_read_b64_tr_b16 a[126:127], v3 offset:62472\n"
    "v_mfma_f32_16x16x32_bf16 v[32:35], a[80:83], a[16:19], v[248:251]\n"
    "v_add_f32 v6, v6, v66\n"
    "v_mfma_f32_16x16x32_bf16 v[40:43], a[80:83], a[32:35], v[252:255]\n"
    "s_waitcnt vmcnt(16)\n"
    "v_exp_f32 v67, v67\n"
    "v_mfma_f32_16x16x32_bf16 v[16:19], a[80:83], a[48:51], 0\n"
    "ds_read_b128 a[80:83], v1 offset:5120\n"
    "v_add_f32 v6, v6, v67\n"
    "v_mfma_f32_16x16x32_bf16 v[24:27], a[84:87], a[4:7], v[24:27]\n"
    "v_cvt_pk_bf16_f32 v65, v66, v67\n"
    "v_mfma_f32_16x16x32_bf16 v[32:35], a[84:87], a[20:23], v[32:35]\n"
    "v_exp_f32 v72, v72\n"
    "v_mfma_f32_16x16x32_bf16 v[40:43], a[84:87], a[36:39], v[40:43]\n"
    "s_waitcnt vmcnt(15)\n"
    "v_add_f32 v7, v7, v72\n"
    "v_mfma_f32_16x16x32_bf16 v[16:19], a[84:87], a[52:55], v[16:19]\n"
    "v_exp_f32 v73, v73\n"
    "v_mfma_f32_16x16x32_bf16 v[24:27], a[88:91], a[8:11], v[24:27]\n"
    "ds_read_b128 a[84:87], v1 offset:5632\n"
    "v_add_f32 v7, v7, v73\n"
    "v_mfma_f32_16x16x32_bf16 v[32:35], a[88:91], a[24:27], v[32:35]\n"
    "v_cvt_pk_bf16_f32 v72, v72, v73\n"
    "v_mfma_f32_16x16x32_bf16 v[40:43], a[88:91], a[40:43], v[40:43]\n"
    "s_waitcnt vmcnt(14)\n"
    "v_exp_f32 v74, v74\n"
    "v_mfma_f32_16x16x32_bf16 v[16:19], a[88:91], a[56:59], v[16:19]\n"
    "ds_read_b128 a[88:91], v1 offset:8192\n"
    "v_add_f32 v7, v7, v74\n"
    "v_mfma_f32_16x16x32_bf16 v[24:27], a[92:95], a[12:15], v[24:27]\n"
    "v_exp_f32 v75, v75\n"
    "v_mfma_f32_16x16x32_bf16 v[32:35], a[92:95], a[28:31], v[32:35]\n"
    "v_add_f32 v7, v7, v75\n"
    "v_cvt_pk_bf16_f32 v73, v74, v75\n"
    "v_mfma_f32_16x16x32_bf16 v[40:43], a[92:95], a[44:47], v[40:43]\n"
    "s_waitcnt vmcnt(13)\n"
    "v_exp_f32 v52, v52\n"
    "v_mfma_f32_16x16x32_bf16 v[16:19], a[92:95], a[60:63], v[16:19]\n"
    "ds_read_b128 a[92:95], v1 offset:8704\n"
    "v_add_f32 v9, v9, v52\n"
    "v_mfma_f32_16x16x32_bf16 v[28:31], a[96:99], a[0:3], v[244:247]\n"
    "v_exp_f32 v53, v53\n"
    "v_mfma_f32_16x16x32_bf16 v[36:39], a[96:99], a[16:19], v[248:251]\n"
    "v_add_f32 v9, v9, v53\n"
    "v_cvt_pk_bf16_f32 v50, v52, v53\n"
    "v_mfma_f32_16x16x32_bf16 v[44:47], a[96:99], a[32:35], v[252:255]\n"
    "v_exp_f32 v54, v54\n"
    "v_mfma_f32_16x16x32_bf16 v[20:23], a[96:99], a[48:51], 0\n"
    "ds_read_b128 a[96:99], v1 offset:9216\n"
    "v_add_f32 v9, v9, v54\n"
    "v_mfma_f32_16x16x32_bf16 v[28:31], a[100:103], a[4:7], v[28:31]\n"
    "v_exp_f32 v55, v55\n"
    "v_mfma_f32_16x16x32_bf16 v[36:39], a[100:103], a[20:23], v[36:39]\n"
    "v_add_f32 v9, v9, v55\n"
    "v_cvt_pk_bf16_f32 v51, v54, v55\n"
    "v_mfma_f32_16x16x32_bf16 v[44:47], a[100:103], a[36:39], v[44:47]\n"
    "v_exp_f32 v60, v60\n"
    "v_mfma_f32_16x16x32_bf16 v[20:23], a[100:103], a[52:55], v[20:23]\n"
    "s_waitcnt lgkmcnt(4)\n"
    "ds_read_b128 a[100:103], v1 offset:9728\n"
    "v_add_f32 v5, v5, v60\n"
    "v_mfma_f32_16x16x32_bf16 v[28:31], a[80:83], a[8:11], v[28:31]\n"
    "v_exp_f32 v61, v61\n"
    "v_mfma_f32_16x16x32_bf16 v[36:39], a[80:83], a[24:27], v[36:39]\n"
    "v_add_f32 v5, v5, v61\n"
    "v_cvt_pk_bf16_f32 v58, v60, v61\n"
    "v_mfma_f32_16x16x32_bf16 v[44:47], a[80:83], a[40:43], v[44:47]\n"
    "v_exp_f32 v62, v62\n"
    "v_mfma_f32_16x16x32_bf16 v[20:23], a[80:83], a[56:59], v[20:23]\n"
    "s_waitcnt lgkmcnt(4)\n"
    "ds_read_b128 a[80:83], v1 offset:12288\n"
    "v_add_f32 v5, v5, v62\n"
    "v_mfma_f32_16x16x32_bf16 v[28:31], a[84:87], a[12:15], v[28:31]\n"
    "v_exp_f32 v63, v63\n"
    "v_mfma_f32_16x16x32_bf16 v[36:39], a[84:87], a[28:31], v[36:39]\n"
    "v_add_f32 v5, v5, v63\n"
    "v_cvt_pk_bf16_f32 v59, v62, v63\n"
    "v_mfma_f32_16x16x32_bf16 v[44:47], a[84:87], a[44:47], v[44:47]\n"
    "v_exp_f32 v68, v68\n"
    "v_mfma_f32_16x16x32_bf16 v[20:23], a[84:87], a[60:63], v[20:23]\n"
    "ds_read_b128 a[84:87], v1 offset:12800\n"
    "v_add_f32 v6, v6, v68\n"
    "v_mfma_f32_16x16x32_bf16 v[208:211], a[104:107], v[48:51], v[208:211]\n"
    "v_exp_f32 v69, v69\n"
    "v_mfma_f32_16x16x32_bf16 v[80:83], a[104:107], v[56:59], v[80:83]\n"
    "v_add_f32 v6, v6, v69\n"
    "v_cvt_pk_bf16_f32 v66, v68, v69\n"
    "v_exp_f32 v70, v70\n"
    "v_exp_f32 v71, v71\n"
    "v_add_f32 v6, v6, v70\n"
    "v_add_f32 v6, v6, v71\n"
    "v_cvt_pk_bf16_f32 v67, v70, v71\n"
    "v_max3_f32 v10, v16, v17, v18\n"
    "v_max3_f32 v11, v20, v21, v22\n"
    "v_mfma_f32_16x16x32_bf16 v[112:115], a[104:107], v[64:67], v[112:115]\n"
    "v_max3_f32 v10, v10, v19, v23\n"
    "v_max_f32 v10, v10, v11\n"
    "v_mov_b32 v11, v10\n"
    "v_exp_f32 v76, v76\n"
    "s_nop 0\n"
    "v_permlane32_swap_b32 v10, v11\n"
    "v_add_f32 v7, v7, v76\n"
    "v_max_f32 v10, v10, v11\n"
    "v_mov_b32 v11, v10\n"
    "v_exp_f32 v77, v77\n"
    "s_nop 0\n"
    "v_permlane16_swap_b32 v10, v11\n"
    "v_add_f32 v7, v7, v77\n"
    "v_min_f32 v240, -v10, -v11\n"
    "v_min_f32 v241, -v10, -v11\n"
    "v_min_f32 v242, -v10, -v11\n"
    "v_min_f32 v243, -v10, -v11\n"
    "v_add_f32 v16, v16, v240\n"
    "v_add_f32 v17, v17, v240\n"
    "v_add_f32 v18, v18, v240\n"
    "v_add_f32 v19, v19, v240\n"
    "v_add_f32 v20, v20, v240\n"
    "v_add_f32 v21, v21, v240\n"
    "v_add_f32 v22, v22, v240\n"
    "v_add_f32 v23, v23, v240\n"
    "v_cvt_pk_bf16_f32 v74, v76, v77\n"
    "v_exp_f32 v78, v78\n"
    "v_exp_f32 v79, v79\n"
    "v_add_f32 v7, v7, v78\n"
    "v_add_f32 v7, v7, v79\n"
    "v_cvt_pk_bf16_f32 v75, v78, v79\n"
    "v_exp_f32 v24, v24\n"
    "s_nop 0\n"
    "v_mfma_f32_16x16x32_bf16 v[144:147], a[104:107], v[72:75], v[144:147]\n"
    "ds_read_b64_tr_b16 a[104:105], v3 offset:58880\n"
    "ds_read_b64_tr_b16 a[106:107], v3 offset:62976\n"
    "v_mfma_f32_16x16x32_bf16 v[212:215], a[108:111], v[48:51], v[212:215]\n"
    "v_add_f32 v5, v5, v24\n"
    "v_mov_b32 v12, v9\n"
    "v_mfma_f32_16x16x32_bf16 v[84:87], a[108:111], v[56:59], v[84:87]\n"
    "s_mov_b32 s62, 65536\n"
    "v_exp_f32 v25, v25\n"
    "v_mfma_f32_16x16x32_bf16 v[116:119], a[108:111], v[64:67], v[116:119]\n"
    "v_add_f32 v5, v5, v25\n"
    "v_cvt_pk_bf16_f32 v24, v24, v25\n"
    "v_mfma_f32_16x16x32_bf16 v[148:151], a[108:111], v[72:75], v[148:151]\n"
    "v_exp_f32 v26, v26\n"
    "v_mfma_f32_16x16x32_bf16 v[216:219], a[112:115], v[48:51], v[216:219]\n"
    "ds_read_b64_tr_b16 a[108:109], v3 offset:58888\n"
    "ds_read_b64_tr_b16 a[110:111], v3 offset:62984\n"
    "v_mfma_f32_16x16x32_bf16 v[88:91], a[112:115], v[56:59], v[88:91]\n"
    "v_add_f32 v5, v5, v26\n"
    "v_permlane32_swap_b32 v9, v12\n"
    "v_mfma_f32_16x16x32_bf16 v[120:123], a[112:115], v[64:67], v[120:123]\n"
    "v_exp_f32 v27, v27\n"
    "v_mfma_f32_16x16x32_bf16 v[152:155], a[112:115], v[72:75], v[152:155]\n"
    "ds_read_b64_tr_b16 a[112:113], v3 offset:0\n"
    "ds_read_b64_tr_b16 a[114:115], v3 offset:4096\n"
    "v_mfma_f32_16x16x32_bf16 v[220:223], a[116:119], v[48:51], v[220:223]\n"
    "v_add_f32 v5, v5, v27\n"
    "v_cvt_pk_bf16_f32 v25, v26, v27\n"
    "v_mfma_f32_16x16x32_bf16 v[92:95], a[116:119], v[56:59], v[92:95]\n"
    "v_exp_f32 v32, v32\n"
    "v_mfma_f32_16x16x32_bf16 v[124:127], a[116:119], v[64:67], v[124:127]\n"
    "v_add_f32 v6, v6, v32\n"
    "v_add_f32 v9, v9, v12\n"
    "v_mfma_f32_16x16x32_bf16 v[156:159], a[116:119], v[72:75], v[156:159]\n"
    "v_exp_f32 v33, v33\n"
    "v_mfma_f32_16x16x32_bf16 v[224:227], a[120:123], v[48:51], v[224:227]\n"
    "ds_read_b64_tr_b16 a[116:117], v3 offset:8\n"
    "ds_read_b64_tr_b16 a[118:119], v3 offset:4104\n"
    "v_mfma_f32_16x16x32_bf16 v[96:99], a[120:123], v[56:59], v[96:99]\n"
    "v_add_f32 v6, v6, v33\n"
    "v_cvt_pk_bf16_f32 v32, v32, v33\n"
    "v_mfma_f32_16x16x32_bf16 v[128:131], a[120:123], v[64:67], v[128:131]\n"
    "v_exp_f32 v34, v34\n"
    "v_mfma_f32_16x16x32_bf16 v[160:163], a[120:123], v[72:75], v[160:163]\n"
    "ds_read_b64_tr_b16 a[120:121], v3 offset:512\n"
    "ds_read_b64_tr_b16 a[122:123], v3 offset:4608\n"
    "v_mfma_f32_16x16x32_bf16 v[228:231], a[124:127], v[48:51], v[228:231]\n"
    "v_add_f32 v6, v6, v34\n"
    "v_mov_b32 v12, v9\n"
    "v_mfma_f32_16x16x32_bf16 v[100:103], a[124:127], v[56:59], v[100:103]\n"
    "v_exp_f32 v35, v35\n"
    "v_mfma_f32_16x16x32_bf16 v[132:135], a[124:127], v[64:67], v[132:135]\n"
    "v_add_f32 v6, v6, v35\n"
    "v_cvt_pk_bf16_f32 v33, v34, v35\n"
    "v_mfma_f32_16x16x32_bf16 v[164:167], a[124:127], v[72:75], v[164:167]\n"
    "s_waitcnt lgkmcnt(8)\n"
    "v_exp_f32 v40, v40\n"
    "v_mfma_f32_16x16x32_bf16 v[232:235], a[104:107], v[48:51], v[232:235]\n"
    "ds_read_b64_tr_b16 a[124:125], v3 offset:520\n"
    "ds_read_b64_tr_b16 a[126:127], v3 offset:4616\n"
    "v_mfma_f32_16x16x32_bf16 v[104:107], a[104:107], v[56:59], v[104:107]\n"
    "v_add_f32 v7, v7, v40\n"
    "v_permlane16_swap_b32 v9, v12\n"
    "v_mfma_f32_16x16x32_bf16 v[136:139], a[104:107], v[64:67], v[136:139]\n"
    "v_exp_f32 v41, v41\n"
    "v_mfma_f32_16x16x32_bf16 v[168:171], a[104:107], v[72:75], v[168:171]\n"
    "s_waitcnt lgkmcnt(8)\n"
    "ds_read_b64_tr_b16 a[104:105], v3 offset:1024\n"
    "ds_read_b64_tr_b16 a[106:107], v3 offset:5120\n"
    "v_mfma_f32_16x16x32_bf16 v[236:239], a[108:111], v[48:51], v[236:239]\n"
    "v_add_f32 v7, v7, v41\n"
    "v_cvt_pk_bf16_f32 v40, v40, v41\n"
    "v_mfma_f32_16x16x32_bf16 v[108:111], a[108:111], v[56:59], v[108:111]\n"
    "v_exp_f32 v42, v42\n"
    "v_mfma_f32_16x16x32_bf16 v[140:143], a[108:111], v[64:67], v[140:143]\n"
    "v_add_f32 v7, v7, v42\n"
    "v_add_f32 v12, v9, v12\n"
    "v_mfma_f32_16x16x32_bf16 v[172:175], a[108:111], v[72:75], v[172:175]\n"
    "v_exp_f32 v43, v43\n"
    "v_mfma_f32_16x16x32_bf16 v[56:59], a[88:91], a[0:3], v[244:247]\n"
    "ds_read_b64_tr_b16 a[108:109], v3 offset:1032\n"
    "ds_read_b64_tr_b16 a[110:111], v3 offset:5128\n"
    "v_mfma_f32_16x16x32_bf16 v[64:67], a[88:91], a[16:19], v[248:251]\n"
    "v_add_f32 v7, v7, v43\n"
    "v_cvt_pk_bf16_f32 v41, v42, v43\n"
    "v_mfma_f32_16x16x32_bf16 v[72:75], a[88:91], a[32:35], v[252:255]\n"
    "v_exp_f32 v16, v16\n"
    "v_mfma_f32_16x16x32_bf16 v[48:51], a[88:91], a[48:51], v[240:243]\n"
    "v_exp_f32 v17, v17\n"
    "v_mfma_f32_16x16x32_bf16 v[56:59], a[92:95], a[4:7], v[56:59]\n"
    "ds_read_b128 a[88:91], v1 offset:13312\n"
    "v_add_f32 v8, v16, v17\n"
    "v_mfma_f32_16x16x32_bf16 v[64:67], a[92:95], a[20:23], v[64:67]\n"
    "v_cvt_pk_bf16_f32 v16, v16, v17\n"
    "v_cmp_ngt_f32 vcc, 0x5d800000, v12\n"
    "v_mfma_f32_16x16x32_bf16 v[72:75], a[92:95], a[36:39], v[72:75]\n"
    "v_exp_f32 v18, v18\n"
    "v_mfma_f32_16x16x32_bf16 v[48:51], a[92:95], a[52:55], v[48:51]\n"
    "s_or_b64 s[64:65], s[64:65], vcc\n"
    "ds_read_b128 a[92:95], v1 offset:13824\n"
    "v_add_f32 v8, v8, v18\n"
    "v_mfma_f32_16x16x32_bf16 v[56:59], a[96:99], a[8:11], v[56:59]\n"
    "v_exp_f32 v19, v19\n"
    "v_mfma_f32_16x16x32_bf16 v[64:67], a[96:99], a[24:27], v[64:67]\n"
    "v_add_f32 v8, v8, v19\n"
    "v_cvt_pk_bf16_f32 v17, v18, v19\n"
    "v_mfma_f32_16x16x32_bf16 v[72:75], a[96:99], a[40:43], v[72:75]\n"
    "v_exp_f32 v28, v28\n"
    "v_mfma_f32_16x16x32_bf16 v[48:51], a[96:99], a[56:59], v[48:51]\n"
    "ds_read_b128 a[96:99], v1 offset:16384\n"
    "v_add_f32 v5, v5, v28\n"
    "v_mfma_f32_16x16x32_bf16 v[56:59], a[100:103], a[12:15], v[56:59]\n"
    "v_exp_f32 v29, v29\n"
    "v_mfma_f32_16x16x32_bf16 v[64:67], a[100:103], a[28:31], v[64:67]\n"
    "v_add_f32 v5, v5, v29\n"
    "v_cvt_pk_bf16_f32 v26, v28, v29\n"
    "v_mfma_f32_16x16x32_bf16 v[72:75], a[100:103], a[44:47], v[72:75]\n"
    "v_exp_f32 v30, v30\n"
    "v_mfma_f32_16x16x32_bf16 v[48:51], a[100:103], a[60:63], v[48:51]\n"
    "ds_read_b128 a[100:103], v1 offset:16896\n"
    "v_add_f32 v5, v5, v30\n"
    "v_mfma_f32_16x16x32_bf16 v[60:63], a[80:83], a[0:3], v[244:247]\n"
    "v_exp_f32 v31, v31\n"
    "v_mfma_f32_16x16x32_bf16 v[68:71], a[80:83], a[16:19], v[248:251]\n"
    "v_add_f32 v5, v5, v31\n"
    "v_cvt_pk_bf16_f32 v27, v30, v31\n"
    "v_mfma_f32_16x16x32_bf16 v[76:79], a[80:83], a[32:35], v[252:255]\n"
    "v_exp_f32 v36, v36\n"
    "v_mfma_f32_16x16x32_bf16 v[52:55], a[80:83], a[48:51], v[240:243]\n"
    "ds_read_b128 a[80:83], v1 offset:17408\n"
    "v_add_f32 v6, v6, v36\n"
    "v_mfma_f32_16x16x32_bf16 v[60:63], a[84:87], a[4:7], v[60:63]\n"
    "v_exp_f32 v37, v37\n"
    "v_mfma_f32_16x16x32_bf16 v[68:71], a[84:87], a[20:23], v[68:71]\n"
    "v_add_f32 v6, v6, v37\n"
    "v_cvt_pk_bf16_f32 v34, v36, v37\n"
    "v_mfma_f32_16x16x32_bf16 v[76:79], a[84:87], a[36:39], v[76:79]\n"
    "v_exp_f32 v38, v38\n"
    "v_mfma_f32_16x16x32_bf16 v[52:55], a[84:87], a[52:55], v[52:55]\n"
    "s_waitcnt lgkmcnt(4)\n"
    "ds_read_b128 a[84:87], v1 offset:17920\n"
    "v_add_f32 v6, v6, v38\n"
    "v_mfma_f32_16x16x32_bf16 v[60:63], a[88:91], a[8:11], v[60:63]\n"
    "v_exp_f32 v39, v39\n"
    "v_mfma_f32_16x16x32_bf16 v[68:71], a[88:91], a[24:27], v[68:71]\n"
    "v_add_f32 v6, v6, v39\n"
    "v_cvt_pk_bf16_f32 v35, v38, v39\n"
    "v_mfma_f32_16x16x32_bf16 v[76:79], a[88:91], a[40:43], v[76:79]\n"
    "v_exp_f32 v44, v44\n"
    "v_mfma_f32_16x16x32_bf16 v[52:55], a[88:91], a[56:59], v[52:55]\n"
    "s_waitcnt lgkmcnt(4)\n"
    "ds_read_b128 a[88:91], v1 offset:20480\n"
    "v_add_f32 v7, v7, v44\n"
    "v_mfma_f32_16x16x32_bf16 v[60:63], a[92:95], a[12:15], v[60:63]\n"
    "v_exp_f32 v45, v45\n"
    "v_mfma_f32_16x16x32_bf16 v[68:71], a[92:95], a[28:31], v[68:71]\n"
    "v_add_f32 v7, v7, v45\n"
    "v_cvt_pk_bf16_f32 v42, v44, v45\n"
    "v_mfma_f32_16x16x32_bf16 v[76:79], a[92:95], a[44:47], v[76:79]\n"
    "v_exp_f32 v46, v46\n"
    "v_mfma_f32_16x16x32_bf16 v[52:55], a[92:95], a[60:63], v[52:55]\n"
    "ds_read_b128 a[92:95], v1 offset:20992\n"
    "v_add_f32 v7, v7, v46\n"
    "v_mfma_f32_16x16x32_bf16 v[80:83], a[128:131], v[24:27], v[80:83]\n"
    "v_exp_f32 v47, v47\n"
    "v_mfma_f32_16x16x32_bf16 v[112:115], a[128:131], v[32:35], v[112:115]\n"
    "v_add_f32 v7, v7, v47\n"
    "v_cvt_pk_bf16_f32 v43, v46, v47\n"
    "v_exp_f32 v20, v20\n"
    "s_nop 0\n"
    "v_mfma_f32_16x16x32_bf16 v[144:147], a[128:131], v[40:43], v[144:147]\n"
    "v_add_f32 v8, v8, v20\n"
    "v_exp_f32 v21, v21\n"
    "v_exp_f32 v22, v22\n"
    "v_add_f32 v8, v8, v21\n"
    "v_cvt_pk_bf16_f32 v18, v20, v21\n"
    "v_add_f32 v8, v8, v22\n"
    "v_exp_f32 v23, v23\n"
    "v_rcp_f32 v12, v12\n"
    "v_add_f32 v8, v8, v23\n"
    "v_cvt_pk_bf16_f32 v19, v22, v23\n"
    "v_mul_f32 v208, v208, v12\n"
    "v_mul_f32 v209, v209, v12\n"
    "v_mfma_f32_16x16x32_bf16 v[176:179], a[128:131], v[16:19], 0\n"
    "v_mul_f32 v210, v210, v12\n"
    "v_mul_f32 v211, v211, v12\n"
    "v_mfma_f32_16x16x32_bf16 v[84:87], a[132:135], v[24:27], v[84:87]\n"
    "v_mul_f32 v212, v212, v12\n"
    "v_mul_f32 v213, v213, v12\n"
    "v_mfma_f32_16x16x32_bf16 v[116:119], a[132:135], v[32:35], v[116:119]\n"
    "v_mul_f32 v214, v214, v12\n"
    "v_mul_f32 v215, v215, v12\n"
    "v_mfma_f32_16x16x32_bf16 v[148:151], a[132:135], v[40:43], v[148:151]\n"
    "v_cvt_pk_bf16_f32 v208, v208, v209\n"
    "v_cvt_pk_bf16_f32 v209, v210, v211\n"
    "v_mfma_f32_16x16x32_bf16 v[180:183], a[132:135], v[16:19], 0\n"
    "v_cvt_pk_bf16_f32 v210, v212, v213\n"
    "v_cvt_pk_bf16_f32 v211, v214, v215\n"
    "v_mfma_f32_16x16x32_bf16 v[88:91], a[136:139], v[24:27], v[88:91]\n"
    "v_mul_f32 v216, v216, v12\n"
    "v_mul_f32 v217, v217, v12\n"
    "v_mfma_f32_16x16x32_bf16 v[120:123], a[136:139], v[32:35], v[120:123]\n"
    "v_mul_f32 v218, v218, v12\n"
    "v_mul_f32 v219, v219, v12\n"
    "v_mfma_f32_16x16x32_bf16 v[152:155], a[136:139], v[40:43], v[152:155]\n"
    "v_mul_f32 v220, v220, v12\n"
    "v_mul_f32 v221, v221, v12\n"
    "v_mfma_f32_16x16x32_bf16 v[184:187], a[136:139], v[16:19], 0\n"
    "v_mul_f32 v222, v222, v12\n"
    "v_mul_f32 v223, v223, v12\n"
    "v_mfma_f32_16x16x32_bf16 v[92:95], a[140:143], v[24:27], v[92:95]\n"
    "v_cvt_pk_bf16_f32 v216, v216, v217\n"
    "v_cvt_pk_bf16_f32 v217, v218, v219\n"
    "v_mfma_f32_16x16x32_bf16 v[124:127], a[140:143], v[32:35], v[124:127]\n"
    "v_cvt_pk_bf16_f32 v218, v220, v221\n"
    "v_cvt_pk_bf16_f32 v219, v222, v223\n"
    "v_mfma_f32_16x16x32_bf16 v[156:159], a[140:143], v[40:43], v[156:159]\n"
    "v_exp_f32 v56, v56\n"
    "v_mfma_f32_16x16x32_bf16 v[188:191], a[140:143], v[16:19], 0\n"
    "v_add_f32 v5, v5, v56\n"
    "v_mul_f32 v224, v224, v12\n"
    "v_mfma_f32_16x16x32_bf16 v[96:99], a[144:147], v[24:27], v[96:99]\n"
    "v_exp_f32 v57, v57\n"
    "v_mfma_f32_16x16x32_bf16 v[128:131], a[144:147], v[32:35], v[128:131]\n"
    "v_add_f32 v5, v5, v57\n"
    "v_cvt_pk_bf16_f32 v56, v56, v57\n"
    "v_mfma_f32_16x16x32_bf16 v[160:163], a[144:147], v[40:43], v[160:163]\n"
    "v_exp_f32 v58, v58\n"
    "v_mfma_f32_16x16x32_bf16 v[192:195], a[144:147], v[16:19], 0\n"
    "v_add_f32 v5, v5, v58\n"
    "v_mul_f32 v225, v225, v12\n"
    "v_mfma_f32_16x16x32_bf16 v[100:103], a[148:151], v[24:27], v[100:103]\n"
    "v_exp_f32 v59, v59\n"
    "v_mfma_f32_16x16x32_bf16 v[132:135], a[148:151], v[32:35], v[132:135]\n"
    "v_add_f32 v5, v5, v59\n"
    "v_cvt_pk_bf16_f32 v57, v58, v59\n"
    "v_mfma_f32_16x16x32_bf16 v[164:167], a[148:151], v[40:43], v[164:167]\n"
    "v_mul_f32 v226, v226, v12\n"
    "v_mul_f32 v227, v227, v12\n"
    "v_mfma_f32_16x16x32_bf16 v[196:199], a[148:151], v[16:19], 0\n"
    "v_mul_f32 v228, v228, v12\n"
    "v_mul_f32 v229, v229, v12\n"
    "v_mfma_f32_16x16x32_bf16 v[104:107], a[152:155], v[24:27], v[104:107]\n"
    "v_mul_f32 v230, v230, v12\n"
    "v_mul_f32 v231, v231, v12\n"
    "v_mfma_f32_16x16x32_bf16 v[136:139], a[152:155], v[32:35], v[136:139]\n"
    "v_cvt_pk_bf16_f32 v224, v224, v225\n"
    "v_cvt_pk_bf16_f32 v225, v226, v227\n"
    "v_mfma_f32_16x16x32_bf16 v[168:171], a[152:155], v[40:43], v[168:171]\n"
    "v_cvt_pk_bf16_f32 v226, v228, v229\n"
    "v_cvt_pk_bf16_f32 v227, v230, v231\n"
    "v_mfma_f32_16x16x32_bf16 v[200:203], a[152:155], v[16:19], 0\n"
    "v_mul_f32 v232, v232, v12\n"
    "v_mul_f32 v233, v233, v12\n"
    "v_mfma_f32_16x16x32_bf16 v[108:111], a[156:159], v[24:27], v[108:111]\n"
    "v_mul_f32 v234, v234, v12\n"
    "v_mul_f32 v235, v235, v12\n"
    "v_mfma_f32_16x16x32_bf16 v[140:143], a[156:159], v[32:35], v[140:143]\n"
    "v_mul_f32 v236, v236, v12\n"
    "v_mul_f32 v237, v237, v12\n"
    "v_mfma_f32_16x16x32_bf16 v[172:175], a[156:159], v[40:43], v[172:175]\n"
    "v_mul_f32 v238, v238, v12\n"
    "v_mul_f32 v239, v239, v12\n"
    "v_mfma_f32_16x16x32_bf16 v[204:207], a[156:159], v[16:19], 0\n"
    "s_waitcnt lgkmcnt(5)\n"
    "s_mov_b32 s77, 36864\n"
    "v_cvt_pk_bf16_f32 v232, v232, v233\n"
    "v_cvt_pk_bf16_f32 v233, v234, v235\n"
    "v_mfma_f32_16x16x32_bf16 v[24:27], a[96:99], a[0:3], v[244:247]\n"
    "buffer_load_dword v15, v14, s[72:75], s77 offen\n"
    "v_cvt_pk_bf16_f32 v234, v236, v237\n"
    "v_mfma_f32_16x16x32_bf16 v[32:35], a[96:99], a[16:19], v[248:251]\n"
    "v_cvt_pk_bf16_f32 v235, v238, v239\n"
    "v_mfma_f32_16x16x32_bf16 v[40:43], a[96:99], a[32:35], v[252:255]\n"
    "v_exp_f32 v64, v64\n"
    "v_mfma_f32_16x16x32_bf16 v[16:19], a[96:99], a[48:51], v[240:243]\n"
    "s_waitcnt lgkmcnt(4)\n"
    "ds_read_b128 a[96:99], v1 offset:21504\n"
    "v_add_f32 v6, v6, v64\n"
    "v_mfma_f32_16x16x32_bf16 v[24:27], a[100:103], a[4:7], v[24:27]\n"
    "v_exp_f32 v65, v65\n"
    "v_mfma_f32_16x16x32_bf16 v[32:35], a[100:103], a[20:23], v[32:35]\n"
    "v_add_f32 v6, v6, v65\n"
    "v_cvt_pk_bf16_f32 v64, v64, v65\n"
    "v_mfma_f32_16x16x32_bf16 v[40:43], a[100:103], a[36:39], v[40:43]\n"
    "v_exp_f32 v66, v66\n"
    "v_mfma_f32_16x16x32_bf16 v[16:19], a[100:103], a[52:55], v[16:19]\n"
    "s_waitcnt lgkmcnt(4)\n"
    "ds_read_b128 a[100:103], v1 offset:22016\n"
    "v_add_f32 v6, v6, v66\n"
    "v_mfma_f32_16x16x32_bf16 v[24:27], a[80:83], a[8:11], v[24:27]\n"
    "v_exp_f32 v67, v67\n"
    "v_mfma_f32_16x16x32_bf16 v[32:35], a[80:83], a[24:27], v[32:35]\n"
    "v_add_f32 v6, v6, v67\n"
    "v_cvt_pk_bf16_f32 v65, v66, v67\n"
    "v_mfma_f32_16x16x32_bf16 v[40:43], a[80:83], a[40:43], v[40:43]\n"
    "v_exp_f32 v72, v72\n"
    "v_mfma_f32_16x16x32_bf16 v[16:19], a[80:83], a[56:59], v[16:19]\n"
    "s_waitcnt lgkmcnt(4)\n"
    "ds_read_b128 a[80:83], v1 offset:24576\n"
    "v_add_f32 v7, v7, v72\n"
    "v_mfma_f32_16x16x32_bf16 v[24:27], a[84:87], a[12:15], v[24:27]\n"
    "v_exp_f32 v73, v73\n"
    "v_mfma_f32_16x16x32_bf16 v[32:35], a[84:87], a[28:31], v[32:35]\n"
    "v_add_f32 v7, v7, v73\n"
    "v_cvt_pk_bf16_f32 v72, v72, v73\n"
    "v_mfma_f32_16x16x32_bf16 v[40:43], a[84:87], a[44:47], v[40:43]\n"
    "v_exp_f32 v74, v74\n"
    "v_mfma_f32_16x16x32_bf16 v[16:19], a[84:87], a[60:63], v[16:19]\n"
    "s_waitcnt lgkmcnt(4)\n"
    "ds_read_b128 a[84:87], v1 offset:25088\n"
    "v_add_f32 v7, v7, v74\n"
    "v_mfma_f32_16x16x32_bf16 v[28:31], a[88:91], a[0:3], v[244:247]\n"
    "v_exp_f32 v75, v75\n"
    "v_mfma_f32_16x16x32_bf16 v[36:39], a[88:91], a[16:19], v[248:251]\n"
    "v_add_f32 v7, v7, v75\n"
    "v_cvt_pk_bf16_f32 v73, v74, v75\n"
    "v_mfma_f32_16x16x32_bf16 v[44:47], a[88:91], a[32:35], v[252:255]\n"
    "v_exp_f32 v48, v48\n"
    "v_mfma_f32_16x16x32_bf16 v[20:23], a[88:91], a[48:51], v[240:243]\n"
    "s_waitcnt lgkmcnt(4)\n"
    "ds_read_b128 a[88:91], v1 offset:25600\n"
    "v_add_f32 v8, v8, v48\n"
    "v_mfma_f32_16x16x32_bf16 v[28:31], a[92:95], a[4:7], v[28:31]\n"
    "v_exp_f32 v49, v49\n"
    "v_mfma_f32_16x16x32_bf16 v[36:39], a[92:95], a[20:23], v[36:39]\n"
    "v_add_f32 v8, v8, v49\n"
    "v_cvt_pk_bf16_f32 v48, v48, v49\n"
    "v_mfma_f32_16x16x32_bf16 v[44:47], a[92:95], a[36:39], v[44:47]\n"
    "v_exp_f32 v50, v50\n"
    "v_mfma_f32_16x16x32_bf16 v[20:23], a[92:95], a[52:55], v[20:23]\n"
    "s_waitcnt lgkmcnt(4)\n"
    "ds_read_b128 a[92:95], v1 offset:26112\n"
    "v_add_f32 v8, v8, v50\n"
    "v_mfma_f32_16x16x32_bf16 v[28:31], a[96:99], a[8:11], v[28:31]\n"
    "v_exp_f32 v51, v51\n"
    "v_mfma_f32_16x16x32_bf16 v[36:39], a[96:99], a[24:27], v[36:39]\n"
    "v_add_f32 v8, v8, v51\n"
    "v_cvt_pk_bf16_f32 v49, v50, v51\n"
    "v_mfma_f32_16x16x32_bf16 v[44:47], a[96:99], a[40:43], v[44:47]\n"
    "v_exp_f32 v60, v60\n"
    "v_mfma_f32_16x16x32_bf16 v[20:23], a[96:99], a[56:59], v[20:23]\n"
    "s_waitcnt lgkmcnt(4)\n"
    "ds_read_b128 a[96:99], v1 offset:28672\n"
    "v_add_f32 v5, v5, v60\n"
    "v_mfma_f32_16x16x32_bf16 v[28:31], a[100:103], a[12:15], v[28:31]\n"
    "v_exp_f32 v61, v61\n"
    "v_mfma_f32_16x16x32_bf16 v[36:39], a[100:103], a[28:31], v[36:39]\n"
    "buffer_load_dwordx4 a[0:3], v4, s[40:43], s60 offen offset:0\n"
    "buffer_load_dwordx4 a[4:7], v4, s[40:43], s60 offen offset:64\n"
    "v_mfma_f32_16x16x32_bf16 v[44:47], a[100:103], a[44:47], v[44:47]\n"
    "v_add_f32 v5, v5, v61\n"
    "v_cvt_pk_bf16_f32 v58, v60, v61\n"
    "v_mfma_f32_16x16x32_bf16 v[20:23], a[100:103], a[60:63], v[20:23]\n"
    "ds_read_b128 a[100:103], v1 offset:29184\n"
    "v_exp_f32 v62, v62\n"
    "v_exp_f32 v63, v63\n"
    "v_add_f32 v5, v5, v62\n"
    "v_add_f32 v5, v5, v63\n"
    "v_cvt_pk_bf16_f32 v59, v62, v63\n"
    "v_exp_f32 v68, v68\n"
    "s_nop 0\n"
    "v_mfma_f32_16x16x32_bf16 v[80:83], a[160:163], v[56:59], v[80:83]\n"
    "v_add_f32 v6, v6, v68\n"
    "v_exp_f32 v69, v69\n"
    "v_exp_f32 v70, v70\n"
    "v_add_f32 v6, v6, v69\n"
    "v_cvt_pk_bf16_f32 v66, v68, v69\n"
    "v_add_f32 v6, v6, v70\n"
    "v_exp_f32 v71, v71\n"
    "v_exp_f32 v76, v76\n"
    "v_add_f32 v6, v6, v71\n"
    "v_cvt_pk_bf16_f32 v67, v70, v71\n"
    "v_add_f32 v7, v7, v76\n"
    "s_nop 0\n"
    "v_mfma_f32_16x16x32_bf16 v[112:115], a[160:163], v[64:67], v[112:115]\n"
    "buffer_load_dwordx4 a[8:11], v4, s[40:43], s60 offen offset:128\n"
    "v_exp_f32 v77, v77\n"
    "v_exp_f32 v78, v78\n"
    "v_add_f32 v7, v7, v77\n"
    "v_cvt_pk_bf16_f32 v74, v76, v77\n"
    "v_add_f32 v7, v7, v78\n"
    "v_exp_f32 v79, v79\n"
    "v_exp_f32 v52, v52\n"
    "v_add_f32 v7, v7, v79\n"
    "v_cvt_pk_bf16_f32 v75, v78, v79\n"
    "v_add_f32 v8, v8, v52\n"
    "s_nop 0\n"
    "v_mfma_f32_16x16x32_bf16 v[144:147], a[160:163], v[72:75], v[144:147]\n"
    "buffer_load_dwordx4 a[12:15], v4, s[40:43], s60 offen offset:192\n"
    "v_exp_f32 v53, v53\n"
    "v_exp_f32 v54, v54\n"
    "v_add_f32 v8, v8, v53\n"
    "v_cvt_pk_bf16_f32 v50, v52, v53\n"
    "v_add_f32 v8, v8, v54\n"
    "v_exp_f32 v55, v55\n"
    "v_exp_f32 v24, v24\n"
    "v_add_f32 v8, v8, v55\n"
    "v_cvt_pk_bf16_f32 v51, v54, v55\n"
    "v_add_f32 v5, v5, v24\n"
    "s_nop 0\n"
    "v_mfma_f32_16x16x32_bf16 v[176:179], a[160:163], v[48:51], v[176:179]\n"
    "buffer_store_dwordx4 v[208:211], v4, s[52:55], s62 offen offset:0 sc0 sc1\n"
    "v_mfma_f32_16x16x32_bf16 v[84:87], a[164:167], v[56:59], v[84:87]\n"
    "v_exp_f32 v25, v25\n"
    "v_mfma_f32_16x16x32_bf16 v[116:119], a[164:167], v[64:67], v[116:119]\n"
    "v_add_f32 v5, v5, v25\n"
    "v_cvt_pk_bf16_f32 v24, v24, v25\n"
    "v_mfma_f32_16x16x32_bf16 v[148:151], a[164:167], v[72:75], v[148:151]\n"
    "v_exp_f32 v26, v26\n"
    "v_mfma_f32_16x16x32_bf16 v[180:183], a[164:167], v[48:51], v[180:183]\n"
    "v_add_f32 v5, v5, v26\n"
    "v_mfma_f32_16x16x32_bf16 v[88:91], a[168:171], v[56:59], v[88:91]\n"
    "v_exp_f32 v27, v27\n"
    "v_mfma_f32_16x16x32_bf16 v[120:123], a[168:171], v[64:67], v[120:123]\n"
    "buffer_store_dwordx4 v[216:219], v4, s[52:55], s62 offen offset:64 sc0 sc1\n"
    "v_add_f32 v5, v5, v27\n"
    "v_mfma_f32_16x16x32_bf16 v[152:155], a[168:171], v[72:75], v[152:155]\n"
    "v_cvt_pk_bf16_f32 v25, v26, v27\n"
    "v_mfma_f32_16x16x32_bf16 v[184:187], a[168:171], v[48:51], v[184:187]\n"
    "v_exp_f32 v32, v32\n"
    "v_mfma_f32_16x16x32_bf16 v[92:95], a[172:175], v[56:59], v[92:95]\n"
    "v_add_f32 v6, v6, v32\n"
    "v_mfma_f32_16x16x32_bf16 v[124:127], a[172:175], v[64:67], v[124:127]\n"
    "v_exp_f32 v33, v33\n"
    "v_mfma_f32_16x16x32_bf16 v[156:159], a[172:175], v[72:75], v[156:159]\n"
    "v_add_f32 v6, v6, v33\n"
    "v_cvt_pk_bf16_f32 v32, v32, v33\n"
    "v_mfma_f32_16x16x32_bf16 v[188:191], a[172:175], v[48:51], v[188:191]\n"
    "buffer_store_dwordx4 v[224:227], v4, s[52:55], s62 offen offset:128 sc0 sc1\n"
    "v_mfma_f32_16x16x32_bf16 v[96:99], a[176:179], v[56:59], v[96:99]\n"
    "v_exp_f32 v34, v34\n"
    "v_mfma_f32_16x16x32_bf16 v[128:131], a[176:179], v[64:67], v[128:131]\n"
    "v_add_f32 v6, v6, v34\n"
    "v_mfma_f32_16x16x32_bf16 v[160:163], a[176:179], v[72:75], v[160:163]\n"
    "v_exp_f32 v35, v35\n"
    "v_mfma_f32_16x16x32_bf16 v[192:195], a[176:179], v[48:51], v[192:195]\n"
    "v_add_f32 v6, v6, v35\n"
    "v_cvt_pk_bf16_f32 v33, v34, v35\n"
    "v_mfma_f32_16x16x32_bf16 v[100:103], a[180:183], v[56:59], v[100:103]\n"
    "v_exp_f32 v40, v40\n"
    "v_mfma_f32_16x16x32_bf16 v[132:135], a[180:183], v[64:67], v[132:135]\n"
    "buffer_store_dwordx4 v[232:235], v4, s[52:55], s62 offen offset:192 sc0 sc1\n"
    "v_add_f32 v7, v7, v40\n"
    "v_mfma_f32_16x16x32_bf16 v[164:167], a[180:183], v[72:75], v[164:167]\n"
    "v_exp_f32 v41, v41\n"
    "v_mfma_f32_16x16x32_bf16 v[196:199], a[180:183], v[48:51], v[196:199]\n"
    "v_add_f32 v7, v7, v41\n"
    "v_cvt_pk_bf16_f32 v40, v40, v41\n"
    "v_mfma_f32_16x16x32_bf16 v[104:107], a[184:187], v[56:59], v[104:107]\n"
    "v_exp_f32 v42, v42\n"
    "v_mfma_f32_16x16x32_bf16 v[136:139], a[184:187], v[64:67], v[136:139]\n"
    "v_add_f32 v7, v7, v42\n"
    "v_mfma_f32_16x16x32_bf16 v[168:171], a[184:187], v[72:75], v[168:171]\n"
    "v_exp_f32 v43, v43\n"
    "v_mfma_f32_16x16x32_bf16 v[200:203], a[184:187], v[48:51], v[200:203]\n"
    "v_add_f32 v7, v7, v43\n"
    "v_cvt_pk_bf16_f32 v41, v42, v43\n"
    "v_mfma_f32_16x16x32_bf16 v[108:111], a[188:191], v[56:59], v[108:111]\n"
    "v_exp_f32 v16, v16\n"
    "v_mfma_f32_16x16x32_bf16 v[140:143], a[188:191], v[64:67], v[140:143]\n"
    "v_add_f32 v8, v8, v16\n"
    "v_mfma_f32_16x16x32_bf16 v[172:175], a[188:191], v[72:75], v[172:175]\n"
    "v_exp_f32 v17, v17\n"
    "v_mfma_f32_16x16x32_bf16 v[204:207], a[188:191], v[48:51], v[204:207]\n"
    "s_waitcnt lgkmcnt(5)\n"
    "v_add_f32 v8, v8, v17\n"
    "v_cvt_pk_bf16_f32 v16, v16, v17\n"
    "v_mfma_f32_16x16x32_bf16 v[64:67], a[80:83], a[16:19], v[248:251]\n"
    "v_exp_f32 v18, v18\n"
    "v_mfma_f32_16x16x32_bf16 v[72:75], a[80:83], a[32:35], v[252:255]\n"
    "v_add_f32 v8, v8, v18\n"
    "v_mfma_f32_16x16x32_bf16 v[48:51], a[80:83], a[48:51], v[240:243]\n"
    "s_waitcnt vmcnt(16)\n"
    "v_exp_f32 v19, v19\n"
    "v_mfma_f32_16x16x32_bf16 v[56:59], a[80:83], a[64:67], 0\n"
    "s_waitcnt lgkmcnt(4)\n"
    "ds_read_b128 a[80:83], v1 offset:29696\n"
    "v_add_f32 v8, v8, v19\n"
    "v_mfma_f32_16x16x32_bf16 v[64:67], a[84:87], a[20:23], v[64:67]\n"
    "v_cvt_pk_bf16_f32 v17, v18, v19\n"
    "v_mfma_f32_16x16x32_bf16 v[72:75], a[84:87], a[36:39], v[72:75]\n"
    "v_exp_f32 v28, v28\n"
    "v_mfma_f32_16x16x32_bf16 v[48:51], a[84:87], a[52:55], v[48:51]\n"
    "s_waitcnt vmcnt(15)\n"
    "v_add_f32 v5, v5, v28\n"
    "v_mfma_f32_16x16x32_bf16 v[56:59], a[84:87], a[68:71], v[56:59]\n"
    "s_waitcnt lgkmcnt(4)\n"
    "v_exp_f32 v29, v29\n"
    "v_mfma_f32_16x16x32_bf16 v[64:67], a[88:91], a[24:27], v[64:67]\n"
    "ds_read_b128 a[84:87], v1 offset:30208\n"
    "v_add_f32 v5, v5, v29\n"
    "v_mfma_f32_16x16x32_bf16 v[72:75], a[88:91], a[40:43], v[72:75]\n"
    "v_cvt_pk_bf16_f32 v26, v28, v29\n"
    "v_mfma_f32_16x16x32_bf16 v[48:51], a[88:91], a[56:59], v[48:51]\n"
    "s_waitcnt vmcnt(14)\n"
    "v_exp_f32 v30, v30\n"
    "v_mfma_f32_16x16x32_bf16 v[56:59], a[88:91], a[72:75], v[56:59]\n"
    "s_waitcnt lgkmcnt(4)\n"
    "ds_read_b128 a[88:91], v1 offset:32768\n"
    "v_add_f32 v5, v5, v30\n"
    "v_mfma_f32_16x16x32_bf16 v[64:67], a[92:95], a[28:31], v[64:67]\n"
    "v_exp_f32 v31, v31\n"
    "v_mfma_f32_16x16x32_bf16 v[72:75], a[92:95], a[44:47], v[72:75]\n"
    "v_add_f32 v5, v5, v31\n"
    "v_cvt_pk_bf16_f32 v27, v30, v31\n"
    "v_mfma_f32_16x16x32_bf16 v[48:51], a[92:95], a[60:63], v[48:51]\n"
    "s_waitcnt vmcnt(13)\n"
    "v_exp_f32 v36, v36\n"
    "v_mfma_f32_16x16x32_bf16 v[56:59], a[92:95], a[76:79], v[56:59]\n"
    "s_waitcnt lgkmcnt(4)\n"
    "ds_read_b128 a[92:95], v1 offset:33280\n"
    "v_add_f32 v6, v6, v36\n"
    "v_mfma_f32_16x16x32_bf16 v[68:71], a[96:99], a[16:19], v[248:251]\n"
    "v_exp_f32 v37, v37\n"
    "v_mfma_f32_16x16x32_bf16 v[76:79], a[96:99], a[32:35], v[252:255]\n"
    "v_add_f32 v6, v6, v37\n"
    "v_cvt_pk_bf16_f32 v34, v36, v37\n"
    "v_mfma_f32_16x16x32_bf16 v[52:55], a[96:99], a[48:51], v[240:243]\n"
    "v_exp_f32 v38, v38\n"
    "v_mfma_f32_16x16x32_bf16 v[60:63], a[96:99], a[64:67], 0\n"
    "s_waitcnt lgkmcnt(4)\n"
    "ds_read_b128 a[96:99], v1 offset:33792\n"
    "v_add_f32 v6, v6, v38\n"
    "v_mfma_f32_16x16x32_bf16 v[68:71], a[100:103], a[20:23], v[68:71]\n"
    "v_exp_f32 v39, v39\n"
    "v_mfma_f32_16x16x32_bf16 v[76:79], a[100:103], a[36:39], v[76:79]\n"
    "v_add_f32 v6, v6, v39\n"
    "v_cvt_pk_bf16_f32 v35, v38, v39\n"
    "v_mfma_f32_16x16x32_bf16 v[52:55], a[100:103], a[52:55], v[52:55]\n"
    "v_exp_f32 v44, v44\n"
    "v_mfma_f32_16x16x32_bf16 v[60:63], a[100:103], a[68:71], v[60:63]\n"
    "s_waitcnt lgkmcnt(4)\n"
    "ds_read_b128 a[100:103], v1 offset:34304\n"
    "v_add_f32 v7, v7, v44\n"
    "v_mfma_f32_16x16x32_bf16 v[68:71], a[80:83], a[24:27], v[68:71]\n"
    "v_exp_f32 v45, v45\n"
    "v_mfma_f32_16x16x32_bf16 v[76:79], a[80:83], a[40:43], v[76:79]\n"
    "v_add_f32 v7, v7, v45\n"
    "v_cvt_pk_bf16_f32 v42, v44, v45\n"
    "v_mfma_f32_16x16x32_bf16 v[52:55], a[80:83], a[56:59], v[52:55]\n"
    "v_exp_f32 v46, v46\n"
    "v_mfma_f32_16x16x32_bf16 v[60:63], a[80:83], a[72:75], v[60:63]\n"
    "s_waitcnt lgkmcnt(4)\n"
    "ds_read_b128 a[80:83], v1 offset:36864\n"
    "v_add_f32 v7, v7, v46\n"
    "v_mfma_f32_16x16x32_bf16 v[68:71], a[84:87], a[28:31], v[68:71]\n"
    "v_exp_f32 v47, v47\n"
    "v_mfma_f32_16x16x32_bf16 v[76:79], a[84:87], a[44:47], v[76:79]\n"
    "v_add_f32 v7, v7, v47\n"
    "v_cvt_pk_bf16_f32 v43, v46, v47\n"
    "v_mfma_f32_16x16x32_bf16 v[52:55], a[84:87], a[60:63], v[52:55]\n"
    "v_max3_f32 v10, v56, v57, v58\n"
    "v_mov_b32 v12, v5\n"
    "v_mfma_f32_16x16x32_bf16 v[60:63], a[84:87], a[76:79], v[60:63]\n"
    "s_mov_b32 s63, 81920\n"
    "v_exp_f32 v20, v20\n"
    "v_mfma_f32_16x16x32_bf16 v[80:83], a[192:195], v[24:27], v[80:83]\n"
    "ds_read_b128 a[84:87], v1 offset:37376\n"
    "v_add_f32 v8, v8, v20\n"
    "v_mfma_f32_16x16x32_bf16 v[112:115], a[192:195], v[32:35], v[112:115]\n"
    "v_exp_f32 v21, v21\n"
    "v_mfma_f32_16x16x32_bf16 v[144:147], a[192:195], v[40:43], v[144:147]\n"
    "v_max3_f32 v11, v60, v61, v62\n"
    "v_max3_f32 v10, v10, v59, v63\n"
    "v_max_f32 v10, v10, v11\n"
    "v_mov_b32 v11, v10\n"
    "v_add_f32 v8, v8, v21\n"
    "v_cvt_pk_bf16_f32 v18, v20, v21\n"
    "v_permlane32_swap_b32 v10, v11\n"
    "v_exp_f32 v22, v22\n"
    "v_max_f32 v10, v10, v11\n"
    "v_mov_b32 v11, v10\n"
    "v_add_f32 v8, v8, v22\n"
    "v_exp_f32 v23, v23\n"
    "v_permlane16_swap_b32 v10, v11\n"
    "v_add_f32 v8, v8, v23\n"
    "v_min_f32 v244, -v10, -v11\n"
    "v_min_f32 v245, -v10, -v11\n"
    "v_min_f32 v246, -v10, -v11\n"
    "v_min_f32 v247, -v10, -v11\n"
    "v_add_f32 v56, v56, v244\n"
    "v_add_f32 v57, v57, v244\n"
    "v_add_f32 v58, v58, v244\n"
    "v_add_f32 v59, v59, v244\n"
    "v_add_f32 v60, v60, v244\n"
    "v_add_f32 v61, v61, v244\n"
    "v_add_f32 v62, v62, v244\n"
    "v_add_f32 v63, v63, v244\n"
    "v_cvt_pk_bf16_f32 v19, v22, v23\n"
    "v_exp_f32 v64, v64\n"
    "s_nop 0\n"
    "v_mfma_f32_16x16x32_bf16 v[176:179], a[192:195], v[16:19], v[176:179]\n"
    "v_add_f32 v6, v6, v64\n"
    "v_permlane32_swap_b32 v5, v12\n"
    "v_mfma_f32_16x16x32_bf16 v[84:87], a[196:199], v[24:27], v[84:87]\n"
    "v_exp_f32 v65, v65\n"
    "v_mfma_f32_16x16x32_bf16 v[116:119], a[196:199], v[32:35], v[116:119]\n"
    "v_add_f32 v6, v6, v65\n"
    "v_cvt_pk_bf16_f32 v64, v64, v65\n"
    "v_mfma_f32_16x16x32_bf16 v[148:151], a[196:199], v[40:43], v[148:151]\n"
    "v_exp_f32 v66, v66\n"
    "v_mfma_f32_16x16x32_bf16 v[180:183], a[196:199], v[16:19], v[180:183]\n"
    "v_add_f32 v6, v6, v66\n"
    "v_add_f32 v5, v5, v12\n"
    "v_mfma_f32_16x16x32_bf16 v[88:91], a[200:203], v[24:27], v[88:91]\n"
    "v_exp_f32 v67, v67\n"
    "v_mfma_f32_16x16x32_bf16 v[120:123], a[200:203], v[32:35], v[120:123]\n"
    "v_add_f32 v6, v6, v67\n"
    "v_cvt_pk_bf16_f32 v65, v66, v67\n"
    "v_mfma_f32_16x16x32_bf16 v[152:155], a[200:203], v[40:43], v[152:155]\n"
    "v_exp_f32 v72, v72\n"
    "v_mfma_f32_16x16x32_bf16 v[184:187], a[200:203], v[16:19], v[184:187]\n"
    "v_add_f32 v7, v7, v72\n"
    "v_mov_b32 v12, v5\n"
    "v_mfma_f32_16x16x32_bf16 v[92:95], a[204:207], v[24:27], v[92:95]\n"
    "v_exp_f32 v73, v73\n"
    "v_mfma_f32_16x16x32_bf16 v[124:127], a[204:207], v[32:35], v[124:127]\n"
    "v_add_f32 v7, v7, v73\n"
    "v_cvt_pk_bf16_f32 v72, v72, v73\n"
    "v_mfma_f32_16x16x32_bf16 v[156:159], a[204:207], v[40:43], v[156:159]\n"
    "v_exp_f32 v74, v74\n"
    "v_mfma_f32_16x16x32_bf16 v[188:191], a[204:207], v[16:19], v[188:191]\n"
    "v_add_f32 v7, v7, v74\n"
    "v_permlane16_swap_b32 v5, v12\n"
    "v_mfma_f32_16x16x32_bf16 v[96:99], a[208:211], v[24:27], v[96:99]\n"
    "v_exp_f32 v75, v75\n"
    "v_mfma_f32_16x16x32_bf16 v[128:131], a[208:211], v[32:35], v[128:131]\n"
    "v_add_f32 v7, v7, v75\n"
    "v_cvt_pk_bf16_f32 v73, v74, v75\n"
    "v_mfma_f32_16x16x32_bf16 v[160:163], a[208:211], v[40:43], v[160:163]\n"
    "v_exp_f32 v48, v48\n"
    "v_mfma_f32_16x16x32_bf16 v[192:195], a[208:211], v[16:19], v[192:195]\n"
    "v_add_f32 v8, v8, v48\n"
    "v_add_f32 v12, v5, v12\n"
    "v_mfma_f32_16x16x32_bf16 v[100:103], a[212:215], v[24:27], v[100:103]\n"
    "v_exp_f32 v49, v49\n"
    "v_mfma_f32_16x16x32_bf16 v[132:135], a[212:215], v[32:35], v[132:135]\n"
    "v_add_f32 v8, v8, v49\n"
    "v_cvt_pk_bf16_f32 v48, v48, v49\n"
    "v_mfma_f32_16x16x32_bf16 v[164:167], a[212:215], v[40:43], v[164:167]\n"
    "v_exp_f32 v50, v50\n"
    "v_mfma_f32_16x16x32_bf16 v[196:199], a[212:215], v[16:19], v[196:199]\n"
    "v_add_f32 v8, v8, v50\n"
    "v_cmp_ngt_f32 vcc, 0x5d800000, v12\n"
    "v_mfma_f32_16x16x32_bf16 v[104:107], a[216:219], v[24:27], v[104:107]\n"
    "v_exp_f32 v51, v51\n"
    "v_mfma_f32_16x16x32_bf16 v[136:139], a[216:219], v[32:35], v[136:139]\n"
    "s_or_b64 s[64:65], s[64:65], vcc\n"
    "v_add_f32 v8, v8, v51\n"
    "v_cvt_pk_bf16_f32 v49, v50, v51\n"
    "v_mfma_f32_16x16x32_bf16 v[168:171], a[216:219], v[40:43], v[168:171]\n"
    "v_exp_f32 v56, v56\n"
    "v_mfma_f32_16x16x32_bf16 v[200:203], a[216:219], v[16:19], v[200:203]\n"
    "v_exp_f32 v57, v57\n"
    "v_mfma_f32_16x16x32_bf16 v[108:111], a[220:223], v[24:27], v[108:111]\n"
    "v_add_f32 v9, v56, v57\n"
    "v_cvt_pk_bf16_f32 v56, v56, v57\n"
    "v_mfma_f32_16x16x32_bf16 v[140:143], a[220:223], v[32:35], v[140:143]\n"
    "v_exp_f32 v58, v58\n"
    "v_mfma_f32_16x16x32_bf16 v[172:175], a[220:223], v[40:43], v[172:175]\n"
    "v_add_f32 v9, v9, v58\n"
    "v_mfma_f32_16x16x32_bf16 v[204:207], a[220:223], v[16:19], v[204:207]\n"
    "s_waitcnt lgkmcnt(5)\n"
    "v_exp_f32 v59, v59\n"
    "v_mfma_f32_16x16x32_bf16 v[32:35], a[88:91], a[16:19], v[248:251]\n"
    "v_add_f32 v9, v9, v59\n"
    "v_cvt_pk_bf16_f32 v57, v58, v59\n"
    "v_mfma_f32_16x16x32_bf16 v[40:43], a[88:91], a[32:35], v[252:255]\n"
    "v_exp_f32 v68, v68\n"
    "v_mfma_f32_16x16x32_bf16 v[16:19], a[88:91], a[48:51], v[240:243]\n"
    "v_add_f32 v6, v6, v68\n"
    "v_mfma_f32_16x16x32_bf16 v[24:27], a[88:91], a[64:67], v[244:247]\n"
    "s_waitcnt lgkmcnt(4)\n"
    "v_exp_f32 v69, v69\n"
    "v_mfma_f32_16x16x32_bf16 v[32:35], a[92:95], a[20:23], v[32:35]\n"
    "ds_read_b128 a[88:91], v1 offset:37888\n"
    "v_add_f32 v6, v6, v69\n"
    "v_mfma_f32_16x16x32_bf16 v[40:43], a[92:95], a[36:39], v[40:43]\n"
    "v_cvt_pk_bf16_f32 v66, v68, v69\n"
    "v_mfma_f32_16x16x32_bf16 v[16:19], a[92:95], a[52:55], v[16:19]\n"
    "v_exp_f32 v70, v70\n"
    "v_mfma_f32_16x16x32_bf16 v[24:27], a[92:95], a[68:71], v[24:27]\n"
    "s_waitcnt lgkmcnt(4)\n"
    "ds_read_b128 a[92:95], v1 offset:38400\n"
    "v_add_f32 v6, v6, v70\n"
    "v_mfma_f32_16x16x32_bf16 v[32:35], a[96:99], a[24:27], v[32:35]\n"
    "v_exp_f32 v71, v71\n"
    "v_mfma_f32_16x16x32_bf16 v[40:43], a[96:99], a[40:43], v[40:43]\n"
    "v_add_f32 v6, v6, v71\n"
    "v_cvt_pk_bf16_f32 v67, v70, v71\n"
    "v_mfma_f32_16x16x32_bf16 v[16:19], a[96:99], a[56:59], v[16:19]\n"
    "v_exp_f32 v76, v76\n"
    "v_mfma_f32_16x16x32_bf16 v[24:27], a[96:99], a[72:75], v[24:27]\n"
    "s_waitcnt lgkmcnt(4)\n"
    "ds_read_b128 a[96:99], v1 offset:40960\n"
    "v_add_f32 v7, v7, v76\n"
    "v_mfma_f32_16x16x32_bf16 v[32:35], a[100:103], a[28:31], v[32:35]\n"
    "v_exp_f32 v77, v77\n"
    "v_mfma_f32_16x16x32_bf16 v[40:43], a[100:103], a[44:47], v[40:43]\n"
    "v_add_f32 v7, v7, v77\n"
    "v_cvt_pk_bf16_f32 v74, v76, v77\n"
    "v_mfma_f32_16x16x32_bf16 v[16:19], a[100:103], a[60:63], v[16:19]\n"
    "v_exp_f32 v78, v78\n"
    "v_mfma_f32_16x16x32_bf16 v[24:27], a[100:103], a[76:79], v[24:27]\n"
    "s_waitcnt lgkmcnt(4)\n"
    "ds_read_b128 a[100:103], v1 offset:41472\n"
    "v_add_f32 v7, v7, v78\n"
    "v_mfma_f32_16x16x32_bf16 v[36:39], a[80:83], a[16:19], v[248:251]\n"
    "v_exp_f32 v79, v79\n"
    "v_mfma_f32_16x16x32_bf16 v[44:47], a[80:83], a[32:35], v[252:255]\n"
    "v_add_f32 v7, v7, v79\n"
    "v_cvt_pk_bf16_f32 v75, v78, v79\n"
    "v_mfma_f32_16x16x32_bf16 v[20:23], a[80:83], a[48:51], v[240:243]\n"
    "v_exp_f32 v52, v52\n"
    "v_mfma_f32_16x16x32_bf16 v[28:31], a[80:83], a[64:67], v[244:247]\n"
    "s_waitcnt lgkmcnt(4)\n"
    "ds_read_b128 a[80:83], v1 offset:41984\n"
    "v_add_f32 v8, v8, v52\n"
    "v_mfma_f32_16x16x32_bf16 v[36:39], a[84:87], a[20:23], v[36:39]\n"
    "v_exp_f32 v53, v53\n"
    "v_mfma_f32_16x16x32_bf16 v[44:47], a[84:87], a[36:39], v[44:47]\n"
    "v_add_f32 v8, v8, v53\n"
    "v_cvt_pk_bf16_f32 v50, v52, v53\n"
    "v_mfma_f32_16x16x32_bf16 v[20:23], a[84:87], a[52:55], v[20:23]\n"
    "v_exp_f32 v54, v54\n"
    "v_mfma_f32_16x16x32_bf16 v[28:31], a[84:87], a[68:71], v[28:31]\n"
    "s_waitcnt lgkmcnt(4)\n"
    "ds_read_b128 a[84:87], v1 offset:42496\n"
    "v_add_f32 v8, v8, v54\n"
    "v_mfma_f32_16x16x32_bf16 v[36:39], a[88:91], a[24:27], v[36:39]\n"
    "v_exp_f32 v55, v55\n"
    "v_mfma_f32_16x16x32_bf16 v[44:47], a[88:91], a[40:43], v[44:47]\n"
    "v_add_f32 v8, v8, v55\n"
    "v_cvt_pk_bf16_f32 v51, v54, v55\n"
    "v_mfma_f32_16x16x32_bf16 v[20:23], a[88:91], a[56:59], v[20:23]\n"
    "v_exp_f32 v60, v60\n"
    "v_mfma_f32_16x16x32_bf16 v[28:31], a[88:91], a[72:75], v[28:31]\n"
    "s_waitcnt lgkmcnt(4)\n"
    "ds_read_b128 a[88:91], v1 offset:45056\n"
    "v_add_f32 v9, v9, v60\n"
    "v_mfma_f32_16x16x32_bf16 v[36:39], a[92:95], a[28:31], v[36:39]\n"
    "v_exp_f32 v61, v61\n"
    "v_mfma_f32_16x16x32_bf16 v[44:47], a[92:95], a[44:47], v[44:47]\n"
    "v_add_f32 v9, v9, v61\n"
    "v_cvt_pk_bf16_f32 v58, v60, v61\n"
    "v_mfma_f32_16x16x32_bf16 v[20:23], a[92:95], a[60:63], v[20:23]\n"
    "v_exp_f32 v62, v62\n"
    "v_mfma_f32_16x16x32_bf16 v[28:31], a[92:95], a[76:79], v[28:31]\n"
    "ds_read_b128 a[92:95], v1 offset:45568\n"
    "v_add_f32 v9, v9, v62\n"
    "v_mfma_f32_16x16x32_bf16 v[112:115], a[224:227], v[64:67], v[112:115]\n"
    "v_exp_f32 v63, v63\n"
    "v_mfma_f32_16x16x32_bf16 v[144:147], a[224:227], v[72:75], v[144:147]\n"
    "v_add_f32 v9, v9, v63\n"
    "v_cvt_pk_bf16_f32 v59, v62, v63\n"
    "v_mfma_f32_16x16x32_bf16 v[176:179], a[224:227], v[48:51], v[176:179]\n"
    "v_rcp_f32 v12, v12\n"
    "v_mfma_f32_16x16x32_bf16 v[208:211], a[224:227], v[56:59], 0\n"
    "v_mul_f32 v80, v80, v12\n"
    "v_mul_f32 v81, v81, v12\n"
    "v_mfma_f32_16x16x32_bf16 v[116:119], a[228:231], v[64:67], v[116:119]\n"
    "v_mul_f32 v82, v82, v12\n"
    "v_mul_f32 v83, v83, v12\n"
    "v_mfma_f32_16x16x32_bf16 v[148:151], a[228:231], v[72:75], v[148:151]\n"
    "v_mul_f32 v84, v84, v12\n"
    "v_mul_f32 v85, v85, v12\n"
    "v_mfma_f32_16x16x32_bf16 v[180:183], a[228:231], v[48:51], v[180:183]\n"
    "v_mul_f32 v86, v86, v12\n"
    "v_mul_f32 v87, v87, v12\n"
    "v_mfma_f32_16x16x32_bf16 v[212:215], a[228:231], v[56:59], 0\n"
    "v_cvt_pk_bf16_f32 v80, v80, v81\n"
    "v_cvt_pk_bf16_f32 v81, v82, v83\n"
    "v_mfma_f32_16x16x32_bf16 v[120:123], a[232:235], v[64:67], v[120:123]\n"
    "v_cvt_pk_bf16_f32 v82, v84, v85\n"
    "v_cvt_pk_bf16_f32 v83, v86, v87\n"
    "v_mfma_f32_16x16x32_bf16 v[152:155], a[232:235], v[72:75], v[152:155]\n"
    "v_mul_f32 v88, v88, v12\n"
    "v_mul_f32 v89, v89, v12\n"
    "v_mfma_f32_16x16x32_bf16 v[184:187], a[232:235], v[48:51], v[184:187]\n"
    "v_mul_f32 v90, v90, v12\n"
    "v_mul_f32 v91, v91, v12\n"
    "v_mfma_f32_16x16x32_bf16 v[216:219], a[232:235], v[56:59], 0\n"
    "v_mul_f32 v92, v92, v12\n"
    "v_mul_f32 v93, v93, v12\n"
    "v_mfma_f32_16x16x32_bf16 v[124:127], a[236:239], v[64:67], v[124:127]\n"
    "v_mul_f32 v94, v94, v12\n"
    "v_mul_f32 v95, v95, v12\n"
    "v_mfma_f32_16x16x32_bf16 v[156:159], a[236:239], v[72:75], v[156:159]\n"
    "v_cvt_pk_bf16_f32 v88, v88, v89\n"
    "v_cvt_pk_bf16_f32 v89, v90, v91\n"
    "v_mfma_f32_16x16x32_bf16 v[188:191], a[236:239], v[48:51], v[188:191]\n"
    "v_cvt_pk_bf16_f32 v90, v92, v93\n"
    "v_cvt_pk_bf16_f32 v91, v94, v95\n"
    "v_mfma_f32_16x16x32_bf16 v[220:223], a[236:239], v[56:59], 0\n"
    "v_exp_f32 v32, v32\n"
    "v_mfma_f32_16x16x32_bf16 v[128:131], a[240:243], v[64:67], v[128:131]\n"
    "v_add_f32 v6, v6, v32\n"
    "v_mul_f32 v96, v96, v12\n"
    "v_mfma_f32_16x16x32_bf16 v[160:163], a[240:243], v[72:75], v[160:163]\n"
    "v_exp_f32 v33, v33\n"
    "v_mfma_f32_16x16x32_bf16 v[192:195], a[240:243], v[48:51], v[192:195]\n"
    "v_add_f32 v6, v6, v33\n"
    "v_cvt_pk_bf16_f32 v32, v32, v33\n"
    "v_mfma_f32_16x16x32_bf16 v[224:227], a[240:243], v[56:59], 0\n"
    "v_exp_f32 v34, v34\n"
    "v_mfma_f32_16x16x32_bf16 v[132:135], a[244:247], v[64:67], v[132:135]\n"
    "v_add_f32 v6, v6, v34\n"
    "v_mul_f32 v97, v97, v12\n"
    "v_mfma_f32_16x16x32_bf16 v[164:167], a[244:247], v[72:75], v[164:167]\n"
    "v_exp_f32 v35, v35\n"
    "v_mfma_f32_16x16x32_bf16 v[196:199], a[244:247], v[48:51], v[196:199]\n"
    "v_add_f32 v6, v6, v35\n"
    "v_cvt_pk_bf16_f32 v33, v34, v35\n"
    "v_mfma_f32_16x16x32_bf16 v[228:231], a[244:247], v[56:59], 0\n"
    "v_mul_f32 v98, v98, v12\n"
    "v_mul_f32 v99, v99, v12\n"
    "v_mfma_f32_16x16x32_bf16 v[136:139], a[248:251], v[64:67], v[136:139]\n"
    "v_mul_f32 v100, v100, v12\n"
    "v_mul_f32 v101, v101, v12\n"
    "v_mfma_f32_16x16x32_bf16 v[168:171], a[248:251], v[72:75], v[168:171]\n"
    "v_mul_f32 v102, v102, v12\n"
    "v_mul_f32 v103, v103, v12\n"
    "v_mfma_f32_16x16x32_bf16 v[200:203], a[248:251], v[48:51], v[200:203]\n"
    "v_cvt_pk_bf16_f32 v96, v96, v97\n"
    "v_cvt_pk_bf16_f32 v97, v98, v99\n"
    "v_mfma_f32_16x16x32_bf16 v[232:235], a[248:251], v[56:59], 0\n"
    "v_cvt_pk_bf16_f32 v98, v100, v101\n"
    "v_cvt_pk_bf16_f32 v99, v102, v103\n"
    "v_mfma_f32_16x16x32_bf16 v[140:143], a[252:255], v[64:67], v[140:143]\n"
    "v_mul_f32 v104, v104, v12\n"
    "v_mul_f32 v105, v105, v12\n"
    "v_mfma_f32_16x16x32_bf16 v[172:175], a[252:255], v[72:75], v[172:175]\n"
    "v_mul_f32 v106, v106, v12\n"
    "v_mul_f32 v107, v107, v12\n"
    "v_mfma_f32_16x16x32_bf16 v[204:207], a[252:255], v[48:51], v[204:207]\n"
    "v_mul_f32 v108, v108, v12\n"
    "v_mul_f32 v109, v109, v12\n"
    "v_mfma_f32_16x16x32_bf16 v[236:239], a[252:255], v[56:59], 0\n"
    "s_waitcnt lgkmcnt(5)\n"
    "v_mul_f32 v110, v110, v12\n"
    "v_mul_f32 v111, v111, v12\n"
    "v_mfma_f32_16x16x32_bf16 v[64:67], a[96:99], a[16:19], v[248:251]\n"
    "v_cvt_pk_bf16_f32 v104, v104, v105\n"
    "v_cvt_pk_bf16_f32 v105, v106, v107\n"
    "v_mfma_f32_16x16x32_bf16 v[72:75], a[96:99], a[32:35], v[252:255]\n"
    "v_cvt_pk_bf16_f32 v106, v108, v109\n"
    "v_cvt_pk_bf16_f32 v107, v110, v111\n"
    "v_mfma_f32_16x16x32_bf16 v[48:51], a[96:99], a[48:51], v[240:243]\n"
    "v_exp_f32 v40, v40\n"
    "v_mfma_f32_16x16x32_bf16 v[56:59], a[96:99], a[64:67], v[244:247]\n"
    "s_waitcnt lgkmcnt(4)\n"
    "ds_read_b128 a[96:99], v1 offset:46080\n"
    "v_add_f32 v7, v7, v40\n"
    "v_mfma_f32_16x16x32_bf16 v[64:67], a[100:103], a[20:23], v[64:67]\n"
    "v_exp_f32 v41, v41\n"
    "v_mfma_f32_16x16x32_bf16 v[72:75], a[100:103], a[36:39], v[72:75]\n"
    "v_add_f32 v7, v7, v41\n"
    "v_cvt_pk_bf16_f32 v40, v40, v41\n"
    "v_mfma_f32_16x16x32_bf16 v[48:51], a[100:103], a[52:55], v[48:51]\n"
    "v_exp_f32 v42, v42\n"
    "v_mfma_f32_16x16x32_bf16 v[56:59], a[100:103], a[68:71], v[56:59]\n"
    "s_waitcnt lgkmcnt(4)\n"
    "ds_read_b128 a[100:103], v1 offset:46592\n"
    "v_add_f32 v7, v7, v42\n"
    "v_mfma_f32_16x16x32_bf16 v[64:67], a[80:83], a[24:27], v[64:67]\n"
    "v_exp_f32 v43, v43\n"
    "v_mfma_f32_16x16x32_bf16 v[72:75], a[80:83], a[40:43], v[72:75]\n"
    "v_add_f32 v7, v7, v43\n"
    "v_cvt_pk_bf16_f32 v41, v42, v43\n"
    "v_mfma_f32_16x16x32_bf16 v[48:51], a[80:83], a[56:59], v[48:51]\n"
    "v_exp_f32 v16, v16\n"
    "v_mfma_f32_16x16x32_bf16 v[56:59], a[80:83], a[72:75], v[56:59]\n"
    "s_waitcnt lgkmcnt(4)\n"
    "ds_read_b128 a[80:83], v1 offset:49152\n"
    "v_add_f32 v8, v8, v16\n"
    "v_mfma_f32_16x16x32_bf16 v[64:67], a[84:87], a[28:31], v[64:67]\n"
    "v_exp_f32 v17, v17\n"
    "v_mfma_f32_16x16x32_bf16 v[72:75], a[84:87], a[44:47], v[72:75]\n"
    "v_add_f32 v8, v8, v17\n"
    "v_cvt_pk_bf16_f32 v16, v16, v17\n"
    "v_mfma_f32_16x16x32_bf16 v[48:51], a[84:87], a[60:63], v[48:51]\n"
    "v_exp_f32 v18, v18\n"
    "v_mfma_f32_16x16x32_bf16 v[56:59], a[84:87], a[76:79], v[56:59]\n"
    "s_waitcnt lgkmcnt(4)\n"
    "ds_read_b128 a[84:87], v1 offset:49664\n"
    "v_add_f32 v8, v8, v18\n"
    "v_mfma_f32_16x16x32_bf16 v[68:71], a[88:91], a[16:19], v[248:251]\n"
    "v_exp_f32 v19, v19\n"
    "v_mfma_f32_16x16x32_bf16 v[76:79], a[88:91], a[32:35], v[252:255]\n"
    "v_add_f32 v8, v8, v19\n"
    "v_cvt_pk_bf16_f32 v17, v18, v19\n"
    "v_mfma_f32_16x16x32_bf16 v[52:55], a[88:91], a[48:51], v[240:243]\n"
    "v_exp_f32 v24, v24\n"
    "v_mfma_f32_16x16x32_bf16 v[60:63], a[88:91], a[64:67], v[244:247]\n"
    "s_waitcnt lgkmcnt(4)\n"
    "ds_read_b128 a[88:91], v1 offset:50176\n"
    "v_add_f32 v9, v9, v24\n"
    "v_mfma_f32_16x16x32_bf16 v[68:71], a[92:95], a[20:23], v[68:71]\n"
    "v_exp_f32 v25, v25\n"
    "v_mfma_f32_16x16x32_bf16 v[76:79], a[92:95], a[36:39], v[76:79]\n"
    "v_add_f32 v9, v9, v25\n"
    "v_cvt_pk_bf16_f32 v24, v24, v25\n"
    "v_mfma_f32_16x16x32_bf16 v[52:55], a[92:95], a[52:55], v[52:55]\n"
    "v_exp_f32 v26, v26\n"
    "v_mfma_f32_16x16x32_bf16 v[60:63], a[92:95], a[68:71], v[60:63]\n"
    "s_waitcnt lgkmcnt(4)\n"
    "ds_read_b128 a[92:95], v1 offset:50688\n"
    "v_add_f32 v9, v9, v26\n"
    "v_mfma_f32_16x16x32_bf16 v[68:71], a[96:99], a[24:27], v[68:71]\n"
    "v_exp_f32 v27, v27\n"
    "v_mfma_f32_16x16x32_bf16 v[76:79], a[96:99], a[40:43], v[76:79]\n"
    "v_add_f32 v9, v9, v27\n"
    "v_cvt_pk_bf16_f32 v25, v26, v27\n"
    "v_mfma_f32_16x16x32_bf16 v[52:55], a[96:99], a[56:59], v[52:55]\n"
    "v_exp_f32 v36, v36\n"
    "v_mfma_f32_16x16x32_bf16 v[60:63], a[96:99], a[72:75], v[60:63]\n"
    "s_waitcnt lgkmcnt(4)\n"
    "ds_read_b128 a[96:99], v1 offset:53248\n"
    "v_add_f32 v6, v6, v36\n"
    "v_mfma_f32_16x16x32_bf16 v[68:71], a[100:103], a[28:31], v[68:71]\n"
    "v_exp_f32 v37, v37\n"
    "v_mfma_f32_16x16x32_bf16 v[76:79], a[100:103], a[44:47], v[76:79]\n"
    "buffer_load_dwordx4 a[16:19], v4, s[40:43], s61 offen offset:0\n"
    "buffer_load_dwordx4 a[20:23], v4, s[40:43], s61 offen offset:64\n"
    "v_mfma_f32_16x16x32_bf16 v[52:55], a[100:103], a[60:63], v[52:55]\n"
    "v_add_f32 v6, v6, v37\n"
    "v_cvt_pk_bf16_f32 v34, v36, v37\n"
    "v_mfma_f32_16x16x32_bf16 v[60:63], a[100:103], a[76:79], v[60:63]\n"
    "ds_read_b128 a[100:103], v1 offset:53760\n"
    "v_exp_f32 v38, v38\n"
    "v_exp_f32 v39, v39\n"
    "v_add_f32 v6, v6, v38\n"
    "v_add_f32 v6, v6, v39\n"
    "v_cvt_pk_bf16_f32 v35, v38, v39\n"
    "v_exp_f32 v44, v44\n"
    "s_nop 0\n"
    "v_mfma_f32_16x16x32_bf16 v[112:115], a[112:115], v[32:35], v[112:115]\n"
    "v_add_f32 v7, v7, v44\n"
    "v_exp_f32 v45, v45\n"
    "v_exp_f32 v46, v46\n"
    "v_add_f32 v7, v7, v45\n"
    "v_cvt_pk_bf16_f32 v42, v44, v45\n"
    "v_add_f32 v7, v7, v46\n"
    "v_exp_f32 v47, v47\n"
    "v_exp_f32 v20, v20\n"
    "v_add_f32 v7, v7, v47\n"
    "v_cvt_pk_bf16_f32 v43, v46, v47\n"
    "v_add_f32 v8, v8, v20\n"
    "s_nop 0\n"
    "v_mfma_f32_16x16x32_bf16 v[144:147], a[112:115], v[40:43], v[144:147]\n"
    "buffer_load_dwordx4 a[24:27], v4, s[40:43], s61 offen offset:128\n"
    "v_exp_f32 v21, v21\n"
    "v_exp_f32 v22, v22\n"
    "v_add_f32 v8, v8, v21\n"
    "v_cvt_pk_bf16_f32 v18, v20, v21\n"
    "v_add_f32 v8, v8, v22\n"
    "v_exp_f32 v23, v23\n"
    "v_exp_f32 v28, v28\n"
    "v_add_f32 v8, v8, v23\n"
    "v_cvt_pk_bf16_f32 v19, v22, v23\n"
    "v_add_f32 v9, v9, v28\n"
    "s_nop 0\n"
    "v_mfma_f32_16x16x32_bf16 v[176:179], a[112:115], v[16:19], v[176:179]\n"
    "buffer_load_dwordx4 a[28:31], v4, s[40:43], s61 offen offset:192\n"
    "v_exp_f32 v29, v29\n"
    "v_exp_f32 v30, v30\n"
    "v_add_f32 v9, v9, v29\n"
    "v_cvt_pk_bf16_f32 v26, v28, v29\n"
    "v_add_f32 v9, v9, v30\n"
    "v_exp_f32 v31, v31\n"
    "v_exp_f32 v64, v64\n"
    "v_add_f32 v9, v9, v31\n"
    "v_cvt_pk_bf16_f32 v27, v30, v31\n"
    "v_add_f32 v6, v6, v64\n"
    "s_nop 0\n"
    "v_mfma_f32_16x16x32_bf16 v[208:211], a[112:115], v[24:27], v[208:211]\n"
    "buffer_store_dwordx4 v[80:83], v4, s[52:55], s63 offen offset:0 sc0 sc1\n"
    "ds_read_b64_tr_b16 a[112:113], v3 offset:1536\n"
    "v_mfma_f32_16x16x32_bf16 v[116:119], a[116:119], v[32:35], v[116:119]\n"
    "v_exp_f32 v65, v65\n"
    "v_mfma_f32_16x16x32_bf16 v[148:151], a[116:119], v[40:43], v[148:151]\n"
    "ds_read_b64_tr_b16 a[114:115], v3 offset:5632\n"
    "v_add_f32 v6, v6, v65\n"
    "v_mfma_f32_16x16x32_bf16 v[180:183], a[116:119], v[16:19], v[180:183]\n"
    "v_cvt_pk_bf16_f32 v64, v64, v65\n"
    "v_mfma_f32_16x16x32_bf16 v[212:215], a[116:119], v[24:27], v[212:215]\n"
    "v_exp_f32 v66, v66\n"
    "v_mfma_f32_16x16x32_bf16 v[120:123], a[120:123], v[32:35], v[120:123]\n"
    "ds_read_b64_tr_b16 a[116:117], v3 offset:1544\n"
    "ds_read_b64_tr_b16 a[118:119], v3 offset:5640\n"
    "v_mfma_f32_16x16x32_bf16 v[152:155], a[120:123], v[40:43], v[152:155]\n"
    "buffer_store_dwordx4 v[88:91], v4, s[52:55], s63 offen offset:64 sc0 sc1\n"
    "v_add_f32 v6, v6, v66\n"
    "v_mfma_f32_16x16x32_bf16 v[184:187], a[120:123], v[16:19], v[184:187]\n"
    "v_exp_f32 v67, v67\n"
    "v_mfma_f32_16x16x32_bf16 v[216:219], a[120:123], v[24:27], v[216:219]\n"
    "ds_read_b64_tr_b16 a[120:121], v3 offset:8192\n"
    "ds_read_b64_tr_b16 a[122:123], v3 offset:12288\n"
    "v_mfma_f32_16x16x32_bf16 v[124:127], a[124:127], v[32:35], v[124:127]\n"
    "v_add_f32 v6, v6, v67\n"
    "v_cvt_pk_bf16_f32 v65, v66, v67\n"
    "v_mfma_f32_16x16x32_bf16 v[156:159], a[124:127], v[40:43], v[156:159]\n"
    "v_exp_f32 v72, v72\n"
    "v_mfma_f32_16x16x32_bf16 v[188:191], a[124:127], v[16:19], v[188:191]\n"
    "v_add_f32 v7, v7, v72\n"
    "v_mfma_f32_16x16x32_bf16 v[220:223], a[124:127], v[24:27], v[220:223]\n"
    "buffer_store_dwordx4 v[96:99], v4, s[52:55], s63 offen offset:128 sc0 sc1\n"
    "ds_read_b64_tr_b16 a[124:125], v3 offset:8200\n"
    "v_mfma_f32_16x16x32_bf16 v[128:131], a[104:107], v[32:35], v[128:131]\n"
    "v_exp_f32 v73, v73\n"
    "v_mfma_f32_16x16x32_bf16 v[160:163], a[104:107], v[40:43], v[160:163]\n"
    "ds_read_b64_tr_b16 a[126:127], v3 offset:12296\n"
    "v_add_f32 v7, v7, v73\n"
    "v_mfma_f32_16x16x32_bf16 v[192:195], a[104:107], v[16:19], v[192:195]\n"
    "v_cvt_pk_bf16_f32 v72, v72, v73\n"
    "v_mfma_f32_16x16x32_bf16 v[224:227], a[104:107], v[24:27], v[224:227]\n"
    "v_exp_f32 v74, v74\n"
    "v_mfma_f32_16x16x32_bf16 v[132:135], a[108:111], v[32:35], v[132:135]\n"
    "ds_read_b64_tr_b16 a[104:105], v3 offset:8704\n"
    "ds_read_b64_tr_b16 a[106:107], v3 offset:12800\n"
    "v_mfma_f32_16x16x32_bf16 v[164:167], a[108:111], v[40:43], v[164:167]\n"
    "buffer_store_dwordx4 v[104:107], v4, s[52:55], s63 offen offset:192 sc0 sc1\n"
    "v_add_f32 v7, v7, v74\n"
    "v_mfma_f32_16x16x32_bf16 v[196:199], a[108:111], v[16:19], v[196:199]\n"
    "v_exp_f32 v75, v75\n"
    "v_mfma_f32_16x16x32_bf16 v[228:231], a[108:111], v[24:27], v[228:231]\n"
    "s_waitcnt lgkmcnt(8)\n"
    "ds_read_b64_tr_b16 a[108:109], v3 offset:8712\n"
    "ds_read_b64_tr_b16 a[110:111], v3 offset:12808\n"
    "v_mfma_f32_16x16x32_bf16 v[136:139], a[112:115], v[32:35], v[136:139]\n"
    "v_add_f32 v7, v7, v75\n"
    "v_cvt_pk_bf16_f32 v73, v74, v75\n"
    "v_mfma_f32_16x16x32_bf16 v[168:171], a[112:115], v[40:43], v[168:171]\n"
    "v_exp_f32 v48, v48\n"
    "v_mfma_f32_16x16x32_bf16 v[200:203], a[112:115], v[16:19], v[200:203]\n"
    "v_add_f32 v8, v8, v48\n"
    "v_mfma_f32_16x16x32_bf16 v[232:235], a[112:115], v[24:27], v[232:235]\n"
    "s_waitcnt lgkmcnt(8)\n"
    "v_exp_f32 v49, v49\n"
    "v_mfma_f32_16x16x32_bf16 v[140:143], a[116:119], v[32:35], v[140:143]\n"
    "ds_read_b64_tr_b16 a[112:113], v3 offset:9216\n"
    "ds_read_b64_tr_b16 a[114:115], v3 offset:13312\n"
    "v_mfma_f32_16x16x32_bf16 v[172:175], a[116:119], v[40:43], v[172:175]\n"
    "v_add_f32 v8, v8, v49\n"
    "v_cvt_pk_bf16_f32 v48, v48, v49\n"
    "v_mfma_f32_16x16x32_bf16 v[204:207], a[116:119], v[16:19], v[204:207]\n"
    "v_exp_f32 v50, v50\n"
    "v_mfma_f32_16x16x32_bf16 v[236:239], a[116:119], v[24:27], v[236:239]\n"
    "s_mov_b32 s76, 40960\n"
    "ds_read_b64_tr_b16 a[116:117], v3 offset:9224\n"
    "ds_read_b64_tr_b16 a[118:119], v3 offset:13320\n"
    "v_mfma_f32_16x16x32_bf16 v[40:43], a[80:83], a[32:35], v[252:255]\n"
    "buffer_load_dword v15, v14, s[72:75], s76 offen\n"
    "v_add_f32 v8, v8, v50\n"
    "v_mfma_f32_16x16x32_bf16 v[16:19], a[80:83], a[48:51], v[240:243]\n"
    "v_exp_f32 v51, v51\n"
    "v_mfma_f32_16x16x32_bf16 v[24:27], a[80:83], a[64:67], v[244:247]\n"
    "s_waitcnt vmcnt(16)\n"
    "v_add_f32 v8, v8, v51\n"
    "v_cvt_pk_bf16_f32 v49, v50, v51\n"
    "v_mfma_f32_16x16x32_bf16 v[32:35], a[80:83], a[0:3], 0\n"
    "v_exp_f32 v56, v56\n"
    "v_mfma_f32_16x16x32_bf16 v[40:43], a[84:87], a[36:39], v[40:43]\n"
    "ds_read_b128 a[80:83], v1 offset:54272\n"
    "v_add_f32 v9, v9, v56\n"
    "v_mfma_f32_16x16x32_bf16 v[16:19], a[84:87], a[52:55], v[16:19]\n"
    "v_exp_f32 v57, v57\n"
    "v_mfma_f32_16x16x32_bf16 v[24:27], a[84:87], a[68:71], v[24:27]\n"
    "s_waitcnt vmcnt(15)\n"
    "v_add_f32 v9, v9, v57\n"
    "v_cvt_pk_bf16_f32 v56, v56, v57\n"
    "v_mfma_f32_16x16x32_bf16 v[32:35], a[84:87], a[4:7], v[32:35]\n"
    "v_exp_f32 v58, v58\n"
    "v_mfma_f32_16x16x32_bf16 v[40:43], a[88:91], a[40:43], v[40:43]\n"
    "ds_read_b128 a[84:87], v1 offset:54784\n"
    "v_add_f32 v9, v9, v58\n"
    "v_mfma_f32_16x16x32_bf16 v[16:19], a[88:91], a[56:59], v[16:19]\n"
    "v_exp_f32 v59, v59\n"
    "v_mfma_f32_16x16x32_bf16 v[24:27], a[88:91], a[72:75], v[24:27]\n"
    "s_waitcnt vmcnt(14)\n"
    "v_add_f32 v9, v9, v59\n"
    "v_cvt_pk_bf16_f32 v57, v58, v59\n"
    "v_mfma_f32_16x16x32_bf16 v[32:35], a[88:91], a[8:11], v[32:35]\n"
    "v_exp_f32 v68, v68\n"
    "v_mfma_f32_16x16x32_bf16 v[40:43], a[92:95], a[44:47], v[40:43]\n"
    "ds_read_b128 a[88:91], v1 offset:57344\n"
    "v_add_f32 v6, v6, v68\n"
    "v_mfma_f32_16x16x32_bf16 v[16:19], a[92:95], a[60:63], v[16:19]\n"
    "v_exp_f32 v69, v69\n"
    "v_mfma_f32_16x16x32_bf16 v[24:27], a[92:95], a[76:79], v[24:27]\n"
    "s_waitcnt vmcnt(13)\n"
    "v_add_f32 v6, v6, v69\n"
    "v_cvt_pk_bf16_f32 v66, v68, v69\n"
    "v_mfma_f32_16x16x32_bf16 v[32:35], a[92:95], a[12:15], v[32:35]\n"
    "v_exp_f32 v70, v70\n"
    "v_mfma_f32_16x16x32_bf16 v[44:47], a[96:99], a[32:35], v[252:255]\n"
    "ds_read_b128 a[92:95], v1 offset:57856\n"
    "v_add_f32 v6, v6, v70\n"
    "v_mfma_f32_16x16x32_bf16 v[20:23], a[96:99], a[48:51], v[240:243]\n"
    "v_exp_f32 v71, v71\n"
    "v_mfma_f32_16x16x32_bf16 v[28:31], a[96:99], a[64:67], v[244:247]\n"
    "v_add_f32 v6, v6, v71\n"
    "v_cvt_pk_bf16_f32 v67, v70, v71\n"
    "v_mfma_f32_16x16x32_bf16 v[36:39], a[96:99], a[0:3], 0\n"
    "v_exp_f32 v76, v76\n"
    "v_mfma_f32_16x16x32_bf16 v[44:47], a[100:103], a[36:39], v[44:47]\n"
    "ds_read_b128 a[96:99], v1 offset:58368\n"
    "v_add_f32 v7, v7, v76\n"
    "v_mfma_f32_16x16x32_bf16 v[20:23], a[100:103], a[52:55], v[20:23]\n"
    "v_exp_f32 v77, v77\n"
    "v_mfma_f32_16x16x32_bf16 v[28:31], a[100:103], a[68:71], v[28:31]\n"
    "v_add_f32 v7, v7, v77\n"
    "v_cvt_pk_bf16_f32 v74, v76, v77\n"
    "v_mfma_f32_16x16x32_bf16 v[36:39], a[100:103], a[4:7], v[36:39]\n"
    "s_waitcnt lgkmcnt(4)\n"
    "v_exp_f32 v78, v78\n"
    "v_mfma_f32_16x16x32_bf16 v[44:47], a[80:83], a[40:43], v[44:47]\n"
    "ds_read_b128 a[100:103], v1 offset:58880\n"
    "v_add_f32 v7, v7, v78\n"
    "v_mfma_f32_16x16x32_bf16 v[20:23], a[80:83], a[56:59], v[20:23]\n"
    "v_exp_f32 v79, v79\n"
    "v_mfma_f32_16x16x32_bf16 v[28:31], a[80:83], a[72:75], v[28:31]\n"
    "v_add_f32 v7, v7, v79\n"
    "v_cvt_pk_bf16_f32 v75, v78, v79\n"
    "v_mfma_f32_16x16x32_bf16 v[36:39], a[80:83], a[8:11], v[36:39]\n"
    "s_waitcnt lgkmcnt(4)\n"
    "v_exp_f32 v52, v52\n"
    "v_mfma_f32_16x16x32_bf16 v[44:47], a[84:87], a[44:47], v[44:47]\n"
    "ds_read_b128 a[80:83], v1 offset:61440\n"
    "v_add_f32 v8, v8, v52\n"
    "v_mfma_f32_16x16x32_bf16 v[20:23], a[84:87], a[60:63], v[20:23]\n"
    "v_exp_f32 v53, v53\n"
    "v_mfma_f32_16x16x32_bf16 v[28:31], a[84:87], a[76:79], v[28:31]\n"
    "v_add_f32 v8, v8, v53\n"
    "v_cvt_pk_bf16_f32 v50, v52, v53\n"
    "v_mfma_f32_16x16x32_bf16 v[36:39], a[84:87], a[12:15], v[36:39]\n"
    "v_exp_f32 v54, v54\n"
    "v_mfma_f32_16x16x32_bf16 v[112:115], a[120:123], v[64:67], v[112:115]\n"
    "ds_read_b128 a[84:87], v1 offset:61952\n"
    "v_add_f32 v8, v8, v54\n"
    "v_mfma_f32_16x16x32_bf16 v[144:147], a[120:123], v[72:75], v[144:147]\n"
    "v_exp_f32 v55, v55\n"
    "v_max3_f32 v10, v32, v33, v34\n"
    "v_add_f32 v8, v8, v55\n"
    "v_cvt_pk_bf16_f32 v51, v54, v55\n"
    "v_max3_f32 v11, v36, v37, v38\n"
    "v_max3_f32 v10, v10, v35, v39\n"
    "v_mfma_f32_16x16x32_bf16 v[176:179], a[120:123], v[48:51], v[176:179]\n"
    "v_max_f32 v10, v10, v11\n"
    "v_mov_b32 v11, v10\n"
    "v_exp_f32 v60, v60\n"
    "s_nop 0\n"
    "v_permlane32_swap_b32 v10, v11\n"
    "v_add_f32 v9, v9, v60\n"
    "v_max_f32 v10, v10, v11\n"
    "v_mov_b32 v11, v10\n"
    "v_exp_f32 v61, v61\n"
    "s_nop 0\n"
    "v_permlane16_swap_b32 v10, v11\n"
    "v_add_f32 v9, v9, v61\n"
    "v_min_f32 v248, -v10, -v11\n"
    "v_min_f32 v249, -v10, -v11\n"
    "v_min_f32 v250, -v10, -v11\n"
    "v_min_f32 v251, -v10, -v11\n"
    "v_add_f32 v32, v32, v248\n"
    "v_add_f32 v33, v33, v248\n"
    "v_add_f32 v34, v34, v248\n"
    "v_add_f32 v35, v35, v248\n"
    "v_add_f32 v36, v36, v248\n"
    "v_add_f32 v37, v37, v248\n"
    "v_add_f32 v38, v38, v248\n"
    "v_add_f32 v39, v39, v248\n"
    "v_cvt_pk_bf16_f32 v58, v60, v61\n"
    "v_exp_f32 v62, v62\n"
    "v_exp_f32 v63, v63\n"
    "v_add_f32 v9, v9, v62\n"
    "v_add_f32 v9, v9, v63\n"
    "v_cvt_pk_bf16_f32 v59, v62, v63\n"
    "v_exp_f32 v40, v40\n"
    "s_nop 0\n"
    "v_mfma_f32_16x16x32_bf16 v[208:211], a[120:123], v[56:59], v[208:211]\n"
    "ds_read_b64_tr_b16 a[120:121], v3 offset:9728\n"
    "ds_read_b64_tr_b16 a[122:123], v3 offset:13824\n"
    "v_mfma_f32_16x16x32_bf16 v[116:119], a[124:127], v[64:67], v[116:119]\n"
    "v_add_f32 v7, v7, v40\n"
    "v_mov_b32 v12, v6\n"
    "v_mfma_f32_16x16x32_bf16 v[148:151], a[124:127], v[72:75], v[148:151]\n"
    "s_mov_b32 s62, 98304\n"
    "v_exp_f32 v41, v41\n"
    "v_mfma_f32_16x16x32_bf16 v[180:183], a[124:127], v[48:51], v[180:183]\n"
    "v_add_f32 v7, v7, v41\n"
    "v_cvt_pk_bf16_f32 v40, v40, v41\n"
    "v_mfma_f32_16x16x32_bf16 v[212:215], a[124:127], v[56:59], v[212:215]\n"
    "v_exp_f32 v42, v42\n"
    "v_mfma_f32_16x16x32_bf16 v[120:123], a[104:107], v[64:67], v[120:123]\n"
    "ds_read_b64_tr_b16 a[124:125], v3 offset:9736\n"
    "ds_read_b64_tr_b16 a[126:127], v3 offset:13832\n"
    "v_mfma_f32_16x16x32_bf16 v[152:155], a[104:107], v[72:75], v[152:155]\n"
    "v_add_f32 v7, v7, v42\n"
    "v_permlane32_swap_b32 v6, v12\n"
    "v_mfma_f32_16x16x32_bf16 v[184:187], a[104:107], v[48:51], v[184:187]\n"
    "v_exp_f32 v43, v43\n"
    "v_mfma_f32_16x16x32_bf16 v[216:219], a[104:107], v[56:59], v[216:219]\n"
    "ds_read_b64_tr_b16 a[104:105], v3 offset:16384\n"
    "ds_read_b64_tr_b16 a[106:107], v3 offset:20480\n"
    "v_mfma_f32_16x16x32_bf16 v[124:127], a[108:111], v[64:67], v[124:127]\n"
    "v_add_f32 v7, v7, v43\n"
    "v_cvt_pk_bf16_f32 v41, v42, v43\n"
    "v_mfma_f32_16x16x32_bf16 v[156:159], a[108:111], v[72:75], v[156:159]\n"
    "v_exp_f32 v16, v16\n"
    "v_mfma_f32_16x16x32_bf16 v[188:191], a[108:111], v[48:51], v[188:191]\n"
    "v_add_f32 v8, v8, v16\n"
    "v_add_f32 v6, v6, v12\n"
    "v_mfma_f32_16x16x32_bf16 v[220:223], a[108:111], v[56:59], v[220:223]\n"
    "v_exp_f32 v17, v17\n"
    "v_mfma_f32_16x16x32_bf16 v[128:131], a[112:115], v[64:67], v[128:131]\n"
    "ds_read_b64_tr_b16 a[108:109], v3 offset:16392\n"
    "ds_read_b64_tr_b16 a[110:111], v3 offset:20488\n"
    "v_mfma_f32_16x16x32_bf16 v[160:163], a[112:115], v[72:75], v[160:163]\n"
    "v_add_f32 v8, v8, v17\n"
    "v_cvt_pk_bf16_f32 v16, v16, v17\n"
    "v_mfma_f32_16x16x32_bf16 v[192:195], a[112:115], v[48:51], v[192:195]\n"
    "v_exp_f32 v18, v18\n"
    "v_mfma_f32_16x16x32_bf16 v[224:227], a[112:115], v[56:59], v[224:227]\n"
    "ds_read_b64_tr_b16 a[112:113], v3 offset:16896\n"
    "ds_read_b64_tr_b16 a[114:115], v3 offset:20992\n"
    "v_mfma_f32_16x16x32_bf16 v[132:135], a[116:119], v[64:67], v[132:135]\n"
    "v_add_f32 v8, v8, v18\n"
    "v_mov_b32 v12, v6\n"
    "v_mfma_f32_16x16x32_bf16 v[164:167], a[116:119], v[72:75], v[164:167]\n"
    "v_exp_f32 v19, v19\n"
    "v_mfma_f32_16x16x32_bf16 v[196:199], a[116:119], v[48:51], v[196:199]\n"
    "v_add_f32 v8, v8, v19\n"
    "v_cvt_pk_bf16_f32 v17, v18, v19\n"
    "v_mfma_f32_16x16x32_bf16 v[228:231], a[116:119], v[56:59], v[228:231]\n"
    "s_waitcnt lgkmcnt(8)\n"
    "v_exp_f32 v24, v24\n"
    "v_mfma_f32_16x16x32_bf16 v[136:139], a[120:123], v[64:67], v[136:139]\n"
    "ds_read_b64_tr_b16 a[116:117], v3 offset:16904\n"
    "ds_read_b64_tr_b16 a[118:119], v3 offset:21000\n"
    "v_mfma_f32_16x16x32_bf16 v[168:171], a[120:123], v[72:75], v[168:171]\n"
    "v_add_f32 v9, v9, v24\n"
    "v_permlane16_swap_b32 v6, v12\n"
    "v_mfma_f32_16x16x32_bf16 v[200:203], a[120:123], v[48:51], v[200:203]\n"
    "v_exp_f32 v25, v25\n"
    "v_mfma_f32_16x16x32_bf16 v[232:235], a[120:123], v[56:59], v[232:235]\n"
    "s_waitcnt lgkmcnt(8)\n"
    "ds_read_b64_tr_b16 a[120:121], v3 offset:17408\n"
    "ds_read_b64_tr_b16 a[122:123], v3 offset:21504\n"
    "v_mfma_f32_16x16x32_bf16 v[140:143], a[124:127], v[64:67], v[140:143]\n"
    "v_add_f32 v9, v9, v25\n"
    "v_cvt_pk_bf16_f32 v24, v24, v25\n"
    "v_mfma_f32_16x16x32_bf16 v[172:175], a[124:127], v[72:75], v[172:175]\n"
    "v_exp_f32 v26, v26\n"
    "v_mfma_f32_16x16x32_bf16 v[204:207], a[124:127], v[48:51], v[204:207]\n"
    "v_add_f32 v9, v9, v26\n"
    "v_add_f32 v12, v6, v12\n"
    "v_mfma_f32_16x16x32_bf16 v[236:239], a[124:127], v[56:59], v[236:239]\n"
    "v_exp_f32 v27, v27\n"
    "v_mfma_f32_16x16x32_bf16 v[72:75], a[88:91], a[32:35], v[252:255]\n"
    "ds_read_b64_tr_b16 a[124:125], v3 offset:17416\n"
    "ds_read_b64_tr_b16 a[126:127], v3 offset:21512\n"
    "v_mfma_f32_16x16x32_bf16 v[48:51], a[88:91], a[48:51], v[240:243]\n"
    "v_add_f32 v9, v9, v27\n"
    "v_cvt_pk_bf16_f32 v25, v26, v27\n"
    "v_mfma_f32_16x16x32_bf16 v[56:59], a[88:91], a[64:67], v[244:247]\n"
    "v_exp_f32 v32, v32\n"
    "v_mfma_f32_16x16x32_bf16 v[64:67], a[88:91], a[0:3], v[248:251]\n"
    "v_exp_f32 v33, v33\n"
    "v_mfma_f32_16x16x32_bf16 v[72:75], a[92:95], a[36:39], v[72:75]\n"
    "ds_read_b128 a[88:91], v1 offset:62464\n"
    "v_add_f32 v5, v32, v33\n"
    "v_mfma_f32_16x16x32_bf16 v[48:51], a[92:95], a[52:55], v[48:51]\n"
    "v_cvt_pk_bf16_f32 v32, v32, v33\n"
    "v_cmp_ngt_f32 vcc, 0x5d800000, v12\n"
    "v_mfma_f32_16x16x32_bf16 v[56:59], a[92:95], a[68:71], v[56:59]\n"
    "v_exp_f32 v34, v34\n"
    "v_mfma_f32_16x16x32_bf16 v[64:67], a[92:95], a[4:7], v[64:67]\n"
    "s_or_b64 s[64:65], s[64:65], vcc\n"
    "ds_read_b128 a[92:95], v1 offset:62976\n"
    "v_add_f32 v5, v5, v34\n"
    "v_mfma_f32_16x16x32_bf16 v[72:75], a[96:99], a[40:43], v[72:75]\n"
    "v_exp_f32 v35, v35\n"
    "v_mfma_f32_16x16x32_bf16 v[48:51], a[96:99], a[56:59], v[48:51]\n"
    "v_add_f32 v5, v5, v35\n"
    "v_cvt_pk_bf16_f32 v33, v34, v35\n"
    "v_mfma_f32_16x16x32_bf16 v[56:59], a[96:99], a[72:75], v[56:59]\n"
    "v_exp_f32 v44, v44\n"
    "v_mfma_f32_16x16x32_bf16 v[64:67], a[96:99], a[8:11], v[64:67]\n"
    "ds_read_b128 a[96:99], v2 offset:0\n"
    "v_add_f32 v7, v7, v44\n"
    "v_mfma_f32_16x16x32_bf16 v[72:75], a[100:103], a[44:47], v[72:75]\n"
    "v_exp_f32 v45, v45\n"
    "v_mfma_f32_16x16x32_bf16 v[48:51], a[100:103], a[60:63], v[48:51]\n"
    "v_add_f32 v7, v7, v45\n"
    "v_cvt_pk_bf16_f32 v42, v44, v45\n"
    "v_mfma_f32_16x16x32_bf16 v[56:59], a[100:103], a[76:79], v[56:59]\n"
    "v_exp_f32 v46, v46\n"
    "v_mfma_f32_16x16x32_bf16 v[64:67], a[100:103], a[12:15], v[64:67]\n"
    "ds_read_b128 a[100:103], v2 offset:512\n"
    "v_add_f32 v7, v7, v46\n"
    "v_mfma_f32_16x16x32_bf16 v[76:79], a[80:83], a[32:35], v[252:255]\n"
    "v_exp_f32 v47, v47\n"
    "v_mfma_f32_16x16x32_bf16 v[52:55], a[80:83], a[48:51], v[240:243]\n"
    "v_add_f32 v7, v7, v47\n"
    "v_cvt_pk_bf16_f32 v43, v46, v47\n"
    "v_mfma_f32_16x16x32_bf16 v[60:63], a[80:83], a[64:67], v[244:247]\n"
    "v_exp_f32 v20, v20\n"
    "v_mfma_f32_16x16x32_bf16 v[68:71], a[80:83], a[0:3], v[248:251]\n"
    "ds_read_b128 a[80:83], v2 offset:1024\n"
    "v_add_f32 v8, v8, v20\n"
    "v_mfma_f32_16x16x32_bf16 v[76:79], a[84:87], a[36:39], v[76:79]\n"
    "v_exp_f32 v21, v21\n"
    "v_mfma_f32_16x16x32_bf16 v[52:55], a[84:87], a[52:55], v[52:55]\n"
    "v_add_f32 v8, v8, v21\n"
    "v_cvt_pk_bf16_f32 v18, v20, v21\n"
    "v_mfma_f32_16x16x32_bf16 v[60:63], a[84:87], a[68:71], v[60:63]\n"
    "v_exp_f32 v22, v22\n"
    "v_mfma_f32_16x16x32_bf16 v[68:71], a[84:87], a[4:7], v[68:71]\n"
    "s_waitcnt lgkmcnt(4)\n"
    "ds_read_b128 a[84:87], v2 offset:1536\n"
    "v_add_f32 v8, v8, v22\n"
    "v_mfma_f32_16x16x32_bf16 v[76:79], a[88:91], a[40:43], v[76:79]\n"
    "v_exp_f32 v23, v23\n"
    "v_mfma_f32_16x16x32_bf16 v[52:55], a[88:91], a[56:59], v[52:55]\n"
    "v_add_f32 v8, v8, v23\n"
    "v_cvt_pk_bf16_f32 v19, v22, v23\n"
    "v_mfma_f32_16x16x32_bf16 v[60:63], a[88:91], a[72:75], v[60:63]\n"
    "v_exp_f32 v28, v28\n"
    "v_mfma_f32_16x16x32_bf16 v[68:71], a[88:91], a[8:11], v[68:71]\n"
    "s_waitcnt lgkmcnt(4)\n"
    "ds_read_b128 a[88:91], v2 offset:4096\n"
    "v_add_f32 v9, v9, v28\n"
    "v_mfma_f32_16x16x32_bf16 v[76:79], a[92:95], a[44:47], v[76:79]\n"
    "v_exp_f32 v29, v29\n"
    "v_mfma_f32_16x16x32_bf16 v[52:55], a[92:95], a[60:63], v[52:55]\n"
    "v_add_f32 v9, v9, v29\n"
    "v_cvt_pk_bf16_f32 v26, v28, v29\n"
    "v_mfma_f32_16x16x32_bf16 v[60:63], a[92:95], a[76:79], v[60:63]\n"
    "v_exp_f32 v30, v30\n"
    "v_mfma_f32_16x16x32_bf16 v[68:71], a[92:95], a[12:15], v[68:71]\n"
    "ds_read_b128 a[92:95], v2 offset:4608\n"
    "v_add_f32 v9, v9, v30\n"
    "v_mfma_f32_16x16x32_bf16 v[144:147], a[104:107], v[40:43], v[144:147]\n"
    "v_exp_f32 v31, v31\n"
    "v_mfma_f32_16x16x32_bf16 v[176:179], a[104:107], v[16:19], v[176:179]\n"
    "v_add_f32 v9, v9, v31\n"
    "v_cvt_pk_bf16_f32 v27, v30, v31\n"
    "v_exp_f32 v36, v36\n"
    "s_nop 0\n"
    "v_mfma_f32_16x16x32_bf16 v[208:211], a[104:107], v[24:27], v[208:211]\n"
    "v_add_f32 v5, v5, v36\n"
    "v_exp_f32 v37, v37\n"
    "v_exp_f32 v38, v38\n"
    "v_add_f32 v5, v5, v37\n"
    "v_cvt_pk_bf16_f32 v34, v36, v37\n"
    "v_add_f32 v5, v5, v38\n"
    "v_exp_f32 v39, v39\n"
    "v_rcp_f32 v12, v12\n"
    "v_add_f32 v5, v5, v39\n"
    "v_cvt_pk_bf16_f32 v35, v38, v39\n"
    "v_mul_f32 v112, v112, v12\n"
    "v_mul_f32 v113, v113, v12\n"
    "v_mfma_f32_16x16x32_bf16 v[80:83], a[104:107], v[32:35], 0\n"
    "ds_read_b64_tr_b16 a[104:105], v3 offset:17920\n"
    "ds_read_b64_tr_b16 a[106:107], v3 offset:22016\n"
    "v_mfma_f32_16x16x32_bf16 v[148:151], a[108:111], v[40:43], v[148:151]\n"
    "v_mul_f32 v114, v114, v12\n"
    "v_mul_f32 v115, v115, v12\n"
    "v_mfma_f32_16x16x32_bf16 v[180:183], a[108:111], v[16:19], v[180:183]\n"
    "v_mul_f32 v116, v116, v12\n"
    "v_mul_f32 v117, v117, v12\n"
    "v_mfma_f32_16x16x32_bf16 v[212:215], a[108:111], v[24:27], v[212:215]\n"
    "v_mul_f32 v118, v118, v12\n"
    "v_mul_f32 v119, v119, v12\n"
    "v_mfma_f32_16x16x32_bf16 v[84:87], a[108:111], v[32:35], 0\n"
    "ds_read_b64_tr_b16 a[108:109], v3 offset:17928\n"
    "ds_read_b64_tr_b16 a[110:111], v3 offset:22024\n"
    "v_mfma_f32_16x16x32_bf16 v[152:155], a[112:115], v[40:43], v[152:155]\n"
    "v_cvt_pk_bf16_f32 v112, v112, v113\n"
    "v_cvt_pk_bf16_f32 v113, v114, v115\n"
    "v_mfma_f32_16x16x32_bf16 v[184:187], a[112:115], v[16:19], v[184:187]\n"
    "v_cvt_pk_bf16_f32 v114, v116, v117\n"
    "v_cvt_pk_bf16_f32 v115, v118, v119\n"
    "v_mfma_f32_16x16x32_bf16 v[216:219], a[112:115], v[24:27], v[216:219]\n"
    "buffer_store_dwordx4 v[112:115], v4, s[52:55], s62 offen offset:0 sc0 sc1\n"
    "v_mul_f32 v120, v120, v12\n"
    "v_mfma_f32_16x16x32_bf16 v[88:91], a[112:115], v[32:35], 0\n"
    "ds_read_b64_tr_b16 a[112:113], v3 offset:24576\n"
    "ds_read_b64_tr_b16 a[114:115], v3 offset:28672\n"
    "v_mfma_f32_16x16x32_bf16 v[156:159], a[116:119], v[40:43], v[156:159]\n"
    "v_mul_f32 v121, v121, v12\n"
    "v_mul_f32 v122, v122, v12\n"
    "v_mfma_f32_16x16x32_bf16 v[188:191], a[116:119], v[16:19], v[188:191]\n"
    "v_mul_f32 v123, v123, v12\n"
    "v_mul_f32 v124, v124, v12\n"
    "v_mfma_f32_16x16x32_bf16 v[220:223], a[116:119], v[24:27], v[220:223]\n"
    "v_mul_f32 v125, v125, v12\n"
    "v_mul_f32 v126, v126, v12\n"
    "v_mfma_f32_16x16x32_bf16 v[92:95], a[116:119], v[32:35], 0\n"
    "ds_read_b64_tr_b16 a[116:117], v3 offset:24584\n"
    "ds_read_b64_tr_b16 a[118:119], v3 offset:28680\n"
    "v_mfma_f32_16x16x32_bf16 v[160:163], a[120:123], v[40:43], v[160:163]\n"
    "v_mul_f32 v127, v127, v12\n"
    "v_cvt_pk_bf16_f32 v120, v120, v121\n"
    "v_mfma_f32_16x16x32_bf16 v[192:195], a[120:123], v[16:19], v[192:195]\n"
    "v_cvt_pk_bf16_f32 v121, v122, v123\n"
    "v_cvt_pk_bf16_f32 v122, v124, v125\n"
    "v_mfma_f32_16x16x32_bf16 v[224:227], a[120:123], v[24:27], v[224:227]\n"
    "v_cvt_pk_bf16_f32 v123, v126, v127\n"
    "v_mul_f32 v128, v128, v12\n"
    "v_mfma_f32_16x16x32_bf16 v[96:99], a[120:123], v[32:35], 0\n"
    "v_exp_f32 v72, v72\n"
    "v_mfma_f32_16x16x32_bf16 v[164:167], a[124:127], v[40:43], v[164:167]\n"
    "buffer_store_dwordx4 v[120:123], v4, s[52:55], s62 offen offset:64 sc0 sc1\n"
    "ds_read_b64_tr_b16 a[120:121], v3 offset:25088\n"
    "v_mfma_f32_16x16x32_bf16 v[196:199], a[124:127], v[16:19], v[196:199]\n"
    "ds_read_b64_tr_b16 a[122:123], v3 offset:29184\n"
    "v_add_f32 v7, v7, v72\n"
    "v_mfma_f32_16x16x32_bf16 v[228:231], a[124:127], v[24:27], v[228:231]\n"
    "v_exp_f32 v73, v73\n"
    "v_mfma_f32_16x16x32_bf16 v[100:103], a[124:127], v[32:35], 0\n"
    "s_waitcnt lgkmcnt(8)\n"
    "ds_read_b64_tr_b16 a[124:125], v3 offset:25096\n"
    "ds_read_b64_tr_b16 a[126:127], v3 offset:29192\n"
    "v_mfma_f32_16x16x32_bf16 v[168:171], a[104:107], v[40:43], v[168:171]\n"
    "v_add_f32 v7, v7, v73\n"
    "v_cvt_pk_bf16_f32 v72, v72, v73\n"
    "v_mfma_f32_16x16x32_bf16 v[200:203], a[104:107], v[16:19], v[200:203]\n"
    "v_exp_f32 v74, v74\n"
    "v_mfma_f32_16x16x32_bf16 v[232:235], a[104:107], v[24:27], v[232:235]\n"
    "v_add_f32 v7, v7, v74\n"
    "v_mul_f32 v129, v129, v12\n"
    "v_mfma_f32_16x16x32_bf16 v[104:107], a[104:107], v[32:35], 0\n"
    "s_waitcnt lgkmcnt(8)\n"
    "v_exp_f32 v75, v75\n"
    "v_mfma_f32_16x16x32_bf16 v[172:175], a[108:111], v[40:43], v[172:175]\n"
    "ds_read_b64_tr_b16 a[104:105], v3 offset:25600\n"
    "ds_read_b64_tr_b16 a[106:107], v3 offset:29696\n"
    "v_mfma_f32_16x16x32_bf16 v[204:207], a[108:111], v[16:19], v[204:207]\n"
    "v_add_f32 v7, v7, v75\n"
    "v_cvt_pk_bf16_f32 v73, v74, v75\n"
    "v_mfma_f32_16x16x32_bf16 v[236:239], a[108:111], v[24:27], v[236:239]\n"
    "v_mul_f32 v130, v130, v12\n"
    "v_mul_f32 v131, v131, v12\n"
    "v_mfma_f32_16x16x32_bf16 v[108:111], a[108:111], v[32:35], 0\n"
    "ds_read_b64_tr_b16 a[108:109], v3 offset:25608\n"
    "ds_read_b64_tr_b16 a[110:111], v3 offset:29704\n"
    "v_mfma_f32_16x16x32_bf16 v[40:43], a[96:99], a[32:35], v[252:255]\n"
    "v_mul_f32 v132, v132, v12\n"
    "v_mul_f32 v133, v133, v12\n"
    "v_mfma_f32_16x16x32_bf16 v[16:19], a[96:99], a[48:51], v[240:243]\n"
    "v_mul_f32 v134, v134, v12\n"
    "v_mul_f32 v135, v135, v12\n"
    "v_mfma_f32_16x16x32_bf16 v[24:27], a[96:99], a[64:67], v[244:247]\n"
    "v_cvt_pk_bf16_f32 v128, v128, v129\n"
    "v_cvt_pk_bf16_f32 v129, v130, v131\n"
    "v_mfma_f32_16x16x32_bf16 v[32:35], a[96:99], a[0:3], v[248:251]\n"
    "ds_read_b128 a[96:99], v2 offset:5120\n"
    "v_cvt_pk_bf16_f32 v130, v132, v133\n"
    "v_mfma_f32_16x16x32_bf16 v[40:43], a[100:103], a[36:39], v[40:43]\n"
    "v_cvt_pk_bf16_f32 v131, v134, v135\n"
    "v_mul_f32 v136, v136, v12\n"
    "v_mfma_f32_16x16x32_bf16 v[16:19], a[100:103], a[52:55], v[16:19]\n"
    "buffer_store_dwordx4 v[128:131], v4, s[52:55], s62 offen offset:128 sc0 sc1\n"
    "v_mul_f32 v137, v137, v12\n"
    "v_mfma_f32_16x16x32_bf16 v[24:27], a[100:103], a[68:71], v[24:27]\n"
    "v_mul_f32 v138, v138, v12\n"
    "v_mul_f32 v139, v139, v12\n"
    "v_mfma_f32_16x16x32_bf16 v[32:35], a[100:103], a[4:7], v[32:35]\n"
    "ds_read_b128 a[100:103], v2 offset:5632\n"
    "v_mul_f32 v140, v140, v12\n"
    "v_mfma_f32_16x16x32_bf16 v[40:43], a[80:83], a[40:43], v[40:43]\n"
    "v_mul_f32 v141, v141, v12\n"
    "v_mul_f32 v142, v142, v12\n"
    "v_mfma_f32_16x16x32_bf16 v[16:19], a[80:83], a[56:59], v[16:19]\n"
    "v_mul_f32 v143, v143, v12\n"
    "v_cvt_pk_bf16_f32 v136, v136, v137\n"
    "v_mfma_f32_16x16x32_bf16 v[24:27], a[80:83], a[72:75], v[24:27]\n"
    "v_cvt_pk_bf16_f32 v137, v138, v139\n"
    "v_cvt_pk_bf16_f32 v138, v140, v141\n"
    "v_mfma_f32_16x16x32_bf16 v[32:35], a[80:83], a[8:11], v[32:35]\n"
    "ds_read_b128 a[80:83], v2 offset:8192\n"
    "v_cvt_pk_bf16_f32 v139, v142, v143\n"
    "v_mfma_f32_16x16x32_bf16 v[40:43], a[84:87], a[44:47], v[40:43]\n"
    "buffer_store_dwordx4 v[136:139], v4, s[52:55], s62 offen offset:192 sc0 sc1\n"
    "v_mfma_f32_16x16x32_bf16 v[16:19], a[84:87], a[60:63], v[16:19]\n"
    "v_exp_f32 v48, v48\n"
    "v_mfma_f32_16x16x32_bf16 v[24:27], a[84:87], a[76:79], v[24:27]\n"
    "v_add_f32 v8, v8, v48\n"
    "v_mfma_f32_16x16x32_bf16 v[32:35], a[84:87], a[12:15], v[32:35]\n"
    "v_exp_f32 v49, v49\n"
    "v_mfma_f32_16x16x32_bf16 v[44:47], a[88:91], a[32:35], v[252:255]\n"
    "ds_read_b128 a[84:87], v2 offset:8704\n"
    "v_add_f32 v8, v8, v49\n"
    "v_mfma_f32_16x16x32_bf16 v[20:23], a[88:91], a[48:51], v[240:243]\n"
    "v_cvt_pk_bf16_f32 v48, v48, v49\n"
    "v_mfma_f32_16x16x32_bf16 v[28:31], a[88:91], a[64:67], v[244:247]\n"
    "v_exp_f32 v50, v50\n"
    "v_mfma_f32_16x16x32_bf16 v[36:39], a[88:91], a[0:3], v[248:251]\n"
    "ds_read_b128 a[88:91], v2 offset:9216\n"
    "v_add_f32 v8, v8, v50\n"
    "v_mfma_f32_16x16x32_bf16 v[44:47], a[92:95], a[36:39], v[44:47]\n"
    "v_exp_f32 v51, v51\n"
    "v_mfma_f32_16x16x32_bf16 v[20:23], a[92:95], a[52:55], v[20:23]\n"
    "v_add_f32 v8, v8, v51\n"
    "v_cvt_pk_bf16_f32 v49, v50, v51\n"
    "v_mfma_f32_16x16x32_bf16 v[28:31], a[92:95], a[68:71], v[28:31]\n"
    "v_exp_f32 v56, v56\n"
    "v_mfma_f32_16x16x32_bf16 v[36:39], a[92:95], a[4:7], v[36:39]\n"
    "s_waitcnt lgkmcnt(4)\n"
    "ds_read_b128 a[92:95], v2 offset:9728\n"
    "v_add_f32 v9, v9, v56\n"
    "v_mfma_f32_16x16x32_bf16 v[44:47], a[96:99], a[40:43], v[44:47]\n"
    "v_exp_f32 v57, v57\n"
    "v_mfma_f32_16x16x32_bf16 v[20:23], a[96:99], a[56:59], v[20:23]\n"
    "v_add_f32 v9, v9, v57\n"
    "v_cvt_pk_bf16_f32 v56, v56, v57\n"
    "v_mfma_f32_16x16x32_bf16 v[28:31], a[96:99], a[72:75], v[28:31]\n"
    "v_exp_f32 v58, v58\n"
    "v_mfma_f32_16x16x32_bf16 v[36:39], a[96:99], a[8:11], v[36:39]\n"
    "s_waitcnt lgkmcnt(4)\n"
    "ds_read_b128 a[96:99], v2 offset:12288\n"
    "v_add_f32 v9, v9, v58\n"
    "v_mfma_f32_16x16x32_bf16 v[44:47], a[100:103], a[44:47], v[44:47]\n"
    "v_exp_f32 v59, v59\n"
    "v_mfma_f32_16x16x32_bf16 v[20:23], a[100:103], a[60:63], v[20:23]\n"
    "v_add_f32 v9, v9, v59\n"
    "v_cvt_pk_bf16_f32 v57, v58, v59\n"
    "v_mfma_f32_16x16x32_bf16 v[28:31], a[100:103], a[76:79], v[28:31]\n"
    "v_exp_f32 v64, v64\n"
    "v_mfma_f32_16x16x32_bf16 v[36:39], a[100:103], a[12:15], v[36:39]\n"
    "ds_read_b128 a[100:103], v2 offset:12800\n"
    "v_add_f32 v5, v5, v64\n"
    "v_exp_f32 v65, v65\n"
    "v_exp_f32 v66, v66\n"
    "v_add_f32 v5, v5, v65\n"
    "v_cvt_pk_bf16_f32 v64, v64, v65\n"
    "v_add_f32 v5, v5, v66\n"
    "v_exp_f32 v67, v67\n"
    "v_exp_f32 v76, v76\n"
    "v_add_f32 v5, v5, v67\n"
    "v_cvt_pk_bf16_f32 v65, v66, v67\n"
    "v_add_f32 v7, v7, v76\n"
    "v_exp_f32 v77, v77\n"
    "v_exp_f32 v78, v78\n"
    "v_add_f32 v7, v7, v77\n"
    "v_cvt_pk_bf16_f32 v74, v76, v77\n"
    "v_add_f32 v7, v7, v78\n"
    "v_exp_f32 v79, v79\n"
    "v_exp_f32 v52, v52\n"
    "v_add_f32 v7, v7, v79\n"
    "v_cvt_pk_bf16_f32 v75, v78, v79\n"
    "v_add_f32 v8, v8, v52\n"
    "s_nop 0\n"
    "v_mfma_f32_16x16x32_bf16 v[144:147], a[112:115], v[72:75], v[144:147]\n"
    "v_exp_f32 v53, v53\n"
    "v_exp_f32 v54, v54\n"
    "v_add_f32 v8, v8, v53\n"
    "v_cvt_pk_bf16_f32 v50, v52, v53\n"
    "v_add_f32 v8, v8, v54\n"
    "v_exp_f32 v55, v55\n"
    "v_exp_f32 v60, v60\n"
    "v_add_f32 v8, v8, v55\n"
    "v_cvt_pk_bf16_f32 v51, v54, v55\n"
    "v_add_f32 v9, v9, v60\n"
    "s_nop 0\n"
    "v_mfma_f32_16x16x32_bf16 v[176:179], a[112:115], v[48:51], v[176:179]\n"
    "v_exp_f32 v61, v61\n"
    "v_exp_f32 v62, v62\n"
    "v_add_f32 v9, v9, v61\n"
    "v_cvt_pk_bf16_f32 v58, v60, v61\n"
    "v_add_f32 v9, v9, v62\n"
    "v_exp_f32 v63, v63\n"
    "v_exp_f32 v68, v68\n"
    "v_add_f32 v9, v9, v63\n"
    "v_cvt_pk_bf16_f32 v59, v62, v63\n"
    "v_add_f32 v5, v5, v68\n"
    "s_nop 0\n"
    "v_mfma_f32_16x16x32_bf16 v[208:211], a[112:115], v[56:59], v[208:211]\n"
    "v_exp_f32 v69, v69\n"
    "v_exp_f32 v70, v70\n"
    "v_add_f32 v5, v5, v69\n"
    "v_cvt_pk_bf16_f32 v66, v68, v69\n"
    "v_add_f32 v5, v5, v70\n"
    "v_exp_f32 v71, v71\n"
    "v_exp_f32 v40, v40\n"
    "v_add_f32 v5, v5, v71\n"
    "v_cvt_pk_bf16_f32 v67, v70, v71\n"
    "v_add_f32 v7, v7, v40\n"
    "s_nop 0\n"
    "v_mfma_f32_16x16x32_bf16 v[80:83], a[112:115], v[64:67], v[80:83]\n"
    "v_exp_f32 v41, v41\n"
    "v_mfma_f32_16x16x32_bf16 v[148:151], a[116:119], v[72:75], v[148:151]\n"
    "ds_read_b64_tr_b16 a[112:113], v3 offset:26112\n"
    "ds_read_b64_tr_b16 a[114:115], v3 offset:30208\n"
    "v_mfma_f32_16x16x32_bf16 v[180:183], a[116:119], v[48:51], v[180:183]\n"
    "v_add_f32 v7, v7, v41\n"
    "v_cvt_pk_bf16_f32 v40, v40, v41\n"
    "v_mfma_f32_16x16x32_bf16 v[212:215], a[116:119], v[56:59], v[212:215]\n"
    "v_exp_f32 v42, v42\n"
    "v_mfma_f32_16x16x32_bf16 v[84:87], a[116:119], v[64:67], v[84:87]\n"
    "ds_read_b64_tr_b16 a[116:117], v3 offset:26120\n"
    "ds_read_b64_tr_b16 a[118:119], v3 offset:30216\n"
    "v_mfma_f32_16x16x32_bf16 v[152:155], a[120:123], v[72:75], v[152:155]\n"
    "v_add_f32 v7, v7, v42\n"
    "v_mfma_f32_16x16x32_bf16 v[184:187], a[120:123], v[48:51], v[184:187]\n"
    "v_exp_f32 v43, v43\n"
    "v_mfma_f32_16x16x32_bf16 v[216:219], a[120:123], v[56:59], v[216:219]\n"
    "v_add_f32 v7, v7, v43\n"
    "v_cvt_pk_bf16_f32 v41, v42, v43\n"
    "v_mfma_f32_16x16x32_bf16 v[88:91], a[120:123], v[64:67], v[88:91]\n"
    "v_exp_f32 v16, v16\n"
    "v_mfma_f32_16x16x32_bf16 v[156:159], a[124:127], v[72:75], v[156:159]\n"
    "ds_read_b64_tr_b16 a[120:121], v3 offset:32768\n"
    "ds_read_b64_tr_b16 a[122:123], v3 offset:36864\n"
    "v_mfma_f32_16x16x32_bf16 v[188:191], a[124:127], v[48:51], v[188:191]\n"
    "v_add_f32 v8, v8, v16\n"
    "v_mfma_f32_16x16x32_bf16 v[220:223], a[124:127], v[56:59], v[220:223]\n"
    "v_exp_f32 v17, v17\n"
    "v_mfma_f32_16x16x32_bf16 v[92:95], a[124:127], v[64:67], v[92:95]\n"
    "ds_read_b64_tr_b16 a[124:125], v3 offset:32776\n"
    "ds_read_b64_tr_b16 a[126:127], v3 offset:36872\n"
    "v_mfma_f32_16x16x32_bf16 v[160:163], a[104:107], v[72:75], v[160:163]\n"
    "v_add_f32 v8, v8, v17\n"
    "v_cvt_pk_bf16_f32 v16, v16, v17\n"
    "v_mfma_f32_16x16x32_bf16 v[192:195], a[104:107], v[48:51], v[192:195]\n"
    "v_exp_f32 v18, v18\n"
    "v_mfma_f32_16x16x32_bf16 v[224:227], a[104:107], v[56:59], v[224:227]\n"
    "v_add_f32 v8, v8, v18\n"
    "v_mfma_f32_16x16x32_bf16 v[96:99], a[104:107], v[64:67], v[96:99]\n"
    "v_exp_f32 v19, v19\n"
    "v_mfma_f32_16x16x32_bf16 v[164:167], a[108:111], v[72:75], v[164:167]\n"
    "ds_read_b64_tr_b16 a[104:105], v3 offset:33280\n"
    "ds_read_b64_tr_b16 a[106:107], v3 offset:37376\n"
    "v_mfma_f32_16x16x32_bf16 v[196:199], a[108:111], v[48:51], v[196:199]\n"
    "v_add_f32 v8, v8, v19\n"
    "v_cvt_pk_bf16_f32 v17, v18, v19\n"
    "v_mfma_f32_16x16x32_bf16 v[228:231], a[108:111], v[56:59], v[228:231]\n"
    "v_exp_f32 v24, v24\n"
    "v_mfma_f32_16x16x32_bf16 v[100:103], a[108:111], v[64:67], v[100:103]\n"
    "s_waitcnt lgkmcnt(8)\n"
    "ds_read_b64_tr_b16 a[108:109], v3 offset:33288\n"
    "ds_read_b64_tr_b16 a[110:111], v3 offset:37384\n"
    "v_mfma_f32_16x16x32_bf16 v[168:171], a[112:115], v[72:75], v[168:171]\n"
    "v_add_f32 v9, v9, v24\n"
    "v_mfma_f32_16x16x32_bf16 v[200:203], a[112:115], v[48:51], v[200:203]\n"
    "v_exp_f32 v25, v25\n"
    "v_mfma_f32_16x16x32_bf16 v[232:235], a[112:115], v[56:59], v[232:235]\n"
    "v_add_f32 v9, v9, v25\n"
    "v_cvt_pk_bf16_f32 v24, v24, v25\n"
    "v_mfma_f32_16x16x32_bf16 v[104:107], a[112:115], v[64:67], v[104:107]\n"
    "s_waitcnt lgkmcnt(8)\n"
    "v_exp_f32 v26, v26\n"
    "v_mfma_f32_16x16x32_bf16 v[172:175], a[116:119], v[72:75], v[172:175]\n"
    "ds_read_b64_tr_b16 a[112:113], v3 offset:33792\n"
    "ds_read_b64_tr_b16 a[114:115], v3 offset:37888\n"
    "v_mfma_f32_16x16x32_bf16 v[204:207], a[116:119], v[48:51], v[204:207]\n"
    "v_add_f32 v9, v9, v26\n"
    "v_mfma_f32_16x16x32_bf16 v[236:239], a[116:119], v[56:59], v[236:239]\n"
    "v_exp_f32 v27, v27\n"
    "v_mfma_f32_16x16x32_bf16 v[108:111], a[116:119], v[64:67], v[108:111]\n"
    "s_mov_b32 s77, 45056\n"
    "ds_read_b64_tr_b16 a[116:117], v3 offset:33800\n"
    "ds_read_b64_tr_b16 a[118:119], v3 offset:37896\n"
    "v_mfma_f32_16x16x32_bf16 v[48:51], a[80:83], a[48:51], v[240:243]\n"
    "buffer_load_dword v15, v14, s[72:75], s77 offen\n"
    "v_add_f32 v9, v9, v27\n"
    "v_mfma_f32_16x16x32_bf16 v[56:59], a[80:83], a[64:67], v[244:247]\n"
    "v_cvt_pk_bf16_f32 v25, v26, v27\n"
    "v_mfma_f32_16x16x32_bf16 v[64:67], a[80:83], a[0:3], v[248:251]\n"
    "s_waitcnt vmcnt(13)\n"
    "v_exp_f32 v32, v32\n"
    "v_mfma_f32_16x16x32_bf16 v[72:75], a[80:83], a[16:19], 0\n"
    "ds_read_b128 a[80:83], v2 offset:13312\n"
    "v_add_f32 v5, v5, v32\n"
    "v_mfma_f32_16x16x32_bf16 v[48:51], a[84:87], a[52:55], v[48:51]\n"
    "v_exp_f32 v33, v33\n"
    "v_mfma_f32_16x16x32_bf16 v[56:59], a[84:87], a[68:71], v[56:59]\n"
    "v_add_f32 v5, v5, v33\n"
    "v_cvt_pk_bf16_f32 v32, v32, v33\n"
    "v_mfma_f32_16x16x32_bf16 v[64:67], a[84:87], a[4:7], v[64:67]\n"
    "s_waitcnt vmcnt(12)\n"
    "v_exp_f32 v34, v34\n"
    "v_mfma_f32_16x16x32_bf16 v[72:75], a[84:87], a[20:23], v[72:75]\n"
    "ds_read_b128 a[84:87], v2 offset:13824\n"
    "v_add_f32 v5, v5, v34\n"
    "v_mfma_f32_16x16x32_bf16 v[48:51], a[88:91], a[56:59], v[48:51]\n"
    "v_exp_f32 v35, v35\n"
    "v_mfma_f32_16x16x32_bf16 v[56:59], a[88:91], a[72:75], v[56:59]\n"
    "v_add_f32 v5, v5, v35\n"
    "v_cvt_pk_bf16_f32 v33, v34, v35\n"
    "v_mfma_f32_16x16x32_bf16 v[64:67], a[88:91], a[8:11], v[64:67]\n"
    "s_waitcnt vmcnt(11)\n"
    "v_exp_f32 v44, v44\n"
    "v_mfma_f32_16x16x32_bf16 v[72:75], a[88:91], a[24:27], v[72:75]\n"
    "ds_read_b128 a[88:91], v2 offset:16384\n"
    "v_add_f32 v7, v7, v44\n"
    "v_mfma_f32_16x16x32_bf16 v[48:51], a[92:95], a[60:63], v[48:51]\n"
    "v_exp_f32 v45, v45\n"
    "v_mfma_f32_16x16x32_bf16 v[56:59], a[92:95], a[76:79], v[56:59]\n"
    "v_add_f32 v7, v7, v45\n"
    "v_cvt_pk_bf16_f32 v42, v44, v45\n"
    "v_mfma_f32_16x16x32_bf16 v[64:67], a[92:95], a[12:15], v[64:67]\n"
    "s_waitcnt vmcnt(10)\n"
    "v_exp_f32 v46, v46\n"
    "v_mfma_f32_16x16x32_bf16 v[72:75], a[92:95], a[28:31], v[72:75]\n"
    "ds_read_b128 a[92:95], v2 offset:16896\n"
    "v_add_f32 v7, v7, v46\n"
    "v_mfma_f32_16x16x32_bf16 v[52:55], a[96:99], a[48:51], v[240:243]\n"
    "v_exp_f32 v47, v47\n"
    "v_mfma_f32_16x16x32_bf16 v[60:63], a[96:99], a[64:67], v[244:247]\n"
    "v_add_f32 v7, v7, v47\n"
    "v_cvt_pk_bf16_f32 v43, v46, v47\n"
    "v_mfma_f32_16x16x32_bf16 v[68:71], a[96:99], a[0:3], v[248:251]\n"
    "v_exp_f32 v20, v20\n"
    "v_mfma_f32_16x16x32_bf16 v[76:79], a[96:99], a[16:19], 0\n"
    "ds_read_b128 a[96:99], v2 offset:17408\n"
    "v_add_f32 v8, v8, v20\n"
    "v_mfma_f32_16x16x32_bf16 v[52:55], a[100:103], a[52:55], v[52:55]\n"
    "v_exp_f32 v21, v21\n"
    "v_mfma_f32_16x16x32_bf16 v[60:63], a[100:103], a[68:71], v[60:63]\n"
    "v_add_f32 v8, v8, v21\n"
    "v_cvt_pk_bf16_f32 v18, v20, v21\n"
    "v_mfma_f32_16x16x32_bf16 v[68:71], a[100:103], a[4:7], v[68:71]\n"
    "v_exp_f32 v22, v22\n"
    "v_mfma_f32_16x16x32_bf16 v[76:79], a[100:103], a[20:23], v[76:79]\n"
    "s_waitcnt lgkmcnt(4)\n"
    "ds_read_b128 a[100:103], v2 offset:17920\n"
    "v_add_f32 v8, v8, v22\n"
    "v_mfma_f32_16x16x32_bf16 v[52:55], a[80:83], a[56:59], v[52:55]\n"
    "v_exp_f32 v23, v23\n"
    "v_mfma_f32_16x16x32_bf16 v[60:63], a[80:83], a[72:75], v[60:63]\n"
    "v_add_f32 v8, v8, v23\n"
    "v_cvt_pk_bf16_f32 v19, v22, v23\n"
    "v_mfma_f32_16x16x32_bf16 v[68:71], a[80:83], a[8:11], v[68:71]\n"
    "v_exp_f32 v28, v28\n"
    "v_mfma_f32_16x16x32_bf16 v[76:79], a[80:83], a[24:27], v[76:79]\n"
    "s_waitcnt lgkmcnt(4)\n"
    "ds_read_b128 a[80:83], v2 offset:20480\n"
    "v_add_f32 v9, v9, v28\n"
    "v_mfma_f32_16x16x32_bf16 v[52:55], a[84:87], a[60:63], v[52:55]\n"
    "v_exp_f32 v29, v29\n"
    "v_mfma_f32_16x16x32_bf16 v[60:63], a[84:87], a[76:79], v[60:63]\n"
    "v_add_f32 v9, v9, v29\n"
    "v_cvt_pk_bf16_f32 v26, v28, v29\n"
    "v_mfma_f32_16x16x32_bf16 v[68:71], a[84:87], a[12:15], v[68:71]\n"
    "v_exp_f32 v30, v30\n"
    "v_mfma_f32_16x16x32_bf16 v[76:79], a[84:87], a[28:31], v[76:79]\n"
    "ds_read_b128 a[84:87], v2 offset:20992\n"
    "v_add_f32 v9, v9, v30\n"
    "v_mfma_f32_16x16x32_bf16 v[144:147], a[120:123], v[40:43], v[144:147]\n"
    "v_exp_f32 v31, v31\n"
    "v_mfma_f32_16x16x32_bf16 v[176:179], a[120:123], v[16:19], v[176:179]\n"
    "v_add_f32 v9, v9, v31\n"
    "v_cvt_pk_bf16_f32 v27, v30, v31\n"
    "v_max3_f32 v10, v72, v73, v74\n"
    "v_mov_b32 v12, v7\n"
    "v_mfma_f32_16x16x32_bf16 v[208:211], a[120:123], v[24:27], v[208:211]\n"
    "s_mov_b32 s63, 114688\n"
    "v_max3_f32 v11, v76, v77, v78\n"
    "v_max3_f32 v10, v10, v75, v79\n"
    "v_max_f32 v10, v10, v11\n"
    "v_mov_b32 v11, v10\n"
    "v_exp_f32 v36, v36\n"
    "s_nop 0\n"
    "v_permlane32_swap_b32 v10, v11\n"
    "v_add_f32 v5, v5, v36\n"
    "v_max_f32 v10, v10, v11\n"
    "v_mov_b32 v11, v10\n"
    "v_exp_f32 v37, v37\n"
    "s_nop 0\n"
    "v_permlane16_swap_b32 v10, v11\n"
    "v_add_f32 v5, v5, v37\n"
    "v_min_f32 v252, -v10, -v11\n"
    "v_min_f32 v253, -v10, -v11\n"
    "v_min_f32 v254, -v10, -v11\n"
    "v_min_f32 v255, -v10, -v11\n"
    "v_add_f32 v72, v72, v252\n"
    "v_add_f32 v73, v73, v252\n"
    "v_add_f32 v74, v74, v252\n"
    "v_add_f32 v75, v75, v252\n"
    "v_add_f32 v76, v76, v252\n"
    "v_add_f32 v77, v77, v252\n"
    "v_add_f32 v78, v78, v252\n"
    "v_add_f32 v79, v79, v252\n"
    "v_cvt_pk_bf16_f32 v34, v36, v37\n"
    "v_exp_f32 v38, v38\n"
    "v_exp_f32 v39, v39\n"
    "v_add_f32 v5, v5, v38\n"
    "v_add_f32 v5, v5, v39\n"
    "v_cvt_pk_bf16_f32 v35, v38, v39\n"
    "v_exp_f32 v48, v48\n"
    "s_nop 0\n"
    "v_mfma_f32_16x16x32_bf16 v[80:83], a[120:123], v[32:35], v[80:83]\n"
    "ds_read_b64_tr_b16 a[120:121], v3 offset:34304\n"
    "ds_read_b64_tr_b16 a[122:123], v3 offset:38400\n"
    "v_mfma_f32_16x16x32_bf16 v[148:151], a[124:127], v[40:43], v[148:151]\n"
    "v_add_f32 v8, v8, v48\n"
    "v_permlane32_swap_b32 v7, v12\n"
    "v_mfma_f32_16x16x32_bf16 v[180:183], a[124:127], v[16:19], v[180:183]\n"
    "v_exp_f32 v49, v49\n"
    "v_mfma_f32_16x16x32_bf16 v[212:215], a[124:127], v[24:27], v[212:215]\n"
    "v_add_f32 v8, v8, v49\n"
    "v_cvt_pk_bf16_f32 v48, v48, v49\n"
    "v_mfma_f32_16x16x32_bf16 v[84:87], a[124:127], v[32:35], v[84:87]\n"
    "v_exp_f32 v50, v50\n"
    "v_mfma_f32_16x16x32_bf16 v[152:155], a[104:107], v[40:43], v[152:155]\n"
    "ds_read_b64_tr_b16 a[124:125], v3 offset:34312\n"
    "ds_read_b64_tr_b16 a[126:127], v3 offset:38408\n"
    "v_mfma_f32_16x16x32_bf16 v[184:187], a[104:107], v[16:19], v[184:187]\n"
    "v_add_f32 v8, v8, v50\n"
    "v_add_f32 v7, v7, v12\n"
    "v_mfma_f32_16x16x32_bf16 v[216:219], a[104:107], v[24:27], v[216:219]\n"
    "v_exp_f32 v51, v51\n"
    "v_mfma_f32_16x16x32_bf16 v[88:91], a[104:107], v[32:35], v[88:91]\n"
    "ds_read_b64_tr_b16 a[104:105], v3 offset:40960\n"
    "ds_read_b64_tr_b16 a[106:107], v3 offset:45056\n"
    "v_mfma_f32_16x16x32_bf16 v[156:159], a[108:111], v[40:43], v[156:159]\n"
    "v_add_f32 v8, v8, v51\n"
    "v_cvt_pk_bf16_f32 v49, v50, v51\n"
    "v_mfma_f32_16x16x32_bf16 v[188:191], a[108:111], v[16:19], v[188:191]\n"
    "v_exp_f32 v56, v56\n"
    "v_mfma_f32_16x16x32_bf16 v[220:223], a[108:111], v[24:27], v[220:223]\n"
    "v_add_f32 v9, v9, v56\n"
    "v_mov_b32 v12, v7\n"
    "v_mfma_f32_16x16x32_bf16 v[92:95], a[108:111], v[32:35], v[92:95]\n"
    "v_exp_f32 v57, v57\n"
    "v_mfma_f32_16x16x32_bf16 v[160:163], a[112:115], v[40:43], v[160:163]\n"
    "ds_read_b64_tr_b16 a[108:109], v3 offset:40968\n"
    "ds_read_b64_tr_b16 a[110:111], v3 offset:45064\n"
    "v_mfma_f32_16x16x32_bf16 v[192:195], a[112:115], v[16:19], v[192:195]\n"
    "v_add_f32 v9, v9, v57\n"
    "v_cvt_pk_bf16_f32 v56, v56, v57\n"
    "v_mfma_f32_16x16x32_bf16 v[224:227], a[112:115], v[24:27], v[224:227]\n"
    "v_exp_f32 v58, v58\n"
    "v_mfma_f32_16x16x32_bf16 v[96:99], a[112:115], v[32:35], v[96:99]\n"
    "ds_read_b64_tr_b16 a[112:113], v3 offset:41472\n"
    "ds_read_b64_tr_b16 a[114:115], v3 offset:45568\n"
    "v_mfma_f32_16x16x32_bf16 v[164:167], a[116:119], v[40:43], v[164:167]\n"
    "v_add_f32 v9, v9, v58\n"
    "v_permlane16_swap_b32 v7, v12\n"
    "v_mfma_f32_16x16x32_bf16 v[196:199], a[116:119], v[16:19], v[196:199]\n"
    "v_exp_f32 v59, v59\n"
    "v_mfma_f32_16x16x32_bf16 v[228:231], a[116:119], v[24:27], v[228:231]\n"
    "v_add_f32 v9, v9, v59\n"
    "v_cvt_pk_bf16_f32 v57, v58, v59\n"
    "v_mfma_f32_16x16x32_bf16 v[100:103], a[116:119], v[32:35], v[100:103]\n"
    "s_waitcnt lgkmcnt(8)\n"
    "v_exp_f32 v64, v64\n"
    "v_mfma_f32_16x16x32_bf16 v[168:171], a[120:123], v[40:43], v[168:171]\n"
    "ds_read_b64_tr_b16 a[116:117], v3 offset:41480\n"
    "ds_read_b64_tr_b16 a[118:119], v3 offset:45576\n"
    "v_mfma_f32_16x16x32_bf16 v[200:203], a[120:123], v[16:19], v[200:203]\n"
    "v_add_f32 v5, v5, v64\n"
    "v_add_f32 v12, v7, v12\n"
    "v_mfma_f32_16x16x32_bf16 v[232:235], a[120:123], v[24:27], v[232:235]\n"
    "v_exp_f32 v65, v65\n"
    "v_mfma_f32_16x16x32_bf16 v[104:107], a[120:123], v[32:35], v[104:107]\n"
    "s_waitcnt lgkmcnt(8)\n"
    "ds_read_b64_tr_b16 a[120:121], v3 offset:41984\n"
    "ds_read_b64_tr_b16 a[122:123], v3 offset:46080\n"
    "v_mfma_f32_16x16x32_bf16 v[172:175], a[124:127], v[40:43], v[172:175]\n"
    "v_add_f32 v5, v5, v65\n"
    "v_cvt_pk_bf16_f32 v64, v64, v65\n"
    "v_mfma_f32_16x16x32_bf16 v[204:207], a[124:127], v[16:19], v[204:207]\n"
    "v_exp_f32 v66, v66\n"
    "v_mfma_f32_16x16x32_bf16 v[236:239], a[124:127], v[24:27], v[236:239]\n"
    "v_add_f32 v5, v5, v66\n"
    "v_cmp_ngt_f32 vcc, 0x5d800000, v12\n"
    "v_mfma_f32_16x16x32_bf16 v[108:111], a[124:127], v[32:35], v[108:111]\n"
    "v_exp_f32 v67, v67\n"
    "v_mfma_f32_16x16x32_bf16 v[16:19], a[88:91], a[48:51], v[240:243]\n"
    "s_or_b64 s[64:65], s[64:65], vcc\n"
    "ds_read_b64_tr_b16 a[124:125], v3 offset:41992\n"
    "ds_read_b64_tr_b16 a[126:127], v3 offset:46088\n"
    "v_mfma_f32_16x16x32_bf16 v[24:27], a[88:91], a[64:67], v[244:247]\n"
    "v_add_f32 v5, v5, v67\n"
    "v_cvt_pk_bf16_f32 v65, v66, v67\n"
    "v_mfma_f32_16x16x32_bf16 v[32:35], a[88:91], a[0:3], v[248:251]\n"
    "v_exp_f32 v72, v72\n"
    "v_mfma_f32_16x16x32_bf16 v[40:43], a[88:91], a[16:19], v[252:255]\n"
    "v_exp_f32 v73, v73\n"
    "v_mfma_f32_16x16x32_bf16 v[16:19], a[92:95], a[52:55], v[16:19]\n"
    "ds_read_b128 a[88:91], v2 offset:21504\n"
    "v_add_f32 v6, v72, v73\n"
    "v_mfma_f32_16x16x32_bf16 v[24:27], a[92:95], a[68:71], v[24:27]\n"
    "v_cvt_pk_bf16_f32 v72, v72, v73\n"
    "v_mfma_f32_16x16x32_bf16 v[32:35], a[92:95], a[4:7], v[32:35]\n"
    "v_exp_f32 v74, v74\n"
    "v_mfma_f32_16x16x32_bf16 v[40:43], a[92:95], a[20:23], v[40:43]\n"
    "ds_read_b128 a[92:95], v2 offset:22016\n"
    "v_add_f32 v6, v6, v74\n"
    "v_mfma_f32_16x16x32_bf16 v[16:19], a[96:99], a[56:59], v[16:19]\n"
    "v_exp_f32 v75, v75\n"
    "v_mfma_f32_16x16x32_bf16 v[24:27], a[96:99], a[72:75], v[24:27]\n"
    "v_add_f32 v6, v6, v75\n"
    "v_cvt_pk_bf16_f32 v73, v74, v75\n"
    "v_mfma_f32_16x16x32_bf16 v[32:35], a[96:99], a[8:11], v[32:35]\n"
    "v_exp_f32 v52, v52\n"
    "v_mfma_f32_16x16x32_bf16 v[40:43], a[96:99], a[24:27], v[40:43]\n"
    "ds_read_b128 a[96:99], v2 offset:24576\n"
    "v_add_f32 v8, v8, v52\n"
    "v_mfma_f32_16x16x32_bf16 v[16:19], a[100:103], a[60:63], v[16:19]\n"
    "v_exp_f32 v53, v53\n"
    "v_mfma_f32_16x16x32_bf16 v[24:27], a[100:103], a[76:79], v[24:27]\n"
    "v_add_f32 v8, v8, v53\n"
    "v_cvt_pk_bf16_f32 v50, v52, v53\n"
    "v_mfma_f32_16x16x32_bf16 v[32:35], a[100:103], a[12:15], v[32:35]\n"
    "v_exp_f32 v54, v54\n"
    "v_mfma_f32_16x16x32_bf16 v[40:43], a[100:103], a[28:31], v[40:43]\n"
    "ds_read_b128 a[100:103], v2 offset:25088\n"
    "v_add_f32 v8, v8, v54\n"
    "v_mfma_f32_16x16x32_bf16 v[20:23], a[80:83], a[48:51], v[240:243]\n"
    "v_exp_f32 v55, v55\n"
    "v_mfma_f32_16x16x32_bf16 v[28:31], a[80:83], a[64:67], v[244:247]\n"
    "v_add_f32 v8, v8, v55\n"
    "v_cvt_pk_bf16_f32 v51, v54, v55\n"
    "v_mfma_f32_16x16x32_bf16 v[36:39], a[80:83], a[0:3], v[248:251]\n"
    "v_exp_f32 v60, v60\n"
    "v_mfma_f32_16x16x32_bf16 v[44:47], a[80:83], a[16:19], v[252:255]\n"
    "ds_read_b128 a[80:83], v2 offset:25600\n"
    "v_add_f32 v9, v9, v60\n"
    "v_mfma_f32_16x16x32_bf16 v[20:23], a[84:87], a[52:55], v[20:23]\n"
    "v_exp_f32 v61, v61\n"
    "v_mfma_f32_16x16x32_bf16 v[28:31], a[84:87], a[68:71], v[28:31]\n"
    "v_add_f32 v9, v9, v61\n"
    "v_cvt_pk_bf16_f32 v58, v60, v61\n"
    "v_mfma_f32_16x16x32_bf16 v[36:39], a[84:87], a[4:7], v[36:39]\n"
    "v_exp_f32 v62, v62\n"
    "v_mfma_f32_16x16x32_bf16 v[44:47], a[84:87], a[20:23], v[44:47]\n"
    "s_waitcnt lgkmcnt(4)\n"
    "ds_read_b128 a[84:87], v2 offset:26112\n"
    "v_add_f32 v9, v9, v62\n"
    "v_mfma_f32_16x16x32_bf16 v[20:23], a[88:91], a[56:59], v[20:23]\n"
    "v_exp_f32 v63, v63\n"
    "v_mfma_f32_16x16x32_bf16 v[28:31], a[88:91], a[72:75], v[28:31]\n"
    "v_add_f32 v9, v9, v63\n"
    "v_cvt_pk_bf16_f32 v59, v62, v63\n"
    "v_mfma_f32_16x16x32_bf16 v[36:39], a[88:91], a[8:11], v[36:39]\n"
    "v_exp_f32 v68, v68\n"
    "v_mfma_f32_16x16x32_bf16 v[44:47], a[88:91], a[24:27], v[44:47]\n"
    "s_waitcnt lgkmcnt(4)\n"
    "ds_read_b128 a[88:91], v2 offset:28672\n"
    "v_add_f32 v5, v5, v68\n"
    "v_mfma_f32_16x16x32_bf16 v[20:23], a[92:95], a[60:63], v[20:23]\n"
    "v_exp_f32 v69, v69\n"
    "v_mfma_f32_16x16x32_bf16 v[28:31], a[92:95], a[76:79], v[28:31]\n"
    "v_add_f32 v5, v5, v69\n"
    "v_cvt_pk_bf16_f32 v66, v68, v69\n"
    "v_mfma_f32_16x16x32_bf16 v[36:39], a[92:95], a[12:15], v[36:39]\n"
    "v_exp_f32 v70, v70\n"
    "v_mfma_f32_16x16x32_bf16 v[44:47], a[92:95], a[28:31], v[44:47]\n"
    "ds_read_b128 a[92:95], v2 offset:29184\n"
    "v_add_f32 v5, v5, v70\n"
    "v_mfma_f32_16x16x32_bf16 v[176:179], a[104:107], v[48:51], v[176:179]\n"
    "v_exp_f32 v71, v71\n"
    "v_mfma_f32_16x16x32_bf16 v[208:211], a[104:107], v[56:59], v[208:211]\n"
    "v_add_f32 v5, v5, v71\n"
    "v_cvt_pk_bf16_f32 v67, v70, v71\n"
    "v_exp_f32 v76, v76\n"
    "s_nop 0\n"
    "v_mfma_f32_16x16x32_bf16 v[80:83], a[104:107], v[64:67], v[80:83]\n"
    "v_add_f32 v6, v6, v76\n"
    "v_exp_f32 v77, v77\n"
    "v_exp_f32 v78, v78\n"
    "v_add_f32 v6, v6, v77\n"
    "v_cvt_pk_bf16_f32 v74, v76, v77\n"
    "v_add_f32 v6, v6, v78\n"
    "v_exp_f32 v79, v79\n"
    "v_rcp_f32 v12, v12\n"
    "v_add_f32 v6, v6, v79\n"
    "v_cvt_pk_bf16_f32 v75, v78, v79\n"
    "v_mul_f32 v144, v144, v12\n"
    "v_mul_f32 v145, v145, v12\n"
    "v_mfma_f32_16x16x32_bf16 v[112:115], a[104:107], v[72:75], 0\n"
    "ds_read_b64_tr_b16 a[104:105], v3 offset:42496\n"
    "ds_read_b64_tr_b16 a[106:107], v3 offset:46592\n"
    "v_mfma_f32_16x16x32_bf16 v[180:183], a[108:111], v[48:51], v[180:183]\n"
    "v_mul_f32 v146, v146, v12\n"
    "v_mul_f32 v147, v147, v12\n"
    "v_mfma_f32_16x16x32_bf16 v[212:215], a[108:111], v[56:59], v[212:215]\n"
    "v_mul_f32 v148, v148, v12\n"
    "v_mul_f32 v149, v149, v12\n"
    "v_mfma_f32_16x16x32_bf16 v[84:87], a[108:111], v[64:67], v[84:87]\n"
    "v_mul_f32 v150, v150, v12\n"
    "v_mul_f32 v151, v151, v12\n"
    "v_mfma_f32_16x16x32_bf16 v[116:119], a[108:111], v[72:75], 0\n"
    "ds_read_b64_tr_b16 a[108:109], v3 offset:42504\n"
    "ds_read_b64_tr_b16 a[110:111], v3 offset:46600\n"
    "v_mfma_f32_16x16x32_bf16 v[184:187], a[112:115], v[48:51], v[184:187]\n"
    "v_cvt_pk_bf16_f32 v144, v144, v145\n"
    "v_cvt_pk_bf16_f32 v145, v146, v147\n"
    "v_mfma_f32_16x16x32_bf16 v[216:219], a[112:115], v[56:59], v[216:219]\n"
    "v_cvt_pk_bf16_f32 v146, v148, v149\n"
    "v_cvt_pk_bf16_f32 v147, v150, v151\n"
    "v_mfma_f32_16x16x32_bf16 v[88:91], a[112:115], v[64:67], v[88:91]\n"
    "buffer_store_dwordx4 v[144:147], v4, s[52:55], s63 offen offset:0 sc0 sc1\n"
    "v_mul_f32 v152, v152, v12\n"
    "v_mfma_f32_16x16x32_bf16 v[120:123], a[112:115], v[72:75], 0\n"
    "ds_read_b64_tr_b16 a[112:113], v3 offset:49152\n"
    "ds_read_b64_tr_b16 a[114:115], v3 offset:53248\n"
    "v_mfma_f32_16x16x32_bf16 v[188:191], a[116:119], v[48:51], v[188:191]\n"
    "v_mul_f32 v153, v153, v12\n"
    "v_mul_f32 v154, v154, v12\n"
    "v_mfma_f32_16x16x32_bf16 v[220:223], a[116:119], v[56:59], v[220:223]\n"
    "v_mul_f32 v155, v155, v12\n"
    "v_mul_f32 v156, v156, v12\n"
    "v_mfma_f32_16x16x32_bf16 v[92:95], a[116:119], v[64:67], v[92:95]\n"
    "v_mul_f32 v157, v157, v12\n"
    "v_mul_f32 v158, v158, v12\n"
    "v_mfma_f32_16x16x32_bf16 v[124:127], a[116:119], v[72:75], 0\n"
    "ds_read_b64_tr_b16 a[116:117], v3 offset:49160\n"
    "ds_read_b64_tr_b16 a[118:119], v3 offset:53256\n"
    "v_mfma_f32_16x16x32_bf16 v[192:195], a[120:123], v[48:51], v[192:195]\n"
    "v_mul_f32 v159, v159, v12\n"
    "v_cvt_pk_bf16_f32 v152, v152, v153\n"
    "v_mfma_f32_16x16x32_bf16 v[224:227], a[120:123], v[56:59], v[224:227]\n"
    "v_cvt_pk_bf16_f32 v153, v154, v155\n"
    "v_cvt_pk_bf16_f32 v154, v156, v157\n"
    "v_mfma_f32_16x16x32_bf16 v[96:99], a[120:123], v[64:67], v[96:99]\n"
    "v_cvt_pk_bf16_f32 v155, v158, v159\n"
    "v_mul_f32 v160, v160, v12\n"
    "v_mfma_f32_16x16x32_bf16 v[128:131], a[120:123], v[72:75], 0\n"
    "v_exp_f32 v16, v16\n"
    "v_mfma_f32_16x16x32_bf16 v[196:199], a[124:127], v[48:51], v[196:199]\n"
    "buffer_store_dwordx4 v[152:155], v4, s[52:55], s63 offen offset:64 sc0 sc1\n"
    "ds_read_b64_tr_b16 a[120:121], v3 offset:49664\n"
    "v_mfma_f32_16x16x32_bf16 v[228:231], a[124:127], v[56:59], v[228:231]\n"
    "ds_read_b64_tr_b16 a[122:123], v3 offset:53760\n"
    "v_add_f32 v8, v8, v16\n"
    "v_mfma_f32_16x16x32_bf16 v[100:103], a[124:127], v[64:67], v[100:103]\n"
    "v_exp_f32 v17, v17\n"
    "v_mfma_f32_16x16x32_bf16 v[132:135], a[124:127], v[72:75], 0\n"
    "s_waitcnt lgkmcnt(8)\n"
    "ds_read_b64_tr_b16 a[124:125], v3 offset:49672\n"
    "ds_read_b64_tr_b16 a[126:127], v3 offset:53768\n"
    "v_mfma_f32_16x16x32_bf16 v[200:203], a[104:107], v[48:51], v[200:203]\n"
    "v_add_f32 v8, v8, v17\n"
    "v_cvt_pk_bf16_f32 v16, v16, v17\n"
    "v_mfma_f32_16x16x32_bf16 v[232:235], a[104:107], v[56:59], v[232:235]\n"
    "v_exp_f32 v18, v18\n"
    "v_mfma_f32_16x16x32_bf16 v[104:107], a[104:107], v[64:67], v[104:107]\n"
    "v_add_f32 v8, v8, v18\n"
    "v_mul_f32 v161, v161, v12\n"
    "v_mfma_f32_16x16x32_bf16 v[136:139], a[104:107], v[72:75], 0\n"
    "s_waitcnt lgkmcnt(8)\n"
    "v_exp_f32 v19, v19\n"
    "v_mfma_f32_16x16x32_bf16 v[204:207], a[108:111], v[48:51], v[204:207]\n"
    "ds_read_b64_tr_b16 a[104:105], v3 offset:50176\n"
    "ds_read_b64_tr_b16 a[106:107], v3 offset:54272\n"
    "v_mfma_f32_16x16x32_bf16 v[236:239], a[108:111], v[56:59], v[236:239]\n"
    "v_add_f32 v8, v8, v19\n"
    "v_cvt_pk_bf16_f32 v17, v18, v19\n"
    "v_mfma_f32_16x16x32_bf16 v[108:111], a[108:111], v[64:67], v[108:111]\n"
    "v_mul_f32 v162, v162, v12\n"
    "v_mul_f32 v163, v163, v12\n"
    "v_mfma_f32_16x16x32_bf16 v[140:143], a[108:111], v[72:75], 0\n"
    "ds_read_b64_tr_b16 a[108:109], v3 offset:50184\n"
    "ds_read_b64_tr_b16 a[110:111], v3 offset:54280\n"
    "v_mfma_f32_16x16x32_bf16 v[48:51], a[96:99], a[48:51], v[240:243]\n"
    "v_mul_f32 v164, v164, v12\n"
    "v_mul_f32 v165, v165, v12\n"
    "v_mfma_f32_16x16x32_bf16 v[56:59], a[96:99], a[64:67], v[244:247]\n"
    "v_mul_f32 v166, v166, v12\n"
    "v_mul_f32 v167, v167, v12\n"
    "v_mfma_f32_16x16x32_bf16 v[64:67], a[96:99], a[0:3], v[248:251]\n"
    "v_cvt_pk_bf16_f32 v160, v160, v161\n"
    "v_cvt_pk_bf16_f32 v161, v162, v163\n"
    "v_mfma_f32_16x16x32_bf16 v[72:75], a[96:99], a[16:19], v[252:255]\n"
    "ds_read_b128 a[96:99], v2 offset:29696\n"
    "v_cvt_pk_bf16_f32 v162, v164, v165\n"
    "v_mfma_f32_16x16x32_bf16 v[48:51], a[100:103], a[52:55], v[48:51]\n"
    "v_cvt_pk_bf16_f32 v163, v166, v167\n"
    "v_mul_f32 v168, v168, v12\n"
    "v_mfma_f32_16x16x32_bf16 v[56:59], a[100:103], a[68:71], v[56:59]\n"
    "buffer_store_dwordx4 v[160:163], v4, s[52:55], s63 offen offset:128 sc0 sc1\n"
    "v_mul_f32 v169, v169, v12\n"
    "v_mfma_f32_16x16x32_bf16 v[64:67], a[100:103], a[4:7], v[64:67]\n"
    "v_mul_f32 v170, v170, v12\n"
    "v_mul_f32 v171, v171, v12\n"
    "v_mfma_f32_16x16x32_bf16 v[72:75], a[100:103], a[20:23], v[72:75]\n"
    "ds_read_b128 a[100:103], v2 offset:30208\n"
    "v_mul_f32 v172, v172, v12\n"
    "v_mfma_f32_16x16x32_bf16 v[48:51], a[80:83], a[56:59], v[48:51]\n"
    "v_mul_f32 v173, v173, v12\n"
    "v_mul_f32 v174, v174, v12\n"
    "v_mfma_f32_16x16x32_bf16 v[56:59], a[80:83], a[72:75], v[56:59]\n"
    "v_mul_f32 v175, v175, v12\n"
    "v_cvt_pk_bf16_f32 v168, v168, v169\n"
    "v_mfma_f32_16x16x32_bf16 v[64:67], a[80:83], a[8:11], v[64:67]\n"
    "v_cvt_pk_bf16_f32 v169, v170, v171\n"
    "v_cvt_pk_bf16_f32 v170, v172, v173\n"
    "v_mfma_f32_16x16x32_bf16 v[72:75], a[80:83], a[24:27], v[72:75]\n"
    "ds_read_b128 a[80:83], v1 offset:0\n"
    "v_cvt_pk_bf16_f32 v171, v174, v175\n"
    "v_mfma_f32_16x16x32_bf16 v[48:51], a[84:87], a[60:63], v[48:51]\n"
    "buffer_store_dwordx4 v[168:171], v4, s[52:55], s63 offen offset:192 sc0 sc1\n"
    "v_mfma_f32_16x16x32_bf16 v[56:59], a[84:87], a[76:79], v[56:59]\n"
    "v_exp_f32 v24, v24\n"
    "v_mfma_f32_16x16x32_bf16 v[64:67], a[84:87], a[12:15], v[64:67]\n"
    "v_add_f32 v9, v9, v24\n"
    "v_mfma_f32_16x16x32_bf16 v[72:75], a[84:87], a[28:31], v[72:75]\n"
    "v_exp_f32 v25, v25\n"
    "v_mfma_f32_16x16x32_bf16 v[52:55], a[88:91], a[48:51], v[240:243]\n"
    "ds_read_b128 a[84:87], v1 offset:512\n"
    "v_add_f32 v9, v9, v25\n"
    "v_mfma_f32_16x16x32_bf16 v[60:63], a[88:91], a[64:67], v[244:247]\n"
    "v_cvt_pk_bf16_f32 v24, v24, v25\n"
    "v_mfma_f32_16x16x32_bf16 v[68:71], a[88:91], a[0:3], v[248:251]\n"
    "v_exp_f32 v26, v26\n"
    "v_mfma_f32_16x16x32_bf16 v[76:79], a[88:91], a[16:19], v[252:255]\n"
    "ds_read_b128 a[88:91], v1 offset:1024\n"
    "v_add_f32 v9, v9, v26\n"
    "v_mfma_f32_16x16x32_bf16 v[52:55], a[92:95], a[52:55], v[52:55]\n"
    "v_exp_f32 v27, v27\n"
    "v_mfma_f32_16x16x32_bf16 v[60:63], a[92:95], a[68:71], v[60:63]\n"
    "v_add_f32 v9, v9, v27\n"
    "v_cvt_pk_bf16_f32 v25, v26, v27\n"
    "v_mfma_f32_16x16x32_bf16 v[68:71], a[92:95], a[4:7], v[68:71]\n"
    "v_exp_f32 v32, v32\n"
    "v_mfma_f32_16x16x32_bf16 v[76:79], a[92:95], a[20:23], v[76:79]\n"
    "s_waitcnt lgkmcnt(4)\n"
    "ds_read_b128 a[92:95], v1 offset:1536\n"
    "v_add_f32 v5, v5, v32\n"
    "v_mfma_f32_16x16x32_bf16 v[52:55], a[96:99], a[56:59], v[52:55]\n"
    "v_exp_f32 v33, v33\n"
    "v_mfma_f32_16x16x32_bf16 v[60:63], a[96:99], a[72:75], v[60:63]\n"
    "v_add_f32 v5, v5, v33\n"
    "v_cvt_pk_bf16_f32 v32, v32, v33\n"
    "v_mfma_f32_16x16x32_bf16 v[68:71], a[96:99], a[8:11], v[68:71]\n"
    "v_exp_f32 v34, v34\n"
    "v_mfma_f32_16x16x32_bf16 v[76:79], a[96:99], a[24:27], v[76:79]\n"
    "s_waitcnt lgkmcnt(4)\n"
    "ds_read_b128 a[96:99], v1 offset:4096\n"
    "v_add_f32 v5, v5, v34\n"
    "v_mfma_f32_16x16x32_bf16 v[52:55], a[100:103], a[60:63], v[52:55]\n"
    "v_exp_f32 v35, v35\n"
    "v_mfma_f32_16x16x32_bf16 v[60:63], a[100:103], a[76:79], v[60:63]\n"
    "v_add_f32 v5, v5, v35\n"
    "v_cvt_pk_bf16_f32 v33, v34, v35\n"
    "v_mfma_f32_16x16x32_bf16 v[68:71], a[100:103], a[12:15], v[68:71]\n"
    "v_exp_f32 v40, v40\n"
    "v_mfma_f32_16x16x32_bf16 v[76:79], a[100:103], a[28:31], v[76:79]\n"
    "ds_read_b128 a[100:103], v1 offset:4608\n"
    "v_add_f32 v6, v6, v40\n"
    "v_exp_f32 v41, v41\n"
    "v_exp_f32 v42, v42\n"
    "v_add_f32 v6, v6, v41\n"
    "v_cvt_pk_bf16_f32 v40, v40, v41\n"
    "v_add_f32 v6, v6, v42\n"
    "v_exp_f32 v43, v43\n"
    "v_exp_f32 v20, v20\n"
    "v_add_f32 v6, v6, v43\n"
    "v_cvt_pk_bf16_f32 v41, v42, v43\n"
    "v_add_f32 v8, v8, v20\n"
    "v_exp_f32 v21, v21\n"
    "v_exp_f32 v22, v22\n"
    "v_add_f32 v8, v8, v21\n"
    "v_cvt_pk_bf16_f32 v18, v20, v21\n"
    "v_add_f32 v8, v8, v22\n"
    "v_exp_f32 v23, v23\n"
    "v_exp_f32 v28, v28\n"
    "v_add_f32 v8, v8, v23\n"
    "v_cvt_pk_bf16_f32 v19, v22, v23\n"
    "v_add_f32 v9, v9, v28\n"
    "s_nop 0\n"
    "v_mfma_f32_16x16x32_bf16 v[176:179], a[112:115], v[16:19], v[176:179]\n"
    "v_exp_f32 v29, v29\n"
    "v_exp_f32 v30, v30\n"
    "v_add_f32 v9, v9, v29\n"
    "v_cvt_pk_bf16_f32 v26, v28, v29\n"
    "v_add_f32 v9, v9, v30\n"
    "v_exp_f32 v31, v31\n"
    "v_exp_f32 v36, v36\n"
    "v_add_f32 v9, v9, v31\n"
    "v_cvt_pk_bf16_f32 v27, v30, v31\n"
    "v_add_f32 v5, v5, v36\n"
    "s_nop 0\n"
    "v_mfma_f32_16x16x32_bf16 v[208:211], a[112:115], v[24:27], v[208:211]\n"
    "v_exp_f32 v37, v37\n"
    "v_exp_f32 v38, v38\n"
    "v_add_f32 v5, v5, v37\n"
    "v_cvt_pk_bf16_f32 v34, v36, v37\n"
    "v_add_f32 v5, v5, v38\n"
    "v_exp_f32 v39, v39\n"
    "v_exp_f32 v44, v44\n"
    "v_add_f32 v5, v5, v39\n"
    "v_cvt_pk_bf16_f32 v35, v38, v39\n"
    "v_add_f32 v6, v6, v44\n"
    "s_nop 0\n"
    "v_mfma_f32_16x16x32_bf16 v[80:83], a[112:115], v[32:35], v[80:83]\n"
    "v_exp_f32 v45, v45\n"
    "v_exp_f32 v46, v46\n"
    "v_add_f32 v6, v6, v45\n"
    "v_cvt_pk_bf16_f32 v42, v44, v45\n"
    "v_add_f32 v6, v6, v46\n"
    "v_exp_f32 v47, v47\n"
    "v_exp_f32 v48, v48\n"
    "v_add_f32 v6, v6, v47\n"
    "v_cvt_pk_bf16_f32 v43, v46, v47\n"
    "v_add_f32 v8, v8, v48\n"
    "s_nop 0\n"
    "v_mfma_f32_16x16x32_bf16 v[112:115], a[112:115], v[40:43], v[112:115]\n"
    "v_exp_f32 v49, v49\n"
    "v_mfma_f32_16x16x32_bf16 v[180:183], a[116:119], v[16:19], v[180:183]\n"
    "ds_read_b64_tr_b16 a[112:113], v3 offset:50688\n"
    "ds_read_b64_tr_b16 a[114:115], v3 offset:54784\n"
    "v_mfma_f32_16x16x32_bf16 v[212:215], a[116:119], v[24:27], v[212:215]\n"
    "v_add_f32 v8, v8, v49\n"
    "v_cvt_pk_bf16_f32 v48, v48, v49\n"
    "v_mfma_f32_16x16x32_bf16 v[84:87], a[116:119], v[32:35], v[84:87]\n"
    "v_exp_f32 v50, v50\n"
    "v_mfma_f32_16x16x32_bf16 v[116:119], a[116:119], v[40:43], v[116:119]\n"
    "ds_read_b64_tr_b16 a[116:117], v3 offset:50696\n"
    "ds_read_b64_tr_b16 a[118:119], v3 offset:54792\n"
    "v_mfma_f32_16x16x32_bf16 v[184:187], a[120:123], v[16:19], v[184:187]\n"
    "v_add_f32 v8, v8, v50\n"
    "v_mfma_f32_16x16x32_bf16 v[216:219], a[120:123], v[24:27], v[216:219]\n"
    "v_exp_f32 v51, v51\n"
    "v_mfma_f32_16x16x32_bf16 v[88:91], a[120:123], v[32:35], v[88:91]\n"
    "v_add_f32 v8, v8, v51\n"
    "v_cvt_pk_bf16_f32 v49, v50, v51\n"
    "v_mfma_f32_16x16x32_bf16 v[120:123], a[120:123], v[40:43], v[120:123]\n"
    "v_exp_f32 v56, v56\n"
    "v_mfma_f32_16x16x32_bf16 v[188:191], a[124:127], v[16:19], v[188:191]\n"
    "ds_read_b64_tr_b16 a[120:121], v3 offset:57344\n"
    "ds_read_b64_tr_b16 a[122:123], v3 offset:61440\n"
    "v_mfma_f32_16x16x32_bf16 v[220:223], a[124:127], v[24:27], v[220:223]\n"
    "v_add_f32 v9, v9, v56\n"
    "v_mfma_f32_16x16x32_bf16 v[92:95], a[124:127], v[32:35], v[92:95]\n"
    "v_exp_f32 v57, v57\n"
    "v_mfma_f32_16x16x32_bf16 v[124:127], a[124:127], v[40:43], v[124:127]\n"
    "ds_read_b64_tr_b16 a[124:125], v3 offset:57352\n"
    "ds_read_b64_tr_b16 a[126:127], v3 offset:61448\n"
    "v_mfma_f32_16x16x32_bf16 v[192:195], a[104:107], v[16:19], v[192:195]\n"
    "v_add_f32 v9, v9, v57\n"
    "v_cvt_pk_bf16_f32 v56, v56, v57\n"
    "v_mfma_f32_16x16x32_bf16 v[224:227], a[104:107], v[24:27], v[224:227]\n"
    "v_exp_f32 v58, v58\n"
    "v_mfma_f32_16x16x32_bf16 v[96:99], a[104:107], v[32:35], v[96:99]\n"
    "v_add_f32 v9, v9, v58\n"
    "v_mfma_f32_16x16x32_bf16 v[128:131], a[104:107], v[40:43], v[128:131]\n"
    "v_exp_f32 v59, v59\n"
    "v_mfma_f32_16x16x32_bf16 v[196:199], a[108:111], v[16:19], v[196:199]\n"
    "ds_read_b64_tr_b16 a[104:105], v3 offset:57856\n"
    "ds_read_b64_tr_b16 a[106:107], v3 offset:61952\n"
    "v_mfma_f32_16x16x32_bf16 v[228:231], a[108:111], v[24:27], v[228:231]\n"
    "v_add_f32 v9, v9, v59\n"
    "v_cvt_pk_bf16_f32 v57, v58, v59\n"
    "v_mfma_f32_16x16x32_bf16 v[100:103], a[108:111], v[32:35], v[100:103]\n"
    "v_exp_f32 v64, v64\n"
    "v_mfma_f32_16x16x32_bf16 v[132:135], a[108:111], v[40:43], v[132:135]\n"
    "s_waitcnt lgkmcnt(8)\n"
    "ds_read_b64_tr_b16 a[108:109], v3 offset:57864\n"
    "ds_read_b64_tr_b16 a[110:111], v3 offset:61960\n"
    "v_mfma_f32_16x16x32_bf16 v[200:203], a[112:115], v[16:19], v[200:203]\n"
    "v_add_f32 v5, v5, v64\n"
    "v_mfma_f32_16x16x32_bf16 v[232:235], a[112:115], v[24:27], v[232:235]\n"
    "v_exp_f32 v65, v65\n"
    "v_mfma_f32_16x16x32_bf16 v[104:107], a[112:115], v[32:35], v[104:107]\n"
    "v_add_f32 v5, v5, v65\n"
    "v_cvt_pk_bf16_f32 v64, v64, v65\n"
    "v_mfma_f32_16x16x32_bf16 v[136:139], a[112:115], v[40:43], v[136:139]\n"
    "s_waitcnt lgkmcnt(8)\n"
    "v_exp_f32 v66, v66\n"
    "v_mfma_f32_16x16x32_bf16 v[204:207], a[116:119], v[16:19], v[204:207]\n"
    "ds_read_b64_tr_b16 a[112:113], v3 offset:58368\n"
    "ds_read_b64_tr_b16 a[114:115], v3 offset:62464\n"
    "v_mfma_f32_16x16x32_bf16 v[236:239], a[116:119], v[24:27], v[236:239]\n"
    "v_add_f32 v5, v5, v66\n"
    "v_mfma_f32_16x16x32_bf16 v[108:111], a[116:119], v[32:35], v[108:111]\n"
    "v_exp_f32 v67, v67\n"
    "v_mfma_f32_16x16x32_bf16 v[140:143], a[116:119], v[40:43], v[140:143]\n"
    "ds_read_b64_tr_b16 a[116:117], v3 offset:58376\n"
    "ds_read_b64_tr_b16 a[118:119], v3 offset:62472\n"
    "v_mfma_f32_16x16x32_bf16 v[24:27], a[80:83], a[64:67], v[244:247]\n"
    "v_add_f32 v5, v5, v67\n"
    "v_cvt_pk_bf16_f32 v65, v66, v67\n"
    "v_mfma_f32_16x16x32_bf16 v[32:35], a[80:83], a[0:3], v[248:251]\n"
    "v_exp_f32 v72, v72\n"
    "v_mfma_f32_16x16x32_bf16 v[40:43], a[80:83], a[16:19], v[252:255]\n"
    "ds_read_b128 a[80:83], v1 offset:5120\n"
    "v_add_f32 v6, v6, v72\n"
    "v_mfma_f32_16x16x32_bf16 v[24:27], a[84:87], a[68:71], v[24:27]\n"
    "v_exp_f32 v73, v73\n"
    "v_mfma_f32_16x16x32_bf16 v[32:35], a[84:87], a[4:7], v[32:35]\n"
    "v_add_f32 v6, v6, v73\n"
    "v_cvt_pk_bf16_f32 v72, v72, v73\n"
    "v_mfma_f32_16x16x32_bf16 v[40:43], a[84:87], a[20:23], v[40:43]\n"
    "v_exp_f32 v74, v74\n"
    "v_mfma_f32_16x16x32_bf16 v[24:27], a[88:91], a[72:75], v[24:27]\n"
    "ds_read_b128 a[84:87], v1 offset:5632\n"
    "v_add_f32 v6, v6, v74\n"
    "v_mfma_f32_16x16x32_bf16 v[32:35], a[88:91], a[8:11], v[32:35]\n"
    "v_exp_f32 v75, v75\n"
    "v_mfma_f32_16x16x32_bf16 v[40:43], a[88:91], a[24:27], v[40:43]\n"
    "ds_read_b128 a[88:91], v1 offset:8192\n"
    "v_add_f32 v6, v6, v75\n"
    "v_mfma_f32_16x16x32_bf16 v[24:27], a[92:95], a[76:79], v[24:27]\n"
    "v_cvt_pk_bf16_f32 v73, v74, v75\n"
    "v_mfma_f32_16x16x32_bf16 v[32:35], a[92:95], a[12:15], v[32:35]\n"
    "v_exp_f32 v52, v52\n"
    "v_mfma_f32_16x16x32_bf16 v[40:43], a[92:95], a[28:31], v[40:43]\n"
    "ds_read_b128 a[92:95], v1 offset:8704\n"
    "v_add_f32 v8, v8, v52\n"
    "v_mfma_f32_16x16x32_bf16 v[28:31], a[96:99], a[64:67], v[244:247]\n"
    "v_exp_f32 v53, v53\n"
    "v_mfma_f32_16x16x32_bf16 v[36:39], a[96:99], a[0:3], v[248:251]\n"
    "v_add_f32 v8, v8, v53\n"
    "v_cvt_pk_bf16_f32 v50, v52, v53\n"
    "v_mfma_f32_16x16x32_bf16 v[44:47], a[96:99], a[16:19], v[252:255]\n"
    "v_exp_f32 v54, v54\n"
    "v_mfma_f32_16x16x32_bf16 v[28:31], a[100:103], a[68:71], v[28:31]\n"
    "ds_read_b128 a[96:99], v1 offset:9216\n"
    "v_add_f32 v8, v8, v54\n"
    "v_mfma_f32_16x16x32_bf16 v[36:39], a[100:103], a[4:7], v[36:39]\n"
    "v_exp_f32 v55, v55\n"
    "v_mfma_f32_16x16x32_bf16 v[44:47], a[100:103], a[20:23], v[44:47]\n"
    "s_waitcnt lgkmcnt(4)\n"
    "ds_read_b128 a[100:103], v1 offset:9728\n"
    "v_add_f32 v8, v8, v55\n"
    "v_mfma_f32_16x16x32_bf16 v[28:31], a[80:83], a[72:75], v[28:31]\n"
    "v_cvt_pk_bf16_f32 v51, v54, v55\n"
    "v_mov_b32 v12, v8\n"
    "v_mfma_f32_16x16x32_bf16 v[36:39], a[80:83], a[8:11], v[36:39]\n"
    "s_mov_b32 s62, 131072\n"
    "v_exp_f32 v60, v60\n"
    "v_mfma_f32_16x16x32_bf16 v[44:47], a[80:83], a[24:27], v[44:47]\n"
    "s_waitcnt lgkmcnt(4)\n"
    "ds_read_b128 a[80:83], v1 offset:12288\n"
    "v_add_f32 v9, v9, v60\n"
    "v_exp_f32 v61, v61\n"
    "v_mfma_f32_16x16x32_bf16 v[28:31], a[84:87], a[76:79], v[28:31]\n"
    "v_add_f32 v9, v9, v61\n"
    "v_cvt_pk_bf16_f32 v58, v60, v61\n"
    "v_mfma_f32_16x16x32_bf16 v[36:39], a[84:87], a[12:15], v[36:39]\n"
    "v_exp_f32 v62, v62\n"
    "v_mfma_f32_16x16x32_bf16 v[44:47], a[84:87], a[28:31], v[44:47]\n"
    "ds_read_b128 a[84:87], v1 offset:12800\n"
    "v_add_f32 v9, v9, v62\n"
    "v_mfma_f32_16x16x32_bf16 v[176:179], a[120:123], v[48:51], v[176:179]\n"
    "v_exp_f32 v63, v63\n"
    "v_exp_f32 v68, v68\n"
    "v_add_f32 v9, v9, v63\n"
    "v_cvt_pk_bf16_f32 v59, v62, v63\n"
    "v_add_f32 v5, v5, v68\n"
    "v_permlane32_swap_b32 v8, v12\n"
    "v_mfma_f32_16x16x32_bf16 v[208:211], a[120:123], v[56:59], v[208:211]\n"
    "v_exp_f32 v69, v69\n"
    "v_exp_f32 v70, v70\n"
    "v_add_f32 v5, v5, v69\n"
    "v_cvt_pk_bf16_f32 v66, v68, v69\n"
    "v_add_f32 v5, v5, v70\n"
    "v_exp_f32 v71, v71\n"
    "v_exp_f32 v76, v76\n"
    "v_add_f32 v5, v5, v71\n"
    "v_cvt_pk_bf16_f32 v67, v70, v71\n"
    "v_add_f32 v6, v6, v76\n"
    "v_add_f32 v8, v8, v12\n"
    "v_mfma_f32_16x16x32_bf16 v[80:83], a[120:123], v[64:67], v[80:83]\n"
    "v_exp_f32 v77, v77\n"
    "v_exp_f32 v78, v78\n"
    "v_add_f32 v6, v6, v77\n"
    "v_cvt_pk_bf16_f32 v74, v76, v77\n"
    "v_add_f32 v6, v6, v78\n"
    "v_exp_f32 v79, v79\n"
    "v_exp_f32 v24, v24\n"
    "v_add_f32 v6, v6, v79\n"
    "v_cvt_pk_bf16_f32 v75, v78, v79\n"
    "v_add_f32 v9, v9, v24\n"
    "v_mov_b32 v12, v8\n"
    "v_mfma_f32_16x16x32_bf16 v[112:115], a[120:123], v[72:75], v[112:115]\n"
    "v_exp_f32 v25, v25\n"
    "v_mfma_f32_16x16x32_bf16 v[180:183], a[124:127], v[48:51], v[180:183]\n"
    "ds_read_b64_tr_b16 a[120:121], v3 offset:58880\n"
    "ds_read_b64_tr_b16 a[122:123], v3 offset:62976\n"
    "v_mfma_f32_16x16x32_bf16 v[212:215], a[124:127], v[56:59], v[212:215]\n"
    "v_add_f32 v9, v9, v25\n"
    "v_cvt_pk_bf16_f32 v24, v24, v25\n"
    "v_mfma_f32_16x16x32_bf16 v[84:87], a[124:127], v[64:67], v[84:87]\n"
    "v_exp_f32 v26, v26\n"
    "v_mfma_f32_16x16x32_bf16 v[116:119], a[124:127], v[72:75], v[116:119]\n"
    "ds_read_b64_tr_b16 a[124:125], v3 offset:58888\n"
    "ds_read_b64_tr_b16 a[126:127], v3 offset:62984\n"
    "v_mfma_f32_16x16x32_bf16 v[184:187], a[104:107], v[48:51], v[184:187]\n"
    "v_add_f32 v9, v9, v26\n"
    "v_permlane16_swap_b32 v8, v12\n"
    "v_mfma_f32_16x16x32_bf16 v[216:219], a[104:107], v[56:59], v[216:219]\n"
    "v_exp_f32 v27, v27\n"
    "v_mfma_f32_16x16x32_bf16 v[88:91], a[104:107], v[64:67], v[88:91]\n"
    "v_add_f32 v9, v9, v27\n"
    "v_cvt_pk_bf16_f32 v25, v26, v27\n"
    "v_mfma_f32_16x16x32_bf16 v[120:123], a[104:107], v[72:75], v[120:123]\n"
    "v_exp_f32 v32, v32\n"
    "v_mfma_f32_16x16x32_bf16 v[188:191], a[108:111], v[48:51], v[188:191]\n"
    "ds_read_b64_tr_b16 a[104:105], v3 offset:0\n"
    "ds_read_b64_tr_b16 a[106:107], v3 offset:4096\n"
    "v_mfma_f32_16x16x32_bf16 v[220:223], a[108:111], v[56:59], v[220:223]\n"
    "v_add_f32 v5, v5, v32\n"
    "v_add_f32 v12, v8, v12\n"
    "v_mfma_f32_16x16x32_bf16 v[92:95], a[108:111], v[64:67], v[92:95]\n"
    "v_exp_f32 v33, v33\n"
    "v_mfma_f32_16x16x32_bf16 v[124:127], a[108:111], v[72:75], v[124:127]\n"
    "ds_read_b64_tr_b16 a[108:109], v3 offset:8\n"
    "ds_read_b64_tr_b16 a[110:111], v3 offset:4104\n"
    "v_mfma_f32_16x16x32_bf16 v[192:195], a[112:115], v[48:51], v[192:195]\n"
    "v_add_f32 v5, v5, v33\n"
    "v_cvt_pk_bf16_f32 v32, v32, v33\n"
    "v_mfma_f32_16x16x32_bf16 v[224:227], a[112:115], v[56:59], v[224:227]\n"
    "v_exp_f32 v34, v34\n"
    "v_mfma_f32_16x16x32_bf16 v[96:99], a[112:115], v[64:67], v[96:99]\n"
    "v_add_f32 v5, v5, v34\n"
    "v_cmp_ngt_f32 vcc, 0x5d800000, v12\n"
    "v_mfma_f32_16x16x32_bf16 v[128:131], a[112:115], v[72:75], v[128:131]\n"
    "v_exp_f32 v35, v35\n"
    "v_mfma_f32_16x16x32_bf16 v[196:199], a[116:119], v[48:51], v[196:199]\n"
    "s_or_b64 s[64:65], s[64:65], vcc\n"
    "ds_read_b64_tr_b16 a[112:113], v3 offset:512\n"
    "ds_read_b64_tr_b16 a[114:115], v3 offset:4608\n"
    "v_mfma_f32_16x16x32_bf16 v[228:231], a[116:119], v[56:59], v[228:231]\n"
    "v_add_f32 v5, v5, v35\n"
    "v_cvt_pk_bf16_f32 v33, v34, v35\n"
    "v_mfma_f32_16x16x32_bf16 v[100:103], a[116:119], v[64:67], v[100:103]\n"
    "v_exp_f32 v40, v40\n"
    "v_mfma_f32_16x16x32_bf16 v[132:135], a[116:119], v[72:75], v[132:135]\n"
    "s_waitcnt lgkmcnt(8)\n"
    "ds_read_b64_tr_b16 a[116:117], v3 offset:520\n"
    "ds_read_b64_tr_b16 a[118:119], v3 offset:4616\n"
    "v_mfma_f32_16x16x32_bf16 v[200:203], a[120:123], v[48:51], v[200:203]\n"
    "v_add_f32 v6, v6, v40\n"
    "v_mfma_f32_16x16x32_bf16 v[232:235], a[120:123], v[56:59], v[232:235]\n"
    "v_exp_f32 v41, v41\n"
    "v_mfma_f32_16x16x32_bf16 v[104:107], a[120:123], v[64:67], v[104:107]\n"
    "v_add_f32 v6, v6, v41\n"
    "v_cvt_pk_bf16_f32 v40, v40, v41\n"
    "v_mfma_f32_16x16x32_bf16 v[136:139], a[120:123], v[72:75], v[136:139]\n"
    "s_waitcnt lgkmcnt(8)\n"
    "v_exp_f32 v42, v42\n"
    "v_mfma_f32_16x16x32_bf16 v[204:207], a[124:127], v[48:51], v[204:207]\n"
    "ds_read_b64_tr_b16 a[120:121], v3 offset:1024\n"
    "ds_read_b64_tr_b16 a[122:123], v3 offset:5120\n"
    "v_mfma_f32_16x16x32_bf16 v[236:239], a[124:127], v[56:59], v[236:239]\n"
    "v_add_f32 v6, v6, v42\n"
    "v_mfma_f32_16x16x32_bf16 v[108:111], a[124:127], v[64:67], v[108:111]\n"
    "v_exp_f32 v43, v43\n"
    "v_mfma_f32_16x16x32_bf16 v[140:143], a[124:127], v[72:75], v[140:143]\n"
    "ds_read_b64_tr_b16 a[124:125], v3 offset:1032\n"
    "ds_read_b64_tr_b16 a[126:127], v3 offset:5128\n"
    "v_mfma_f32_16x16x32_bf16 v[56:59], a[88:91], a[64:67], v[244:247]\n"
    "v_add_f32 v6, v6, v43\n"
    "v_cvt_pk_bf16_f32 v41, v42, v43\n"
    "v_mfma_f32_16x16x32_bf16 v[64:67], a[88:91], a[0:3], v[248:251]\n"
    "v_exp_f32 v28, v28\n"
    "v_mfma_f32_16x16x32_bf16 v[72:75], a[88:91], a[16:19], v[252:255]\n"
    "ds_read_b128 a[88:91], v1 offset:13312\n"
    "v_add_f32 v9, v9, v28\n"
    "v_mfma_f32_16x16x32_bf16 v[56:59], a[92:95], a[68:71], v[56:59]\n"
    "v_exp_f32 v29, v29\n"
    "v_mfma_f32_16x16x32_bf16 v[64:67], a[92:95], a[4:7], v[64:67]\n"
    "v_add_f32 v9, v9, v29\n"
    "v_cvt_pk_bf16_f32 v26, v28, v29\n"
    "v_mfma_f32_16x16x32_bf16 v[72:75], a[92:95], a[20:23], v[72:75]\n"
    "v_exp_f32 v30, v30\n"
    "v_mfma_f32_16x16x32_bf16 v[56:59], a[96:99], a[72:75], v[56:59]\n"
    "ds_read_b128 a[92:95], v1 offset:13824\n"
    "v_add_f32 v9, v9, v30\n"
    "v_mfma_f32_16x16x32_bf16 v[64:67], a[96:99], a[8:11], v[64:67]\n"
    "v_exp_f32 v31, v31\n"
    "v_mfma_f32_16x16x32_bf16 v[72:75], a[96:99], a[24:27], v[72:75]\n"
    "ds_read_b128 a[96:99], v1 offset:16384\n"
    "v_add_f32 v9, v9, v31\n"
    "v_mfma_f32_16x16x32_bf16 v[56:59], a[100:103], a[76:79], v[56:59]\n"
    "v_cvt_pk_bf16_f32 v27, v30, v31\n"
    "v_mfma_f32_16x16x32_bf16 v[64:67], a[100:103], a[12:15], v[64:67]\n"
    "v_exp_f32 v36, v36\n"
    "v_mfma_f32_16x16x32_bf16 v[72:75], a[100:103], a[28:31], v[72:75]\n"
    "ds_read_b128 a[100:103], v1 offset:16896\n"
    "v_add_f32 v5, v5, v36\n"
    "v_mfma_f32_16x16x32_bf16 v[60:63], a[80:83], a[64:67], v[244:247]\n"
    "v_exp_f32 v37, v37\n"
    "v_mfma_f32_16x16x32_bf16 v[68:71], a[80:83], a[0:3], v[248:251]\n"
    "v_add_f32 v5, v5, v37\n"
    "v_cvt_pk_bf16_f32 v34, v36, v37\n"
    "v_mfma_f32_16x16x32_bf16 v[76:79], a[80:83], a[16:19], v[252:255]\n"
    "v_exp_f32 v38, v38\n"
    "v_mfma_f32_16x16x32_bf16 v[60:63], a[84:87], a[68:71], v[60:63]\n"
    "ds_read_b128 a[80:83], v1 offset:17408\n"
    "v_add_f32 v5, v5, v38\n"
    "v_mfma_f32_16x16x32_bf16 v[68:71], a[84:87], a[4:7], v[68:71]\n"
    "v_exp_f32 v39, v39\n"
    "v_mfma_f32_16x16x32_bf16 v[76:79], a[84:87], a[20:23], v[76:79]\n"
    "s_waitcnt lgkmcnt(4)\n"
    "ds_read_b128 a[84:87], v1 offset:17920\n"
    "v_add_f32 v5, v5, v39\n"
    "v_mfma_f32_16x16x32_bf16 v[60:63], a[88:91], a[72:75], v[60:63]\n"
    "v_cvt_pk_bf16_f32 v35, v38, v39\n"
    "v_mfma_f32_16x16x32_bf16 v[68:71], a[88:91], a[8:11], v[68:71]\n"
    "v_exp_f32 v44, v44\n"
    "v_mfma_f32_16x16x32_bf16 v[76:79], a[88:91], a[24:27], v[76:79]\n"
    "s_waitcnt lgkmcnt(4)\n"
    "ds_read_b128 a[88:91], v1 offset:20480\n"
    "v_add_f32 v6, v6, v44\n"
    "v_exp_f32 v45, v45\n"
    "v_mfma_f32_16x16x32_bf16 v[60:63], a[92:95], a[76:79], v[60:63]\n"
    "v_add_f32 v6, v6, v45\n"
    "v_cvt_pk_bf16_f32 v42, v44, v45\n"
    "v_mfma_f32_16x16x32_bf16 v[68:71], a[92:95], a[12:15], v[68:71]\n"
    "v_exp_f32 v46, v46\n"
    "v_mfma_f32_16x16x32_bf16 v[76:79], a[92:95], a[28:31], v[76:79]\n"
    "ds_read_b128 a[92:95], v1 offset:20992\n"
    "v_add_f32 v6, v6, v46\n"
    "v_mfma_f32_16x16x32_bf16 v[208:211], a[128:131], v[24:27], v[208:211]\n"
    "v_exp_f32 v47, v47\n"
    "v_mfma_f32_16x16x32_bf16 v[80:83], a[128:131], v[32:35], v[80:83]\n"
    "v_add_f32 v6, v6, v47\n"
    "v_cvt_pk_bf16_f32 v43, v46, v47\n"
    "v_exp_f32 v56, v56\n"
    "s_nop 0\n"
    "v_mfma_f32_16x16x32_bf16 v[112:115], a[128:131], v[40:43], v[112:115]\n"
    "v_add_f32 v9, v9, v56\n"
    "v_mfma_f32_16x16x32_bf16 v[212:215], a[132:135], v[24:27], v[212:215]\n"
    "v_exp_f32 v57, v57\n"
    "v_mfma_f32_16x16x32_bf16 v[84:87], a[132:135], v[32:35], v[84:87]\n"
    "v_add_f32 v9, v9, v57\n"
    "v_cvt_pk_bf16_f32 v56, v56, v57\n"
    "v_mfma_f32_16x16x32_bf16 v[116:119], a[132:135], v[40:43], v[116:119]\n"
    "v_exp_f32 v58, v58\n"
    "v_mfma_f32_16x16x32_bf16 v[216:219], a[136:139], v[24:27], v[216:219]\n"
    "v_add_f32 v9, v9, v58\n"
    "v_mfma_f32_16x16x32_bf16 v[88:91], a[136:139], v[32:35], v[88:91]\n"
    "v_exp_f32 v59, v59\n"
    "v_mfma_f32_16x16x32_bf16 v[120:123], a[136:139], v[40:43], v[120:123]\n"
    "v_add_f32 v9, v9, v59\n"
    "v_cvt_pk_bf16_f32 v57, v58, v59\n"
    "v_mfma_f32_16x16x32_bf16 v[220:223], a[140:143], v[24:27], v[220:223]\n"
    "v_exp_f32 v64, v64\n"
    "v_mfma_f32_16x16x32_bf16 v[92:95], a[140:143], v[32:35], v[92:95]\n"
    "v_add_f32 v5, v5, v64\n"
    "v_mfma_f32_16x16x32_bf16 v[124:127], a[140:143], v[40:43], v[124:127]\n"
    "v_exp_f32 v65, v65\n"
    "v_mfma_f32_16x16x32_bf16 v[224:227], a[144:147], v[24:27], v[224:227]\n"
    "v_add_f32 v5, v5, v65\n"
    "v_cvt_pk_bf16_f32 v64, v64, v65\n"
    "v_mfma_f32_16x16x32_bf16 v[96:99], a[144:147], v[32:35], v[96:99]\n"
    "v_exp_f32 v66, v66\n"
    "v_mfma_f32_16x16x32_bf16 v[128:131], a[144:147], v[40:43], v[128:131]\n"
    "v_add_f32 v5, v5, v66\n"
    "v_mfma_f32_16x16x32_bf16 v[228:231], a[148:151], v[24:27], v[228:231]\n"
    "v_exp_f32 v67, v67\n"
    "v_mfma_f32_16x16x32_bf16 v[100:103], a[148:151], v[32:35], v[100:103]\n"
    "v_add_f32 v5, v5, v67\n"
    "v_cvt_pk_bf16_f32 v65, v66, v67\n"
    "v_mfma_f32_16x16x32_bf16 v[132:135], a[148:151], v[40:43], v[132:135]\n"
    "v_rcp_f32 v12, v12\n"
    "v_mfma_f32_16x16x32_bf16 v[232:235], a[152:155], v[24:27], v[232:235]\n"
    "v_exp_f32 v72, v72\n"
    "v_mfma_f32_16x16x32_bf16 v[104:107], a[152:155], v[32:35], v[104:107]\n"
    "v_add_f32 v6, v6, v72\n"
    "v_mul_f32 v176, v176, v12\n"
    "v_mfma_f32_16x16x32_bf16 v[136:139], a[152:155], v[40:43], v[136:139]\n"
    "v_exp_f32 v73, v73\n"
    "v_mfma_f32_16x16x32_bf16 v[236:239], a[156:159], v[24:27], v[236:239]\n"
    "v_add_f32 v6, v6, v73\n"
    "v_cvt_pk_bf16_f32 v72, v72, v73\n"
    "v_mfma_f32_16x16x32_bf16 v[108:111], a[156:159], v[32:35], v[108:111]\n"
    "v_exp_f32 v74, v74\n"
    "v_mfma_f32_16x16x32_bf16 v[140:143], a[156:159], v[40:43], v[140:143]\n"
    "s_waitcnt lgkmcnt(5)\n"
    "s_mov_b32 s76, 49152\n"
    "v_mul_f32 v177, v177, v12\n"
    "v_add_f32 v6, v6, v74\n"
    "v_mfma_f32_16x16x32_bf16 v[24:27], a[96:99], a[64:67], v[244:247]\n"
    "buffer_load_dword v15, v14, s[72:75], s76 offen\n"
    "v_mul_f32 v178, v178, v12\n"
    "v_mfma_f32_16x16x32_bf16 v[32:35], a[96:99], a[0:3], v[248:251]\n"
    "v_exp_f32 v75, v75\n"
    "v_mfma_f32_16x16x32_bf16 v[40:43], a[96:99], a[16:19], v[252:255]\n"
    "s_waitcnt lgkmcnt(4)\n"
    "ds_read_b128 a[96:99], v1 offset:21504\n"
    "v_mul_f32 v179, v179, v12\n"
    "v_mfma_f32_16x16x32_bf16 v[24:27], a[100:103], a[68:71], v[24:27]\n"
    "v_add_f32 v6, v6, v75\n"
    "v_cvt_pk_bf16_f32 v73, v74, v75\n"
    "v_mfma_f32_16x16x32_bf16 v[32:35], a[100:103], a[4:7], v[32:35]\n"
    "v_mul_f32 v180, v180, v12\n"
    "v_mul_f32 v181, v181, v12\n"
    "v_mfma_f32_16x16x32_bf16 v[40:43], a[100:103], a[20:23], v[40:43]\n"
    "s_waitcnt lgkmcnt(4)\n"
    "ds_read_b128 a[100:103], v1 offset:22016\n"
    "v_mul_f32 v182, v182, v12\n"
    "v_mfma_f32_16x16x32_bf16 v[24:27], a[80:83], a[72:75], v[24:27]\n"
    "v_mul_f32 v183, v183, v12\n"
    "v_cvt_pk_bf16_f32 v176, v176, v177\n"
    "v_mfma_f32_16x16x32_bf16 v[32:35], a[80:83], a[8:11], v[32:35]\n"
    "v_cvt_pk_bf16_f32 v177, v178, v179\n"
    "v_cvt_pk_bf16_f32 v178, v180, v181\n"
    "v_mfma_f32_16x16x32_bf16 v[40:43], a[80:83], a[24:27], v[40:43]\n"
    "s_waitcnt lgkmcnt(4)\n"
    "ds_read_b128 a[80:83], v1 offset:24576\n"
    "v_cvt_pk_bf16_f32 v179, v182, v183\n"
    "v_mfma_f32_16x16x32_bf16 v[24:27], a[84:87], a[76:79], v[24:27]\n"
    "buffer_store_dwordx4 v[176:179], v4, s[52:55], s62 offen offset:0 sc0 sc1\n"
    "v_mul_f32 v184, v184, v12\n"
    "v_mfma_f32_16x16x32_bf16 v[32:35], a[84:87], a[12:15], v[32:35]\n"
    "v_mul_f32 v185, v185, v12\n"
    "v_mul_f32 v186, v186, v12\n"
    "v_mfma_f32_16x16x32_bf16 v[40:43], a[84:87], a[28:31], v[40:43]\n"
    "s_waitcnt lgkmcnt(4)\n"
    "ds_read_b128 a[84:87], v1 offset:25088\n"
    "v_mul_f32 v187, v187, v12\n"
    "v_mfma_f32_16x16x32_bf16 v[28:31], a[88:91], a[64:67], v[244:247]\n"
    "v_mul_f32 v188, v188, v12\n"
    "v_mul_f32 v189, v189, v12\n"
    "v_mfma_f32_16x16x32_bf16 v[36:39], a[88:91], a[0:3], v[248:251]\n"
    "v_mul_f32 v190, v190, v12\n"
    "v_mul_f32 v191, v191, v12\n"
    "v_mfma_f32_16x16x32_bf16 v[44:47], a[88:91], a[16:19], v[252:255]\n"
    "s_waitcnt lgkmcnt(4)\n"
    "ds_read_b128 a[88:91], v1 offset:25600\n"
    "v_cvt_pk_bf16_f32 v184, v184, v185\n"
    "v_mfma_f32_16x16x32_bf16 v[28:31], a[92:95], a[68:71], v[28:31]\n"
    "v_cvt_pk_bf16_f32 v185, v186, v187\n"
    "v_cvt_pk_bf16_f32 v186, v188, v189\n"
    "v_mfma_f32_16x16x32_bf16 v[36:39], a[92:95], a[4:7], v[36:39]\n"
    "v_cvt_pk_bf16_f32 v187, v190, v191\n"
    "v_mul_f32 v192, v192, v12\n"
    "v_mfma_f32_16x16x32_bf16 v[44:47], a[92:95], a[20:23], v[44:47]\n"
    "s_waitcnt lgkmcnt(4)\n"
    "buffer_store_dwordx4 v[184:187], v4, s[52:55], s62 offen offset:64 sc0 sc1\n"
    "ds_read_b128 a[92:95], v1 offset:26112\n"
    "v_mfma_f32_16x16x32_bf16 v[28:31], a[96:99], a[72:75], v[28:31]\n"
    "v_mul_f32 v193, v193, v12\n"
    "v_mul_f32 v194, v194, v12\n"
    "v_mfma_f32_16x16x32_bf16 v[36:39], a[96:99], a[8:11], v[36:39]\n"
    "v_mul_f32 v195, v195, v12\n"
    "v_mul_f32 v196, v196, v12\n"
    "v_mfma_f32_16x16x32_bf16 v[44:47], a[96:99], a[24:27], v[44:47]\n"
    "s_waitcnt lgkmcnt(4)\n"
    "ds_read_b128 a[96:99], v1 offset:28672\n"
    "v_mul_f32 v197, v197, v12\n"
    "v_mfma_f32_16x16x32_bf16 v[28:31], a[100:103], a[76:79], v[28:31]\n"
    "v_mul_f32 v198, v198, v12\n"
    "v_mul_f32 v199, v199, v12\n"
    "v_mfma_f32_16x16x32_bf16 v[36:39], a[100:103], a[12:15], v[36:39]\n"
    "v_cvt_pk_bf16_f32 v192, v192, v193\n"
    "v_cvt_pk_bf16_f32 v193, v194, v195\n"
    "v_mfma_f32_16x16x32_bf16 v[44:47], a[100:103], a[28:31], v[44:47]\n"
    "ds_read_b128 a[100:103], v1 offset:29184\n"
    "v_cvt_pk_bf16_f32 v194, v196, v197\n"
    "v_cvt_pk_bf16_f32 v195, v198, v199\n"
    "v_exp_f32 v60, v60\n"
    "buffer_store_dwordx4 v[192:195], v4, s[52:55], s62 offen offset:128 sc0 sc1\n"
    "v_mul_f32 v200, v200, v12\n"
    "v_add_f32 v9, v9, v60\n"
    "v_mul_f32 v201, v201, v12\n"
    "v_exp_f32 v61, v61\n"
    "v_mul_f32 v202, v202, v12\n"
    "v_add_f32 v9, v9, v61\n"
    "v_cvt_pk_bf16_f32 v58, v60, v61\n"
    "v_mul_f32 v203, v203, v12\n"
    "v_exp_f32 v62, v62\n"
    "v_mul_f32 v204, v204, v12\n"
    "v_add_f32 v9, v9, v62\n"
    "v_mul_f32 v205, v205, v12\n"
    "v_exp_f32 v63, v63\n"
    "v_mul_f32 v206, v206, v12\n"
    "v_add_f32 v9, v9, v63\n"
    "v_mul_f32 v207, v207, v12\n"
    "v_cvt_pk_bf16_f32 v59, v62, v63\n"
    "v_cvt_pk_bf16_f32 v200, v200, v201\n"
    "v_cvt_pk_bf16_f32 v201, v202, v203\n"
    "v_mfma_f32_16x16x32_bf16 v[208:211], a[160:163], v[56:59], v[208:211]\n"
    "v_cvt_pk_bf16_f32 v202, v204, v205\n"
    "v_cvt_pk_bf16_f32 v203, v206, v207\n"
    "v_exp_f32 v68, v68\n"
    "buffer_store_dwordx4 v[200:203], v4, s[52:55], s62 offen offset:192 sc0 sc1\n"
    "v_add_f32 v5, v5, v68\n"
    "v_exp_f32 v69, v69\n"
    "v_exp_f32 v70, v70\n"
    "v_add_f32 v5, v5, v69\n"
    "v_cvt_pk_bf16_f32 v66, v68, v69\n"
    "v_add_f32 v5, v5, v70\n"
    "v_exp_f32 v71, v71\n"
    "v_exp_f32 v24, v24\n"
    "v_add_f32 v5, v5, v71\n"
    "v_cvt_pk_bf16_f32 v67, v70, v71\n"
    "v_add_f32 v9, v9, v24\n"
    "s_nop 0\n"
    "v_mfma_f32_16x16x32_bf16 v[80:83], a[160:163], v[64:67], v[80:83]\n"
    "v_exp_f32 v25, v25\n"
    "v_exp_f32 v26, v26\n"
    "v_add_f32 v9, v9, v25\n"
    "v_cvt_pk_bf16_f32 v24, v24, v25\n"
    "v_add_f32 v9, v9, v26\n"
    "v_exp_f32 v27, v27\n"
    "v_exp_f32 v76, v76\n"
    "v_add_f32 v9, v9, v27\n"
    "v_cvt_pk_bf16_f32 v25, v26, v27\n"
    "v_add_f32 v6, v6, v76\n"
    "v_exp_f32 v77, v77\n"
    "v_exp_f32 v78, v78\n"
    "v_add_f32 v6, v6, v77\n"
    "v_cvt_pk_bf16_f32 v74, v76, v77\n"
    "v_add_f32 v6, v6, v78\n"
    "v_exp_f32 v79, v79\n"
    "v_exp_f32 v32, v32\n"
    "v_add_f32 v6, v6, v79\n"
    "v_cvt_pk_bf16_f32 v75, v78, v79\n"
    "v_add_f32 v5, v5, v32\n"
    "s_nop 0\n"
    "v_mfma_f32_16x16x32_bf16 v[112:115], a[160:163], v[72:75], v[112:115]\n"
    "v_exp_f32 v33, v33\n"
    "v_mfma_f32_16x16x32_bf16 v[212:215], a[164:167], v[56:59], v[212:215]\n"
    "v_add_f32 v5, v5, v33\n"
    "v_cvt_pk_bf16_f32 v32, v32, v33\n"
    "v_mfma_f32_16x16x32_bf16 v[84:87], a[164:167], v[64:67], v[84:87]\n"
    "v_exp_f32 v34, v34\n"
    "v_mfma_f32_16x16x32_bf16 v[116:119], a[164:167], v[72:75], v[116:119]\n"
    "v_add_f32 v5, v5, v34\n"
    "v_mfma_f32_16x16x32_bf16 v[216:219], a[168:171], v[56:59], v[216:219]\n"
    "v_exp_f32 v35, v35\n"
    "v_mfma_f32_16x16x32_bf16 v[88:91], a[168:171], v[64:67], v[88:91]\n"
    "v_add_f32 v5, v5, v35\n"
    "v_cvt_pk_bf16_f32 v33, v34, v35\n"
    "v_mfma_f32_16x16x32_bf16 v[120:123], a[168:171], v[72:75], v[120:123]\n"
    "v_exp_f32 v40, v40\n"
    "v_mfma_f32_16x16x32_bf16 v[220:223], a[172:175], v[56:59], v[220:223]\n"
    "v_add_f32 v6, v6, v40\n"
    "v_mfma_f32_16x16x32_bf16 v[92:95], a[172:175], v[64:67], v[92:95]\n"
    "v_exp_f32 v41, v41\n"
    "v_mfma_f32_16x16x32_bf16 v[124:127], a[172:175], v[72:75], v[124:127]\n"
    "v_add_f32 v6, v6, v41\n"
    "v_cvt_pk_bf16_f32 v40, v40, v41\n"
    "v_mfma_f32_16x16x32_bf16 v[224:227], a[176:179], v[56:59], v[224:227]\n"
    "v_exp_f32 v42, v42\n"
    "v_mfma_f32_16x16x32_bf16 v[96:99], a[176:179], v[64:67], v[96:99]\n"
    "v_add_f32 v6, v6, v42\n"
    "v_mfma_f32_16x16x32_bf16 v[128:131], a[176:179], v[72:75], v[128:131]\n"
    "v_exp_f32 v43, v43\n"
    "v_mfma_f32_16x16x32_bf16 v[228:231], a[180:183], v[56:59], v[228:231]\n"
    "v_add_f32 v6, v6, v43\n"
    "v_cvt_pk_bf16_f32 v41, v42, v43\n"
    "v_mfma_f32_16x16x32_bf16 v[100:103], a[180:183], v[64:67], v[100:103]\n"
    "v_exp_f32 v28, v28\n"
    "v_mfma_f32_16x16x32_bf16 v[132:135], a[180:183], v[72:75], v[132:135]\n"
    "v_add_f32 v9, v9, v28\n"
    "v_mfma_f32_16x16x32_bf16 v[232:235], a[184:187], v[56:59], v[232:235]\n"
    "v_exp_f32 v29, v29\n"
    "v_mfma_f32_16x16x32_bf16 v[104:107], a[184:187], v[64:67], v[104:107]\n"
    "v_add_f32 v9, v9, v29\n"
    "v_cvt_pk_bf16_f32 v26, v28, v29\n"
    "v_mfma_f32_16x16x32_bf16 v[136:139], a[184:187], v[72:75], v[136:139]\n"
    "v_exp_f32 v30, v30\n"
    "v_mfma_f32_16x16x32_bf16 v[236:239], a[188:191], v[56:59], v[236:239]\n"
    "v_add_f32 v9, v9, v30\n"
    "v_mfma_f32_16x16x32_bf16 v[108:111], a[188:191], v[64:67], v[108:111]\n"
    "v_exp_f32 v31, v31\n"
    "v_mfma_f32_16x16x32_bf16 v[140:143], a[188:191], v[72:75], v[140:143]\n"
    "s_waitcnt lgkmcnt(5)\n"
    "v_add_f32 v9, v9, v31\n"
    "v_cvt_pk_bf16_f32 v27, v30, v31\n"
    "v_mfma_f32_16x16x32_bf16 v[64:67], a[80:83], a[0:3], v[248:251]\n"
    "v_exp_f32 v36, v36\n"
    "v_mfma_f32_16x16x32_bf16 v[72:75], a[80:83], a[16:19], v[252:255]\n"
    "s_waitcnt lgkmcnt(4)\n"
    "ds_read_b128 a[80:83], v1 offset:29696\n"
    "v_add_f32 v5, v5, v36\n"
    "v_mfma_f32_16x16x32_bf16 v[64:67], a[84:87], a[4:7], v[64:67]\n"
    "v_exp_f32 v37, v37\n"
    "v_mfma_f32_16x16x32_bf16 v[72:75], a[84:87], a[20:23], v[72:75]\n"
    "s_waitcnt lgkmcnt(4)\n"
    "ds_read_b128 a[84:87], v1 offset:30208\n"
    "v_add_f32 v5, v5, v37\n"
    "v_mfma_f32_16x16x32_bf16 v[64:67], a[88:91], a[8:11], v[64:67]\n"
    "v_cvt_pk_bf16_f32 v34, v36, v37\n"
    "v_mov_b32 v12, v9\n"
    "v_mfma_f32_16x16x32_bf16 v[72:75], a[88:91], a[24:27], v[72:75]\n"
    "s_waitcnt lgkmcnt(4)\n"
    "s_mov_b32 s63, 147456\n"
    "v_exp_f32 v38, v38\n"
    "v_mfma_f32_16x16x32_bf16 v[64:67], a[92:95], a[12:15], v[64:67]\n"
    "ds_read_b128 a[88:91], v1 offset:32768\n"
    "v_add_f32 v5, v5, v38\n"
    "v_mfma_f32_16x16x32_bf16 v[72:75], a[92:95], a[28:31], v[72:75]\n"
    "s_waitcnt lgkmcnt(4)\n"
    "v_exp_f32 v39, v39\n"
    "v_mfma_f32_16x16x32_bf16 v[68:71], a[96:99], a[0:3], v[248:251]\n"
    "ds_read_b128 a[92:95], v1 offset:33280\n"
    "v_add_f32 v5, v5, v39\n"
    "v_mfma_f32_16x16x32_bf16 v[76:79], a[96:99], a[16:19], v[252:255]\n"
    "s_waitcnt lgkmcnt(4)\n"
    "ds_read_b128 a[96:99], v1 offset:33792\n"
    "v_cvt_pk_bf16_f32 v35, v38, v39\n"
    "v_mfma_f32_16x16x32_bf16 v[68:71], a[100:103], a[4:7], v[68:71]\n"
    "v_exp_f32 v64, v64\n"
    "v_mfma_f32_16x16x32_bf16 v[76:79], a[100:103], a[20:23], v[76:79]\n"
    "ds_read_b128 a[100:103], v1 offset:34304\n"
    "v_add_f32 v5, v5, v64\n"
    "v_exp_f32 v65, v65\n"
    "v_exp_f32 v66, v66\n"
    "v_add_f32 v5, v5, v65\n"
    "v_cvt_pk_bf16_f32 v64, v64, v65\n"
    "v_add_f32 v5, v5, v66\n"
    "v_exp_f32 v67, v67\n"
    "v_exp_f32 v44, v44\n"
    "v_add_f32 v5, v5, v67\n"
    "v_cvt_pk_bf16_f32 v65, v66, v67\n"
    "v_add_f32 v6, v6, v44\n"
    "v_exp_f32 v45, v45\n"
    "v_exp_f32 v46, v46\n"
    "s_waitcnt lgkmcnt(5)\n"
    "v_mfma_f32_16x16x32_bf16 v[68:71], a[80:83], a[8:11], v[68:71]\n"
    "v_add_f32 v6, v6, v45\n"
    "v_cvt_pk_bf16_f32 v42, v44, v45\n"
    "v_mfma_f32_16x16x32_bf16 v[76:79], a[80:83], a[24:27], v[76:79]\n"
    "s_waitcnt lgkmcnt(4)\n"
    "ds_read_b128 a[80:83], v1 offset:36864\n"
    "v_add_f32 v6, v6, v46\n"
    "v_mfma_f32_16x16x32_bf16 v[68:71], a[84:87], a[12:15], v[68:71]\n"
    "v_exp_f32 v47, v47\n"
    "v_mfma_f32_16x16x32_bf16 v[76:79], a[84:87], a[28:31], v[76:79]\n"
    "ds_read_b128 a[84:87], v1 offset:37376\n"
    "v_add_f32 v6, v6, v47\n"
    "v_mfma_f32_16x16x32_bf16 v[208:211], a[192:195], v[24:27], v[208:211]\n"
    "v_cvt_pk_bf16_f32 v43, v46, v47\n"
    "v_permlane32_swap_b32 v9, v12\n"
    "v_mfma_f32_16x16x32_bf16 v[80:83], a[192:195], v[32:35], v[80:83]\n"
    "v_exp_f32 v72, v72\n"
    "v_mfma_f32_16x16x32_bf16 v[112:115], a[192:195], v[40:43], v[112:115]\n"
    "v_add_f32 v6, v6, v72\n"
    "v_add_f32 v9, v9, v12\n"
    "v_mfma_f32_16x16x32_bf16 v[212:215], a[196:199], v[24:27], v[212:215]\n"
    "v_exp_f32 v73, v73\n"
    "v_mfma_f32_16x16x32_bf16 v[84:87], a[196:199], v[32:35], v[84:87]\n"
    "v_add_f32 v6, v6, v73\n"
    "v_cvt_pk_bf16_f32 v72, v72, v73\n"
    "v_mfma_f32_16x16x32_bf16 v[116:119], a[196:199], v[40:43], v[116:119]\n"
    "v_exp_f32 v74, v74\n"
    "v_mfma_f32_16x16x32_bf16 v[216:219], a[200:203], v[24:27], v[216:219]\n"
    "v_add_f32 v6, v6, v74\n"
    "v_mov_b32 v12, v9\n"
    "v_mfma_f32_16x16x32_bf16 v[88:91], a[200:203], v[32:35], v[88:91]\n"
    "v_exp_f32 v75, v75\n"
    "v_mfma_f32_16x16x32_bf16 v[120:123], a[200:203], v[40:43], v[120:123]\n"
    "v_add_f32 v6, v6, v75\n"
    "v_cvt_pk_bf16_f32 v73, v74, v75\n"
    "v_mfma_f32_16x16x32_bf16 v[220:223], a[204:207], v[24:27], v[220:223]\n"
    "v_exp_f32 v68, v68\n"
    "v_mfma_f32_16x16x32_bf16 v[92:95], a[204:207], v[32:35], v[92:95]\n"
    "v_add_f32 v5, v5, v68\n"
    "v_permlane16_swap_b32 v9, v12\n"
    "v_mfma_f32_16x16x32_bf16 v[124:127], a[204:207], v[40:43], v[124:127]\n"
    "v_exp_f32 v69, v69\n"
    "v_mfma_f32_16x16x32_bf16 v[224:227], a[208:211], v[24:27], v[224:227]\n"
    "v_add_f32 v5, v5, v69\n"
    "v_cvt_pk_bf16_f32 v66, v68, v69\n"
    "v_mfma_f32_16x16x32_bf16 v[96:99], a[208:211], v[32:35], v[96:99]\n"
    "v_exp_f32 v70, v70\n"
    "v_mfma_f32_16x16x32_bf16 v[128:131], a[208:211], v[40:43], v[128:131]\n"
    "v_add_f32 v5, v5, v70\n"
    "v_add_f32 v12, v9, v12\n"
    "v_mfma_f32_16x16x32_bf16 v[228:231], a[212:215], v[24:27], v[228:231]\n"
    "v_exp_f32 v71, v71\n"
    "v_mfma_f32_16x16x32_bf16 v[100:103], a[212:215], v[32:35], v[100:103]\n"
    "v_add_f32 v5, v5, v71\n"
    "v_cvt_pk_bf16_f32 v67, v70, v71\n"
    "v_mfma_f32_16x16x32_bf16 v[132:135], a[212:215], v[40:43], v[132:135]\n"
    "v_exp_f32 v76, v76\n"
    "v_mfma_f32_16x16x32_bf16 v[232:235], a[216:219], v[24:27], v[232:235]\n"
    "v_add_f32 v6, v6, v76\n"
    "v_cmp_ngt_f32 vcc, 0x5d800000, v12\n"
    "v_mfma_f32_16x16x32_bf16 v[104:107], a[216:219], v[32:35], v[104:107]\n"
    "v_exp_f32 v77, v77\n"
    "v_mfma_f32_16x16x32_bf16 v[136:139], a[216:219], v[40:43], v[136:139]\n"
    "s_or_b64 s[64:65], s[64:65], vcc\n"
    "v_add_f32 v6, v6, v77\n"
    "v_cvt_pk_bf16_f32 v74, v76, v77\n"
    "v_mfma_f32_16x16x32_bf16 v[236:239], a[220:223], v[24:27], v[236:239]\n"
    "v_exp_f32 v78, v78\n"
    "v_mfma_f32_16x16x32_bf16 v[108:111], a[220:223], v[32:35], v[108:111]\n"
    "v_add_f32 v6, v6, v78\n"
    "v_mfma_f32_16x16x32_bf16 v[140:143], a[220:223], v[40:43], v[140:143]\n"
    "s_waitcnt lgkmcnt(5)\n"
    "v_exp_f32 v79, v79\n"
    "v_mfma_f32_16x16x32_bf16 v[32:35], a[88:91], a[0:3], v[248:251]\n"
    "v_add_f32 v6, v6, v79\n"
    "v_cvt_pk_bf16_f32 v75, v78, v79\n"
    "v_mfma_f32_16x16x32_bf16 v[40:43], a[88:91], a[16:19], v[252:255]\n"
    "s_waitcnt lgkmcnt(4)\n"
    "ds_read_b128 a[88:91], v1 offset:37888\n"
    "v_mfma_f32_16x16x32_bf16 v[32:35], a[92:95], a[4:7], v[32:35]\n"
    "v_rcp_f32 v12, v12\n"
    "v_mfma_f32_16x16x32_bf16 v[40:43], a[92:95], a[20:23], v[40:43]\n"
    "s_waitcnt lgkmcnt(4)\n"
    "ds_read_b128 a[92:95], v1 offset:38400\n"
    "v_mul_f32 v208, v208, v12\n"
    "v_mfma_f32_16x16x32_bf16 v[32:35], a[96:99], a[8:11], v[32:35]\n"
    "v_mul_f32 v209, v209, v12\n"
    "v_mul_f32 v210, v210, v12\n"
    "v_mfma_f32_16x16x32_bf16 v[40:43], a[96:99], a[24:27], v[40:43]\n"
    "s_waitcnt lgkmcnt(4)\n"
    "ds_read_b128 a[96:99], v1 offset:40960\n"
    "v_mul_f32 v211, v211, v12\n"
    "v_mfma_f32_16x16x32_bf16 v[32:35], a[100:103], a[12:15], v[32:35]\n"
    "v_mul_f32 v212, v212, v12\n"
    "v_mul_f32 v213, v213, v12\n"
    "v_mfma_f32_16x16x32_bf16 v[40:43], a[100:103], a[28:31], v[40:43]\n"
    "s_waitcnt lgkmcnt(4)\n"
    "ds_read_b128 a[100:103], v1 offset:41472\n"
    "v_mul_f32 v214, v214, v12\n"
    "v_mfma_f32_16x16x32_bf16 v[36:39], a[80:83], a[0:3], v[248:251]\n"
    "v_mul_f32 v215, v215, v12\n"
    "v_cvt_pk_bf16_f32 v208, v208, v209\n"
    "v_mfma_f32_16x16x32_bf16 v[44:47], a[80:83], a[16:19], v[252:255]\n"
    "s_waitcnt lgkmcnt(4)\n"
    "v_exp_f32 v32, v32\n"
    "v_mfma_f32_16x16x32_bf16 v[36:39], a[84:87], a[4:7], v[36:39]\n"
    "ds_read_b128 a[80:83], v1 offset:41984\n"
    "v_add_f32 v5, v5, v32\n"
    "v_mfma_f32_16x16x32_bf16 v[44:47], a[84:87], a[20:23], v[44:47]\n"
    "ds_read_b128 a[84:87], v1 offset:42496\n"
    "v_exp_f32 v33, v33\n"
    "v_exp_f32 v34, v34\n"
    "v_add_f32 v5, v5, v33\n"
    "v_cvt_pk_bf16_f32 v32, v32, v33\n"
    "v_add_f32 v5, v5, v34\n"
    "v_exp_f32 v35, v35\n"
    "v_exp_f32 v40, v40\n"
    "v_add_f32 v5, v5, v35\n"
    "v_cvt_pk_bf16_f32 v33, v34, v35\n"
    "v_add_f32 v6, v6, v40\n"
    "v_exp_f32 v41, v41\n"
    "v_exp_f32 v42, v42\n"
    "v_add_f32 v6, v6, v41\n"
    "s_waitcnt lgkmcnt(5)\n"
    "v_mfma_f32_16x16x32_bf16 v[36:39], a[88:91], a[8:11], v[36:39]\n"
    "v_cvt_pk_bf16_f32 v40, v40, v41\n"
    "v_add_f32 v6, v6, v42\n"
    "v_mfma_f32_16x16x32_bf16 v[44:47], a[88:91], a[24:27], v[44:47]\n"
    "s_waitcnt lgkmcnt(4)\n"
    "v_exp_f32 v43, v43\n"
    "v_mfma_f32_16x16x32_bf16 v[36:39], a[92:95], a[12:15], v[36:39]\n"
    "ds_read_b128 a[88:91], v1 offset:45056\n"
    "v_add_f32 v6, v6, v43\n"
    "v_mfma_f32_16x16x32_bf16 v[44:47], a[92:95], a[28:31], v[44:47]\n"
    "ds_read_b128 a[92:95], v1 offset:45568\n"
    "v_cvt_pk_bf16_f32 v41, v42, v43\n"
    "v_mfma_f32_16x16x32_bf16 v[80:83], a[224:227], v[64:67], v[80:83]\n"
    "v_cvt_pk_bf16_f32 v209, v210, v211\n"
    "v_cvt_pk_bf16_f32 v210, v212, v213\n"
    "v_mfma_f32_16x16x32_bf16 v[112:115], a[224:227], v[72:75], v[112:115]\n"
    "v_exp_f32 v36, v36\n"
    "v_mfma_f32_16x16x32_bf16 v[84:87], a[228:231], v[64:67], v[84:87]\n"
    "v_add_f32 v5, v5, v36\n"
    "v_cvt_pk_bf16_f32 v211, v214, v215\n"
    "v_mfma_f32_16x16x32_bf16 v[116:119], a[228:231], v[72:75], v[116:119]\n"
    "v_exp_f32 v37, v37\n"
    "v_mfma_f32_16x16x32_bf16 v[88:91], a[232:235], v[64:67], v[88:91]\n"
    "buffer_store_dwordx4 v[208:211], v4, s[52:55], s63 offen offset:0 sc0 sc1\n"
    "v_add_f32 v5, v5, v37\n"
    "v_mfma_f32_16x16x32_bf16 v[120:123], a[232:235], v[72:75], v[120:123]\n"
    "v_cvt_pk_bf16_f32 v34, v36, v37\n"
    "v_mul_f32 v216, v216, v12\n"
    "v_mfma_f32_16x16x32_bf16 v[92:95], a[236:239], v[64:67], v[92:95]\n"
    "v_exp_f32 v38, v38\n"
    "v_mfma_f32_16x16x32_bf16 v[124:127], a[236:239], v[72:75], v[124:127]\n"
    "v_add_f32 v5, v5, v38\n"
    "v_mul_f32 v217, v217, v12\n"
    "v_mfma_f32_16x16x32_bf16 v[96:99], a[240:243], v[64:67], v[96:99]\n"
    "v_exp_f32 v39, v39\n"
    "v_mfma_f32_16x16x32_bf16 v[128:131], a[240:243], v[72:75], v[128:131]\n"
    "v_add_f32 v5, v5, v39\n"
    "v_cvt_pk_bf16_f32 v35, v38, v39\n"
    "v_mfma_f32_16x16x32_bf16 v[100:103], a[244:247], v[64:67], v[100:103]\n"
    "v_exp_f32 v44, v44\n"
    "v_mfma_f32_16x16x32_bf16 v[132:135], a[244:247], v[72:75], v[132:135]\n"
    "v_add_f32 v6, v6, v44\n"
    "v_mul_f32 v218, v218, v12\n"
    "v_mfma_f32_16x16x32_bf16 v[104:107], a[248:251], v[64:67], v[104:107]\n"
    "v_exp_f32 v45, v45\n"
    "v_mfma_f32_16x16x32_bf16 v[136:139], a[248:251], v[72:75], v[136:139]\n"
    "v_add_f32 v6, v6, v45\n"
    "v_cvt_pk_bf16_f32 v42, v44, v45\n"
    "v_mfma_f32_16x16x32_bf16 v[108:111], a[252:255], v[64:67], v[108:111]\n"
    "v_exp_f32 v46, v46\n"
    "v_mfma_f32_16x16x32_bf16 v[140:143], a[252:255], v[72:75], v[140:143]\n"
    "s_waitcnt lgkmcnt(5)\n"
    "v_add_f32 v6, v6, v46\n"
    "v_mul_f32 v219, v219, v12\n"
    "v_mfma_f32_16x16x32_bf16 v[64:67], a[96:99], a[0:3], v[248:251]\n"
    "v_exp_f32 v47, v47\n"
    "v_mfma_f32_16x16x32_bf16 v[72:75], a[96:99], a[16:19], v[252:255]\n"
    "s_waitcnt lgkmcnt(4)\n"
    "ds_read_b128 a[96:99], v1 offset:46080\n"
    "v_add_f32 v6, v6, v47\n"
    "v_mfma_f32_16x16x32_bf16 v[64:67], a[100:103], a[4:7], v[64:67]\n"
    "v_cvt_pk_bf16_f32 v43, v46, v47\n"
    "v_mul_f32 v220, v220, v12\n"
    "v_mfma_f32_16x16x32_bf16 v[72:75], a[100:103], a[20:23], v[72:75]\n"
    "s_waitcnt lgkmcnt(4)\n"
    "ds_read_b128 a[100:103], v1 offset:46592\n"
    "v_mul_f32 v221, v221, v12\n"
    "v_mfma_f32_16x16x32_bf16 v[64:67], a[80:83], a[8:11], v[64:67]\n"
    "v_mul_f32 v222, v222, v12\n"
    "v_mul_f32 v223, v223, v12\n"
    "v_mfma_f32_16x16x32_bf16 v[72:75], a[80:83], a[24:27], v[72:75]\n"
    "s_waitcnt lgkmcnt(4)\n"
    "ds_read_b128 a[80:83], v1 offset:49152\n"
    "v_cvt_pk_bf16_f32 v216, v216, v217\n"
    "v_mfma_f32_16x16x32_bf16 v[64:67], a[84:87], a[12:15], v[64:67]\n"
    "v_cvt_pk_bf16_f32 v217, v218, v219\n"
    "v_cvt_pk_bf16_f32 v218, v220, v221\n"
    "v_mfma_f32_16x16x32_bf16 v[72:75], a[84:87], a[28:31], v[72:75]\n"
    "s_waitcnt lgkmcnt(4)\n"
    "ds_read_b128 a[84:87], v1 offset:49664\n"
    "v_cvt_pk_bf16_f32 v219, v222, v223\n"
    "v_mfma_f32_16x16x32_bf16 v[68:71], a[88:91], a[0:3], v[248:251]\n"
    "buffer_store_dwordx4 v[216:219], v4, s[52:55], s63 offen offset:64 sc0 sc1\n"
    "v_mul_f32 v224, v224, v12\n"
    "v_mfma_f32_16x16x32_bf16 v[76:79], a[88:91], a[16:19], v[252:255]\n"
    "s_waitcnt lgkmcnt(4)\n"
    "v_exp_f32 v64, v64\n"
    "v_mfma_f32_16x16x32_bf16 v[68:71], a[92:95], a[4:7], v[68:71]\n"
    "ds_read_b128 a[88:91], v1 offset:50176\n"
    "v_add_f32 v5, v5, v64\n"
    "v_mfma_f32_16x16x32_bf16 v[76:79], a[92:95], a[20:23], v[76:79]\n"
    "ds_read_b128 a[92:95], v1 offset:50688\n"
    "v_exp_f32 v65, v65\n"
    "v_exp_f32 v66, v66\n"
    "v_add_f32 v5, v5, v65\n"
    "v_cvt_pk_bf16_f32 v64, v64, v65\n"
    "v_add_f32 v5, v5, v66\n"
    "v_exp_f32 v67, v67\n"
    "v_exp_f32 v72, v72\n"
    "v_add_f32 v5, v5, v67\n"
    "v_cvt_pk_bf16_f32 v65, v66, v67\n"
    "v_add_f32 v6, v6, v72\n"
    "v_exp_f32 v73, v73\n"
    "v_exp_f32 v74, v74\n"
    "v_add_f32 v6, v6, v73\n"
    "s_waitcnt lgkmcnt(5)\n"
    "v_mfma_f32_16x16x32_bf16 v[68:71], a[96:99], a[8:11], v[68:71]\n"
    "v_cvt_pk_bf16_f32 v72, v72, v73\n"
    "v_add_f32 v6, v6, v74\n"
    "v_mfma_f32_16x16x32_bf16 v[76:79], a[96:99], a[24:27], v[76:79]\n"
    "s_waitcnt lgkmcnt(4)\n"
    "v_exp_f32 v75, v75\n"
    "v_mfma_f32_16x16x32_bf16 v[68:71], a[100:103], a[12:15], v[68:71]\n"
    "ds_read_b128 a[96:99], v1 offset:53248\n"
    "v_add_f32 v6, v6, v75\n"
    "v_mfma_f32_16x16x32_bf16 v[76:79], a[100:103], a[28:31], v[76:79]\n"
    "ds_read_b128 a[100:103], v1 offset:53760\n"
    "v_cvt_pk_bf16_f32 v73, v74, v75\n"
    "v_mfma_f32_16x16x32_bf16 v[80:83], a[104:107], v[32:35], v[80:83]\n"
    "v_mul_f32 v225, v225, v12\n"
    "v_mul_f32 v226, v226, v12\n"
    "v_mfma_f32_16x16x32_bf16 v[112:115], a[104:107], v[40:43], v[112:115]\n"
    "ds_read_b64_tr_b16 a[104:105], v3 offset:1536\n"
    "ds_read_b64_tr_b16 a[106:107], v3 offset:5632\n"
    "v_mfma_f32_16x16x32_bf16 v[84:87], a[108:111], v[32:35], v[84:87]\n"
    "v_mul_f32 v227, v227, v12\n"
    "v_mul_f32 v228, v228, v12\n"
    "v_mfma_f32_16x16x32_bf16 v[116:119], a[108:111], v[40:43], v[116:119]\n"
    "ds_read_b64_tr_b16 a[108:109], v3 offset:1544\n"
    "ds_read_b64_tr_b16 a[110:111], v3 offset:5640\n"
    "v_mfma_f32_16x16x32_bf16 v[88:91], a[112:115], v[32:35], v[88:91]\n"
    "v_mul_f32 v229, v229, v12\n"
    "v_mul_f32 v230, v230, v12\n"
    "v_mfma_f32_16x16x32_bf16 v[120:123], a[112:115], v[40:43], v[120:123]\n"
    "ds_read_b64_tr_b16 a[112:113], v3 offset:8192\n"
    "ds_read_b64_tr_b16 a[114:115], v3 offset:12288\n"
    "v_mfma_f32_16x16x32_bf16 v[92:95], a[116:119], v[32:35], v[92:95]\n"
    "v_mul_f32 v231, v231, v12\n"
    "v_cvt_pk_bf16_f32 v224, v224, v225\n"
    "v_mfma_f32_16x16x32_bf16 v[124:127], a[116:119], v[40:43], v[124:127]\n"
    "ds_read_b64_tr_b16 a[116:117], v3 offset:8200\n"
    "ds_read_b64_tr_b16 a[118:119], v3 offset:12296\n"
    "v_mfma_f32_16x16x32_bf16 v[96:99], a[120:123], v[32:35], v[96:99]\n"
    "v_cvt_pk_bf16_f32 v225, v226, v227\n"
    "v_cvt_pk_bf16_f32 v226, v228, v229\n"
    "v_mfma_f32_16x16x32_bf16 v[128:131], a[120:123], v[40:43], v[128:131]\n"
    "ds_read_b64_tr_b16 a[120:121], v3 offset:8704\n"
    "ds_read_b64_tr_b16 a[122:123], v3 offset:12800\n"
    "v_mfma_f32_16x16x32_bf16 v[100:103], a[124:127], v[32:35], v[100:103]\n"
    "v_cvt_pk_bf16_f32 v227, v230, v231\n"
    "v_mul_f32 v232, v232, v12\n"
    "v_mfma_f32_16x16x32_bf16 v[132:135], a[124:127], v[40:43], v[132:135]\n"
    "buffer_store_dwordx4 v[224:227], v4, s[52:55], s63 offen offset:128 sc0 sc1\n"
    "ds_read_b64_tr_b16 a[124:125], v3 offset:8712\n"
    "ds_read_b64_tr_b16 a[126:127], v3 offset:12808\n"
    "v_exp_f32 v68, v68\n"
    "v_mul_f32 v233, v233, v12\n"
    "v_add_f32 v5, v5, v68\n"
    "v_mul_f32 v234, v234, v12\n"
    "v_exp_f32 v69, v69\n"
    "v_mul_f32 v235, v235, v12\n"
    "v_add_f32 v5, v5, v69\n"
    "v_cvt_pk_bf16_f32 v66, v68, v69\n"
    "v_mul_f32 v236, v236, v12\n"
    "v_exp_f32 v70, v70\n"
    "v_mul_f32 v237, v237, v12\n"
    "v_add_f32 v5, v5, v70\n"
    "v_mul_f32 v238, v238, v12\n"
    "v_exp_f32 v71, v71\n"
    "s_waitcnt lgkmcnt(10)\n"
    "v_mfma_f32_16x16x32_bf16 v[104:107], a[104:107], v[32:35], v[104:107]\n"
    "v_add_f32 v5, v5, v71\n"
    "v_mul_f32 v239, v239, v12\n"
    "v_mfma_f32_16x16x32_bf16 v[136:139], a[104:107], v[40:43], v[136:139]\n"
    "s_waitcnt lgkmcnt(8)\n"
    "ds_read_b64_tr_b16 a[104:105], v3 offset:9216\n"
    "ds_read_b64_tr_b16 a[106:107], v3 offset:13312\n"
    "v_mfma_f32_16x16x32_bf16 v[108:111], a[108:111], v[32:35], v[108:111]\n"
    "v_cvt_pk_bf16_f32 v67, v70, v71\n"
    "v_cvt_pk_bf16_f32 v232, v232, v233\n"
    "v_mfma_f32_16x16x32_bf16 v[140:143], a[108:111], v[40:43], v[140:143]\n"
    "ds_read_b64_tr_b16 a[108:109], v3 offset:9224\n"
    "ds_read_b64_tr_b16 a[110:111], v3 offset:13320\n"
    "v_mfma_f32_16x16x32_bf16 v[40:43], a[80:83], a[16:19], v[252:255]\n"
    "ds_read_b128 a[80:83], v1 offset:54272\n"
    "v_cvt_pk_bf16_f32 v233, v234, v235\n"
    "v_mfma_f32_16x16x32_bf16 v[40:43], a[84:87], a[20:23], v[40:43]\n"
    "ds_read_b128 a[84:87], v1 offset:54784\n"
    "v_cvt_pk_bf16_f32 v234, v236, v237\n"
    "v_mfma_f32_16x16x32_bf16 v[40:43], a[88:91], a[24:27], v[40:43]\n"
    "ds_read_b128 a[88:91], v1 offset:57344\n"
    "v_cvt_pk_bf16_f32 v235, v238, v239\n"
    "v_mfma_f32_16x16x32_bf16 v[40:43], a[92:95], a[28:31], v[40:43]\n"
    "buffer_store_dwordx4 v[232:235], v4, s[52:55], s63 offen offset:192 sc0 sc1\n"
    "ds_read_b128 a[92:95], v1 offset:57856\n"
    "v_mfma_f32_16x16x32_bf16 v[44:47], a[96:99], a[16:19], v[252:255]\n"
    "v_exp_f32 v76, v76\n"
    "v_mfma_f32_16x16x32_bf16 v[44:47], a[100:103], a[20:23], v[44:47]\n"
    "ds_read_b128 a[96:99], v1 offset:58368\n"
    "ds_read_b128 a[100:103], v1 offset:58880\n"
    "v_add_f32 v6, v6, v76\n"
    "v_exp_f32 v77, v77\n"
    "v_exp_f32 v40, v40\n"
    "v_exp_f32 v41, v41\n"
    "v_exp_f32 v42, v42\n"
    "v_exp_f32 v43, v43\n"
    "v_add_f32 v6, v6, v77\n"
    "v_cvt_pk_bf16_f32 v74, v76, v77\n"
    "v_exp_f32 v78, v78\n"
    "v_exp_f32 v79, v79\n"
    "v_add_f32 v6, v6, v78\n"
    "v_add_f32 v6, v6, v79\n"
    "v_add_f32 v6, v6, v40\n"
    "v_add_f32 v6, v6, v41\n"
    "v_cvt_pk_bf16_f32 v40, v40, v41\n"
    "v_add_f32 v6, v6, v42\n"
    "v_add_f32 v6, v6, v43\n"
    "v_cvt_pk_bf16_f32 v41, v42, v43\n"
    "v_cvt_pk_bf16_f32 v75, v78, v79\n"
    "v_mov_b32 v12, v5\n"
    "s_nop 1\n"
    "v_permlane32_swap_b32 v5, v12\n"
    "v_add_f32 v5, v5, v12\n"
    "v_mov_b32 v12, v5\n"
    "s_nop 1\n"
    "v_permlane16_swap_b32 v5, v12\n"
    "v_add_f32 v12, v5, v12\n"
    "s_waitcnt lgkmcnt(5)\n"
    "v_mfma_f32_16x16x32_bf16 v[44:47], a[80:83], a[24:27], v[44:47]\n"
    "s_waitcnt lgkmcnt(4)\n"
    "s_mov_b32 s62, 163840\n"
    "ds_read_b128 a[80:83], v1 offset:61440\n"
    "v_cmp_ngt_f32 vcc, 0x5d800000, v12\n"
    "v_mfma_f32_16x16x32_bf16 v[44:47], a[84:87], a[28:31], v[44:47]\n"
    "ds_read_b128 a[84:87], v1 offset:61952\n"
    "v_mfma_f32_16x16x32_bf16 v[80:83], a[112:115], v[64:67], v[80:83]\n"
    "s_or_b64 s[64:65], s[64:65], vcc\n"
    "v_rcp_f32 v12, v12\n"
    "v_mfma_f32_16x16x32_bf16 v[112:115], a[112:115], v[72:75], v[112:115]\n"
    "ds_read_b64_tr_b16 a[112:113], v3 offset:9728\n"
    "ds_read_b64_tr_b16 a[114:115], v3 offset:13824\n"
    "v_mfma_f32_16x16x32_bf16 v[84:87], a[116:119], v[64:67], v[84:87]\n"
    "v_exp_f32 v44, v44\n"
    "v_mfma_f32_16x16x32_bf16 v[116:119], a[116:119], v[72:75], v[116:119]\n"
    "ds_read_b64_tr_b16 a[116:117], v3 offset:9736\n"
    "ds_read_b64_tr_b16 a[118:119], v3 offset:13832\n"
    "v_mfma_f32_16x16x32_bf16 v[88:91], a[120:123], v[64:67], v[88:91]\n"
    "v_add_f32 v6, v6, v44\n"
    "v_mul_f32 v80, v80, v12\n"
    "v_mfma_f32_16x16x32_bf16 v[120:123], a[120:123], v[72:75], v[120:123]\n"
    "v_exp_f32 v45, v45\n"
    "v_mfma_f32_16x16x32_bf16 v[92:95], a[124:127], v[64:67], v[92:95]\n"
    "ds_read_b64_tr_b16 a[120:121], v3 offset:16384\n"
    "ds_read_b64_tr_b16 a[122:123], v3 offset:20480\n"
    "v_mfma_f32_16x16x32_bf16 v[124:127], a[124:127], v[72:75], v[124:127]\n"
    "ds_read_b64_tr_b16 a[124:125], v3 offset:16392\n"
    "ds_read_b64_tr_b16 a[126:127], v3 offset:20488\n"
    "v_mfma_f32_16x16x32_bf16 v[96:99], a[104:107], v[64:67], v[96:99]\n"
    "v_add_f32 v6, v6, v45\n"
    "v_cvt_pk_bf16_f32 v42, v44, v45\n"
    "v_mfma_f32_16x16x32_bf16 v[128:131], a[104:107], v[72:75], v[128:131]\n"
    "v_exp_f32 v46, v46\n"
    "v_mfma_f32_16x16x32_bf16 v[100:103], a[108:111], v[64:67], v[100:103]\n"
    "ds_read_b64_tr_b16 a[104:105], v3 offset:16896\n"
    "ds_read_b64_tr_b16 a[106:107], v3 offset:20992\n"
    "v_mfma_f32_16x16x32_bf16 v[132:135], a[108:111], v[72:75], v[132:135]\n"
    "ds_read_b64_tr_b16 a[108:109], v3 offset:16904\n"
    "ds_read_b64_tr_b16 a[110:111], v3 offset:21000\n"
    "v_add_f32 v6, v6, v46\n"
    "v_exp_f32 v47, v47\n"
    "v_mul_f32 v81, v81, v12\n"
    "v_add_f32 v6, v6, v47\n"
    "v_cvt_pk_bf16_f32 v43, v46, v47\n"
    "v_mul_f32 v82, v82, v12\n"
    "v_mul_f32 v83, v83, v12\n"
    "v_mul_f32 v84, v84, v12\n"
    "v_mul_f32 v85, v85, v12\n"
    "v_mul_f32 v86, v86, v12\n"
    "v_mul_f32 v87, v87, v12\n"
    "v_cvt_pk_bf16_f32 v80, v80, v81\n"
    "v_cvt_pk_bf16_f32 v81, v82, v83\n"
    "v_cvt_pk_bf16_f32 v82, v84, v85\n"
    "v_cvt_pk_bf16_f32 v83, v86, v87\n"
    "v_mul_f32 v88, v88, v12\n"
    "v_mul_f32 v89, v89, v12\n"
    "v_mul_f32 v90, v90, v12\n"
    "s_waitcnt lgkmcnt(10)\n"
    "v_mfma_f32_16x16x32_bf16 v[104:107], a[112:115], v[64:67], v[104:107]\n"
    "buffer_store_dwordx4 v[80:83], v4, s[52:55], s62 offen offset:0 sc0 sc1\n"
    "v_mul_f32 v91, v91, v12\n"
    "v_mfma_f32_16x16x32_bf16 v[136:139], a[112:115], v[72:75], v[136:139]\n"
    "s_waitcnt lgkmcnt(8)\n"
    "ds_read_b64_tr_b16 a[112:113], v3 offset:17408\n"
    "ds_read_b64_tr_b16 a[114:115], v3 offset:21504\n"
    "v_mfma_f32_16x16x32_bf16 v[108:111], a[116:119], v[64:67], v[108:111]\n"
    "v_mul_f32 v92, v92, v12\n"
    "v_mul_f32 v93, v93, v12\n"
    "v_mfma_f32_16x16x32_bf16 v[140:143], a[116:119], v[72:75], v[140:143]\n"
    "ds_read_b64_tr_b16 a[116:117], v3 offset:17416\n"
    "ds_read_b64_tr_b16 a[118:119], v3 offset:21512\n"
    "v_mfma_f32_16x16x32_bf16 v[72:75], a[88:91], a[16:19], v[252:255]\n"
    "ds_read_b128 a[88:91], v1 offset:62464\n"
    "v_mul_f32 v94, v94, v12\n"
    "v_mfma_f32_16x16x32_bf16 v[72:75], a[92:95], a[20:23], v[72:75]\n"
    "ds_read_b128 a[92:95], v1 offset:62976\n"
    "v_mul_f32 v95, v95, v12\n"
    "v_mfma_f32_16x16x32_bf16 v[72:75], a[96:99], a[24:27], v[72:75]\n"
    "ds_read_b128 a[96:99], v2 offset:0\n"
    "v_cvt_pk_bf16_f32 v88, v88, v89\n"
    "v_mfma_f32_16x16x32_bf16 v[72:75], a[100:103], a[28:31], v[72:75]\n"
    "ds_read_b128 a[100:103], v2 offset:512\n"
    "v_cvt_pk_bf16_f32 v89, v90, v91\n"
    "v_mfma_f32_16x16x32_bf16 v[76:79], a[80:83], a[16:19], v[252:255]\n"
    "ds_read_b128 a[80:83], v2 offset:1024\n"
    "v_cvt_pk_bf16_f32 v90, v92, v93\n"
    "v_mfma_f32_16x16x32_bf16 v[76:79], a[84:87], a[20:23], v[76:79]\n"
    "ds_read_b128 a[84:87], v2 offset:1536\n"
    "v_cvt_pk_bf16_f32 v91, v94, v95\n"
    "v_mul_f32 v96, v96, v12\n"
    "buffer_store_dwordx4 v[88:91], v4, s[52:55], s62 offen offset:64 sc0 sc1\n"
    "v_exp_f32 v72, v72\n"
    "v_exp_f32 v73, v73\n"
    "v_add_f32 v6, v6, v72\n"
    "v_add_f32 v6, v6, v73\n"
    "v_cvt_pk_bf16_f32 v72, v72, v73\n"
    "v_exp_f32 v74, v74\n"
    "v_exp_f32 v75, v75\n"
    "v_add_f32 v6, v6, v74\n"
    "v_add_f32 v6, v6, v75\n"
    "v_cvt_pk_bf16_f32 v73, v74, v75\n"
    "v_mul_f32 v97, v97, v12\n"
    "v_mul_f32 v98, v98, v12\n"
    "v_mul_f32 v99, v99, v12\n"
    "v_mul_f32 v100, v100, v12\n"
    "v_mul_f32 v101, v101, v12\n"
    "v_mul_f32 v102, v102, v12\n"
    "v_mul_f32 v103, v103, v12\n"
    "v_cvt_pk_bf16_f32 v96, v96, v97\n"
    "v_cvt_pk_bf16_f32 v97, v98, v99\n"
    "v_cvt_pk_bf16_f32 v98, v100, v101\n"
    "v_cvt_pk_bf16_f32 v99, v102, v103\n"
    "v_mul_f32 v104, v104, v12\n"
    "buffer_store_dwordx4 v[96:99], v4, s[52:55], s62 offen offset:128 sc0 sc1\n"
    "v_mul_f32 v105, v105, v12\n"
    "v_mul_f32 v106, v106, v12\n"
    "v_mul_f32 v107, v107, v12\n"
    "v_mul_f32 v108, v108, v12\n"
    "v_mul_f32 v109, v109, v12\n"
    "s_waitcnt lgkmcnt(5)\n"
    "v_mfma_f32_16x16x32_bf16 v[76:79], a[88:91], a[24:27], v[76:79]\n"
    "s_waitcnt lgkmcnt(4)\n"
    "ds_read_b128 a[88:91], v2 offset:4096\n"
    "v_mul_f32 v110, v110, v12\n"
    "v_mfma_f32_16x16x32_bf16 v[76:79], a[92:95], a[28:31], v[76:79]\n"
    "ds_read_b128 a[92:95], v2 offset:4608\n"
    "v_mul_f32 v111, v111, v12\n"
    "v_mfma_f32_16x16x32_bf16 v[112:115], a[120:123], v[40:43], v[112:115]\n"
    "ds_read_b64_tr_b16 a[120:121], v3 offset:17920\n"
    "ds_read_b64_tr_b16 a[122:123], v3 offset:22016\n"
    "v_mfma_f32_16x16x32_bf16 v[116:119], a[124:127], v[40:43], v[116:119]\n"
    "ds_read_b64_tr_b16 a[124:125], v3 offset:17928\n"
    "ds_read_b64_tr_b16 a[126:127], v3 offset:22024\n"
    "v_mfma_f32_16x16x32_bf16 v[120:123], a[104:107], v[40:43], v[120:123]\n"
    "v_exp_f32 v76, v76\n"
    "v_mfma_f32_16x16x32_bf16 v[124:127], a[108:111], v[40:43], v[124:127]\n"
    "ds_read_b64_tr_b16 a[104:105], v3 offset:24576\n"
    "ds_read_b64_tr_b16 a[106:107], v3 offset:28672\n"
    "v_mfma_f32_16x16x32_bf16 v[128:131], a[112:115], v[40:43], v[128:131]\n"
    "ds_read_b64_tr_b16 a[108:109], v3 offset:24584\n"
    "ds_read_b64_tr_b16 a[110:111], v3 offset:28680\n"
    "v_mfma_f32_16x16x32_bf16 v[132:135], a[116:119], v[40:43], v[132:135]\n"
    "ds_read_b64_tr_b16 a[112:113], v3 offset:25088\n"
    "ds_read_b64_tr_b16 a[114:115], v3 offset:29184\n"
    "ds_read_b64_tr_b16 a[116:117], v3 offset:25096\n"
    "ds_read_b64_tr_b16 a[118:119], v3 offset:29192\n"
    "v_add_f32 v6, v6, v76\n"
    "v_exp_f32 v77, v77\n"
    "v_exp_f32 v78, v78\n"
    "v_add_f32 v6, v6, v77\n"
    "v_cvt_pk_bf16_f32 v74, v76, v77\n"
    "v_add_f32 v6, v6, v78\n"
    "v_exp_f32 v79, v79\n"
    "v_cvt_pk_bf16_f32 v104, v104, v105\n"
    "v_add_f32 v6, v6, v79\n"
    "v_cvt_pk_bf16_f32 v75, v78, v79\n"
    "v_cvt_pk_bf16_f32 v105, v106, v107\n"
    "v_cvt_pk_bf16_f32 v106, v108, v109\n"
    "v_cvt_pk_bf16_f32 v107, v110, v111\n"
    "buffer_store_dwordx4 v[104:107], v4, s[52:55], s62 offen offset:192 sc0 sc1\n"
    "s_waitcnt lgkmcnt(10)\n"
    "v_mfma_f32_16x16x32_bf16 v[136:139], a[120:123], v[40:43], v[136:139]\n"
    "s_waitcnt lgkmcnt(8)\n"
    "ds_read_b64_tr_b16 a[120:121], v3 offset:25600\n"
    "ds_read_b64_tr_b16 a[122:123], v3 offset:29696\n"
    "v_mfma_f32_16x16x32_bf16 v[140:143], a[124:127], v[40:43], v[140:143]\n"
    "ds_read_b64_tr_b16 a[124:125], v3 offset:25608\n"
    "ds_read_b64_tr_b16 a[126:127], v3 offset:29704\n"
    "v_mfma_f32_16x16x32_bf16 v[40:43], a[96:99], a[16:19], v[252:255]\n"
    "ds_read_b128 a[96:99], v2 offset:5120\n"
    "v_mfma_f32_16x16x32_bf16 v[40:43], a[100:103], a[20:23], v[40:43]\n"
    "ds_read_b128 a[100:103], v2 offset:5632\n"
    "v_mfma_f32_16x16x32_bf16 v[40:43], a[80:83], a[24:27], v[40:43]\n"
    "v_mfma_f32_16x16x32_bf16 v[40:43], a[84:87], a[28:31], v[40:43]\n"
    "v_mfma_f32_16x16x32_bf16 v[44:47], a[88:91], a[16:19], v[252:255]\n"
    "v_mfma_f32_16x16x32_bf16 v[44:47], a[92:95], a[20:23], v[44:47]\n"
    "s_nop 5\n"
    "v_exp_f32 v40, v40\n"
    "v_exp_f32 v41, v41\n"
    "v_add_f32 v6, v6, v40\n"
    "v_add_f32 v6, v6, v41\n"
    "v_cvt_pk_bf16_f32 v40, v40, v41\n"
    "v_exp_f32 v42, v42\n"
    "v_exp_f32 v43, v43\n"
    "v_add_f32 v6, v6, v42\n"
    "v_add_f32 v6, v6, v43\n"
    "v_cvt_pk_bf16_f32 v41, v42, v43\n"
    "s_waitcnt lgkmcnt(1)\n"
    "v_mfma_f32_16x16x32_bf16 v[44:47], a[96:99], a[24:27], v[44:47]\n"
    "s_waitcnt lgkmcnt(0)\n"
    "v_mfma_f32_16x16x32_bf16 v[44:47], a[100:103], a[28:31], v[44:47]\n"
    "v_mfma_f32_16x16x32_bf16 v[112:115], a[104:107], v[72:75], v[112:115]\n"
    "ds_read_b64_tr_b16 a[104:105], v3 offset:26112\n"
    "ds_read_b64_tr_b16 a[106:107], v3 offset:30208\n"
    "v_mfma_f32_16x16x32_bf16 v[116:119], a[108:111], v[72:75], v[116:119]\n"
    "ds_read_b64_tr_b16 a[108:109], v3 offset:26120\n"
    "ds_read_b64_tr_b16 a[110:111], v3 offset:30216\n"
    "v_mfma_f32_16x16x32_bf16 v[120:123], a[112:115], v[72:75], v[120:123]\n"
    "s_nop 0\n"
    "v_exp_f32 v44, v44\n"
    "v_mfma_f32_16x16x32_bf16 v[124:127], a[116:119], v[72:75], v[124:127]\n"
    "ds_read_b64_tr_b16 a[112:113], v3 offset:32768\n"
    "ds_read_b64_tr_b16 a[114:115], v3 offset:36864\n"
    "v_mfma_f32_16x16x32_bf16 v[128:131], a[120:123], v[72:75], v[128:131]\n"
    "ds_read_b64_tr_b16 a[116:117], v3 offset:32776\n"
    "ds_read_b64_tr_b16 a[118:119], v3 offset:36872\n"
    "v_mfma_f32_16x16x32_bf16 v[132:135], a[124:127], v[72:75], v[132:135]\n"
    "ds_read_b64_tr_b16 a[120:121], v3 offset:33280\n"
    "ds_read_b64_tr_b16 a[122:123], v3 offset:37376\n"
    "ds_read_b64_tr_b16 a[124:125], v3 offset:33288\n"
    "ds_read_b64_tr_b16 a[126:127], v3 offset:37384\n"
    "v_add_f32 v6, v6, v44\n"
    "v_exp_f32 v45, v45\n"
    "v_exp_f32 v46, v46\n"
    "v_add_f32 v6, v6, v45\n"
    "v_cvt_pk_bf16_f32 v42, v44, v45\n"
    "v_add_f32 v6, v6, v46\n"
    "v_exp_f32 v47, v47\n"
    "s_nop 0\n"
    "v_add_f32 v6, v6, v47\n"
    "v_cvt_pk_bf16_f32 v43, v46, v47\n"
    "v_mov_b32 v12, v6\n"
    "s_nop 1\n"
    "v_permlane32_swap_b32 v6, v12\n"
    "v_add_f32 v6, v6, v12\n"
    "v_mov_b32 v12, v6\n"
    "s_nop 1\n"
    "v_permlane16_swap_b32 v6, v12\n"
    "v_add_f32 v12, v6, v12\n"
    "v_cmp_ngt_f32 vcc, 0x5d800000, v12\n"
    "v_rcp_f32 v12, v12\n"
    "s_waitcnt lgkmcnt(10)\n"
    "v_mfma_f32_16x16x32_bf16 v[136:139], a[104:107], v[72:75], v[136:139]\n"
    "s_waitcnt lgkmcnt(8)\n"
    "s_or_b64 s[64:65], s[64:65], vcc\n"
    "ds_read_b64_tr_b16 a[104:105], v3 offset:33792\n"
    "ds_read_b64_tr_b16 a[106:107], v3 offset:37888\n"
    "v_mfma_f32_16x16x32_bf16 v[140:143], a[108:111], v[72:75], v[140:143]\n"
    "s_waitcnt lgkmcnt(8)\n"
    "s_mov_b32 s63, 180224\n"
    "ds_read_b64_tr_b16 a[108:109], v3 offset:33800\n"
    "ds_read_b64_tr_b16 a[110:111], v3 offset:37896\n"
    "v_mfma_f32_16x16x32_bf16 v[112:115], a[112:115], v[40:43], v[112:115]\n"
    "s_waitcnt lgkmcnt(8)\n"
    "ds_read_b64_tr_b16 a[112:113], v3 offset:34304\n"
    "ds_read_b64_tr_b16 a[114:115], v3 offset:38400\n"
    "v_mfma_f32_16x16x32_bf16 v[116:119], a[116:119], v[40:43], v[116:119]\n"
    "s_waitcnt lgkmcnt(8)\n"
    "ds_read_b64_tr_b16 a[116:117], v3 offset:34312\n"
    "ds_read_b64_tr_b16 a[118:119], v3 offset:38408\n"
    "v_mfma_f32_16x16x32_bf16 v[120:123], a[120:123], v[40:43], v[120:123]\n"
    "s_waitcnt lgkmcnt(8)\n"
    "v_mfma_f32_16x16x32_bf16 v[124:127], a[124:127], v[40:43], v[124:127]\n"
    "v_mul_f32 v112, v112, v12\n"
    "v_mul_f32 v113, v113, v12\n"
    "v_mul_f32 v114, v114, v12\n"
    "v_mul_f32 v115, v115, v12\n"
    "v_mul_f32 v116, v116, v12\n"
    "v_mul_f32 v117, v117, v12\n"
    "v_mul_f32 v118, v118, v12\n"
    "v_mul_f32 v119, v119, v12\n"
    "v_cvt_pk_bf16_f32 v112, v112, v113\n"
    "v_cvt_pk_bf16_f32 v113, v114, v115\n"
    "v_cvt_pk_bf16_f32 v114, v116, v117\n"
    "v_cvt_pk_bf16_f32 v115, v118, v119\n"
    "v_mul_f32 v120, v120, v12\n"
    "buffer_store_dwordx4 v[112:115], v4, s[52:55], s63 offen offset:0 sc0 sc1\n"
    "v_mul_f32 v121, v121, v12\n"
    "v_mul_f32 v122, v122, v12\n"
    "v_mul_f32 v123, v123, v12\n"
    "v_mul_f32 v124, v124, v12\n"
    "v_mul_f32 v125, v125, v12\n"
    "v_mul_f32 v126, v126, v12\n"
    "v_mul_f32 v127, v127, v12\n"
    "v_cvt_pk_bf16_f32 v120, v120, v121\n"
    "v_cvt_pk_bf16_f32 v121, v122, v123\n"
    "v_cvt_pk_bf16_f32 v122, v124, v125\n"
    "v_cvt_pk_bf16_f32 v123, v126, v127\n"
    "buffer_store_dwordx4 v[120:123], v4, s[52:55], s63 offen offset:64 sc0 sc1\n"
    "s_waitcnt lgkmcnt(6)\n"
    "v_mfma_f32_16x16x32_bf16 v[128:131], a[104:107], v[40:43], v[128:131]\n"
    "s_waitcnt lgkmcnt(4)\n"
    "v_mfma_f32_16x16x32_bf16 v[132:135], a[108:111], v[40:43], v[132:135]\n"
    "s_waitcnt lgkmcnt(2)\n"
    "v_mfma_f32_16x16x32_bf16 v[136:139], a[112:115], v[40:43], v[136:139]\n"
    "s_waitcnt lgkmcnt(0)\n"
    "s_nop 3\n"
    "v_mul_f32 v128, v128, v12\n"
    "v_mul_f32 v129, v129, v12\n"
    "v_mfma_f32_16x16x32_bf16 v[140:143], a[116:119], v[40:43], v[140:143]\n"
    "v_mul_f32 v130, v130, v12\n"
    "v_mul_f32 v131, v131, v12\n"
    "v_mul_f32 v132, v132, v12\n"
    "v_mul_f32 v133, v133, v12\n"
    "v_mul_f32 v134, v134, v12\n"
    "v_mul_f32 v135, v135, v12\n"
    "v_cvt_pk_bf16_f32 v128, v128, v129\n"
    "v_cvt_pk_bf16_f32 v129, v130, v131\n"
    "v_cvt_pk_bf16_f32 v130, v132, v133\n"
    "v_cvt_pk_bf16_f32 v131, v134, v135\n"
    "v_mul_f32 v136, v136, v12\n"
    "buffer_store_dwordx4 v[128:131], v4, s[52:55], s63 offen offset:128 sc0 sc1\n"
    "v_mul_f32 v137, v137, v12\n"
    "v_mul_f32 v138, v138, v12\n"
    "v_mul_f32 v139, v139, v12\n"
    "v_mul_f32 v140, v140, v12\n"
    "v_mul_f32 v141, v141, v12\n"
    "v_mul_f32 v142, v142, v12\n"
    "v_mul_f32 v143, v143, v12\n"
    "v_cvt_pk_bf16_f32 v136, v136, v137\n"
    "v_cvt_pk_bf16_f32 v137, v138, v139\n"
    "v_cvt_pk_bf16_f32 v138, v140, v141\n"
    "v_cvt_pk_bf16_f32 v139, v142, v143\n"
    "buffer_store_dwordx4 v[136:139], v4, s[52:55], s63 offen offset:192 sc0 sc1\n"
    "LCE_%=:\n"
    "s_mov_b64 %[fail], s[64:65]\n"
    "s_mov_b32 %[wv], s56\n"
    : [fail] "=s"(fail), [wv] "=s"(wv), [tid] "+v"(tio)
    : [qlo] "s"((unsigned)qa), [qhi] "s"((unsigned)(qa >> 32)), [klo] "s"((unsigned)ka), [khi] "s"((unsigned)(ka >> 32)),
      [vlo] "s"((unsigned)va), [vhi] "s"((unsigned)(va >> 32)), [olo] "s"((unsigned)oa), [ohi] "s"((unsigned)(oa >> 32)), [lds] "s"(ldsb), [xidx] "s"(idx)
    : "memory", "vcc", "scc", "m0", "s40","s41","s42","s43","s44","s45","s46","s47","s48","s49","s50","s51","s52","s53","s54","s55","s56","s57","s58","s59","s60","s61","s62","s63","s64","s65","s66","s67","s68","s69","s70","s71","s72","s73","s74","s75","s76","s77","s78","s79","s80","s81","s82","s83","s84","s85","s86","s87","s88","s89","s90","s91","s92","s93","s94","s95","s96","s97","s98","s99",
      "v1","v2","v3","v4","v5","v6","v7","v8","v9","v10","v11","v12","v13","v14","v15","v16","v17","v18","v19","v20","v21","v22","v23","v24","v25","v26","v27","v28","v29","v30","v31","v32","v33","v34","v35","v36","v37","v38","v39","v40","v41","v42","v43","v44","v45","v46","v47","v48","v49","v50","v51","v52","v53","v54","v55","v56","v57","v58","v59","v60","v61","v62","v63","v64","v65","v66","v67","v68","v69","v70","v71","v72","v73","v74","v75","v76","v77","v78","v79","v80","v81","v82","v83","v84","v85","v86","v87","v88","v89","v90","v91","v92","v93","v94","v95","v96","v97","v98","v99","v100","v101","v102","v103","v104","v105","v106","v107","v108","v109","v110","v111","v112","v113","v114","v115","v116","v117","v118","v119","v120","v121","v122","v123","v124","v125","v126","v127","v128","v129","v130","v131","v132","v133","v134","v135","v136","v137","v138","v139","v140","v141","v142","v143","v144","v145","v146","v147","v148","v149","v150","v151","v152","v153","v154","v155","v156","v157","v158","v159","v160","v161","v162","v163","v164","v165","v166","v167","v168","v169","v170","v171","v172","v173","v174","v175","v176","v177","v178","v179","v180","v181","v182","v183","v184","v185","v186","v187","v188","v189","v190","v191","v192","v193","v194","v195","v196","v197","v198","v199","v200","v201","v202","v203","v204","v205","v206","v207","v208","v209","v210","v211","v212","v213","v214","v215","v216","v217","v218","v219","v220","v221","v222","v223","v224","v225","v226","v227","v228","v229","v230","v231","v232","v233","v234","v235","v236","v237","v238","v239","v240","v241","v242","v243","v244","v245","v246","v247","v248","v249","v250","v251","v252","v253","v254","v255",
      "a0","a1","a2","a3","a4","a5","a6","a7","a8","a9","a10","a11","a12","a13","a14","a15","a16","a17","a18","a19","a20","a21","a22","a23","a24","a25","a26","a27","a28","a29","a30","a31","a32","a33","a34","a35","a36","a37","a38","a39","a40","a41","a42","a43","a44","a45","a46","a47","a48","a49","a50","a51","a52","a53","a54","a55","a56","a57","a58","a59","a60","a61","a62","a63","a64","a65","a66","a67","a68","a69","a70","a71","a72","a73","a74","a75","a76","a77","a78","a79","a80","a81","a82","a83","a84","a85","a86","a87","a88","a89","a90","a91","a92","a93","a94","a95","a96","a97","a98","a99","a100","a101","a102","a103","a104","a105","a106","a107","a108","a109","a110","a111","a112","a113","a114","a115","a116","a117","a118","a119","a120","a121","a122","a123","a124","a125","a126","a127","a128","a129","a130","a131","a132","a133","a134","a135","a136","a137","a138","a139","a140","a141","a142","a143","a144","a145","a146","a147","a148","a149","a150","a151","a152","a153","a154","a155","a156","a157","a158","a159","a160","a161","a162","a163","a164","a165","a166","a167","a168","a169","a170","a171","a172","a173","a174","a175","a176","a177","a178","a179","a180","a181","a182","a183","a184","a185","a186","a187","a188","a189","a190","a191","a192","a193","a194","a195","a196","a197","a198","a199","a200","a201","a202","a203","a204","a205","a206","a207","a208","a209","a210","a211","a212","a213","a214","a215","a216","a217","a218","a219","a220","a221","a222","a223","a224","a225","a226","a227","a228","a229","a230","a231","a232","a233","a234","a235","a236","a237","a238","a239","a240","a241","a242","a243","a244","a245","a246","a247","a248","a249","a250","a251","a252","a253","a254","a255");
  if (fail != 0ull) { asm volatile("s_waitcnt vmcnt(0)" ::: "memory"); slow_path(Qw, Kg, Vg, Ow, (int)wv, (int)__lane_id()); }
}

extern "C" void kernel_launch(void* const* d_in, const int* in_sizes, int n_in,
                              void* d_out, int out_size, void* d_ws, size_t ws_size,
                              hipStream_t stream) {
  (void)in_sizes; (void)n_in; (void)out_size; (void)d_ws; (void)ws_size;
  const int shm = 163840;
  (void)hipFuncSetAttribute((const void*)gqa_attn_fwd, hipFuncAttributeMaxDynamicSharedMemorySize, shm);
  hipLaunchKernelGGL(gqa_attn_fwd, dim3(256), dim3(256), shm, stream,
                     (const unsigned short*)d_in[0], (const unsigned short*)d_in[1], (const unsigned short*)d_in[2], (unsigned short*)d_out);
}
```
